```python
import torch

def _softmax(x, axis=-1):
    return torch.softmax(x, dim=axis)
def _gelu(x):
    return torch.nn.functional.gelu(x, approximate="tanh")
def _silu(x):
    return torch.nn.functional.silu(x)
def _sigmoid(x):
    return torch.sigmoid(x)
def _layernorm(x, axis=-1, eps=1e-5):
    m = x.mean(dim=axis, keepdim=True)
    v = x.var(dim=axis, keepdim=True, unbiased=False)
    return (x - m) / torch.sqrt(v + eps)
def _scatter_add(tgt, idx, src, axis=0):
    return tgt.clone().index_add_(axis, idx, src)
def _segment_sum(src, seg, n):
    out = torch.zeros((n,) + src.shape[1:], dtype=src.dtype)
    return out.index_add_(0, seg, src)
def _pad(x, pw):
    flat = tuple(v for pair in reversed(pw) for v in pair)
    return torch.nn.functional.pad(x, flat)

kernel_name = 'recurrent_scan__mha_block_cached__glu_gated_reglu__prompt'

def setup_inputs():

    _w = (lambda a: (lambda t: t.to(torch.bfloat16) if t.is_floating_point() else t)(torch.as_tensor(a)))
    def _A_setup():
        B, Sq, D = 1, 2048, 1024
        H, Sk, Dh = 32, 2048, 128
        return {
            "x": _w((lambda *s: torch.randn(*s, dtype=torch.bfloat16) * 0.05)(B, Sq, D)),
            "k_cache": _w((lambda *s: torch.randn(*s, dtype=torch.bfloat16) * 1.6)(B, H, Sk, Dh)),
            "v_cache": _w((lambda *s: torch.randn(*s, dtype=torch.bfloat16) * 1.6)(B, H, Sk, Dh)),
            "wq": _w((lambda *s: torch.randn(*s, dtype=torch.bfloat16) * 1.6)(D, H * Dh) * (D ** -0.5)),
            "wo": _w((lambda *s: torch.randn(*s, dtype=torch.bfloat16) * 1.6)(H * Dh, D) * ((H * Dh) ** -0.5)),
        }
    _A_in = _A_setup()

    def _B_setup():
        B, S, D, F = 1, 2048, 1024, 3584
        return {
            "x": _w((lambda *s: torch.randn(*s, dtype=torch.bfloat16) * 0.05)(B, S, D)),
            "W_gate": _w((lambda *s: torch.randn(*s, dtype=torch.bfloat16) * 0.2)(D, F) * (2.0 / D) ** 0.5),
            "W_up": _w((lambda *s: torch.randn(*s, dtype=torch.bfloat16) * 0.2)(D, F) * (2.0 / D) ** 0.5),
            "W_down": _w((lambda *s: torch.randn(*s, dtype=torch.bfloat16) * 0.2)(F, D) * (1.0 / F) ** 0.5),
        }
    _B_in = _B_setup()

    return {
        "A_x": _A_in["x"],
        "A_k_cache": _A_in["k_cache"],
        "A_v_cache": _A_in["v_cache"],
        "A_wq": _A_in["wq"],
        "A_wo": _A_in["wo"],
        "B_x": _B_in["x"],
        "B_W_gate": _B_in["W_gate"],
        "B_W_up": _B_in["W_up"],
        "B_W_down": _B_in["W_down"],
    }

def reference(A_x, A_k_cache, A_v_cache, A_wq, A_wo, B_x, B_W_gate, B_W_up, B_W_down):
    def _A_ref(x, k_cache, v_cache, wq, wo):
        B, Sq, D = x.shape
        H, Dh = 32, 128
        q = (lambda x, *d: x.permute(*d))((x @ wq).reshape(B, Sq, H, Dh), 0, 2, 1, 3)
        scores = torch.einsum("bhqd,bhkd->bhqk", q, k_cache) * (Dh ** -0.5)
        p = _softmax(scores, axis=-1)
        o = torch.einsum("bhqk,bhkd->bhqd", p, v_cache)
        return (lambda x, *d: x.permute(*d))(o, 0, 2, 1, 3).reshape(B, Sq, H * Dh) @ wo

    def _B_ref(x, W_gate, W_up, W_down):
        gate = torch.einsum("bsd,df->bsf", x, W_gate)
        up = torch.einsum("bsd,df->bsf", x, W_up)
        gate = gate * (lambda x, dt: x.to(dt))(gate > 0, gate.dtype)
        h = gate * up
        return torch.einsum("bsf,fd->bsd", h, W_down)

    x = A_x
    xn = _layernorm(x, axis=-1)
    s = _A_ref(xn, A_k_cache, A_v_cache, A_wq, A_wo)
    g = _silu(_B_ref(xn, B_W_gate, B_W_up, B_W_down))
    return x + s * g
```

```cpp
#define CODEPF 2
#define FST 4
#define RAMPV 0
#define RAMPB 0
#define XAUX 16
#define FAUX 16
#define OAUX 16
#define LN_TOUCH 0
#include <hip/hip_runtime.h>
#include <hip/hip_bf16.h>
#include <stdint.h>

typedef unsigned short u16;
typedef float f32x4 __attribute__((ext_vector_type(4)));
typedef float f32x2 __attribute__((ext_vector_type(2)));
typedef float f32x16 __attribute__((ext_vector_type(16)));
typedef float f32x32 __attribute__((ext_vector_type(32)));
typedef unsigned u32x4 __attribute__((ext_vector_type(4)));
typedef unsigned u32x2 __attribute__((ext_vector_type(2)));
typedef unsigned u32x16 __attribute__((ext_vector_type(16)));
typedef unsigned u32x32 __attribute__((ext_vector_type(32)));
typedef __bf16 bf16x2_t __attribute__((ext_vector_type(2)));
#define DI __device__ __forceinline__
#define SPIN_MAX (1u << 22)

constexpr int SEQ = 2048, DM = 1024, NH = 32, DH = 128, FF = 3584, HD = NH * DH;
constexpr float QS = 0.08838834764831845f * 1.4426950408889634f;
constexpr size_t WS_XN = 0;
constexpr size_t WS_H  = 4u << 20;
constexpr size_t WS_O  = 20u << 20;
constexpr size_t WS_G  = 40u << 20;
constexpr size_t WS_CNT = 96u << 20;
constexpr size_t WS_QF = 64u << 20;

DI float bf2f(u16 b) { return __uint_as_float(((unsigned)b) << 16); }
DI unsigned cvtpk(float lo, float hi) { f32x2 v = {lo, hi}; bf16x2_t b = __builtin_convertvector(v, bf16x2_t); return __builtin_bit_cast(unsigned, b); }
DI float bflo(unsigned w) { return __uint_as_float(w << 16); }
DI float bfhi(unsigned w) { return __uint_as_float(w & 0xffff0000u); }
DI u32x4 make_srd(const void* p, unsigned bytes) {
  uint64_t a = (uint64_t)p; u32x4 s; s[0] = (unsigned)a; s[1] = (unsigned)(a >> 32) & 0xffffu; s[2] = bytes; s[3] = 0x00020000u; return s;
}
#define XNS QS
DI float wave_sum(float v) {
  v += __builtin_bit_cast(float, __builtin_amdgcn_update_dpp(0, __builtin_bit_cast(int, v), 0xB1, 0xF, 0xF, true));
  v += __builtin_bit_cast(float, __builtin_amdgcn_update_dpp(0, __builtin_bit_cast(int, v), 0x4E, 0xF, 0xF, true));
  v += __builtin_bit_cast(float, __builtin_amdgcn_update_dpp(0, __builtin_bit_cast(int, v), 0x141, 0xF, 0xF, true));
  v += __builtin_bit_cast(float, __builtin_amdgcn_update_dpp(0, __builtin_bit_cast(int, v), 0x140, 0xF, 0xF, true));
  { auto s = __builtin_amdgcn_permlane16_swap(__float_as_uint(v), __float_as_uint(v), false, false); v = __uint_as_float(s[0]) + __uint_as_float(s[1]); }
  { auto s = __builtin_amdgcn_permlane32_swap(__float_as_uint(v), __float_as_uint(v), false, false); v = __uint_as_float(s[0]) + __uint_as_float(s[1]); }
  return v;
}
__global__ __launch_bounds__(256, 1) void k_main(const u16* __restrict__ wq, const u16* __restrict__ wg, const u16* __restrict__ wu,
                                                 const u16* __restrict__ Kc, const u16* __restrict__ Vc, char* __restrict__ ws,
                                                 const u16* __restrict__ wo, const u16* __restrict__ wd,
                                                 const u16* __restrict__ x, u16* __restrict__ out) {
  __shared__ __attribute__((aligned(16))) char lds[163840];
  const int tid = threadIdx.x, lane = tid & 63, w = __builtin_amdgcn_readfirstlane(tid >> 6);
  const int bid = blockIdx.x, xcd = bid & 7, j = bid >> 3, c = xcd * 4 + (j & 3), rb = j >> 2;
  const int g = lane >> 4, q4 = (lane >> 2) & 3, p = lane & 3, m16 = lane & 15, sg = ((g & 1) << 1) | (g >> 1);
  const unsigned ldsb = (unsigned)(size_t)lds;
  u32x32 qf0, qf1; unsigned fa, fao; u32x4 ep; unsigned long long badm;
  const int q = xcd & 3, rb3 = (xcd >> 2) * 4 + (j & 3), cb = j >> 2, tile = rb3 * 8 + cb;
  unsigned tag = __builtin_amdgcn_readfirstlane(*(const unsigned*)(ws + WS_CNT + 4096)) + 1u;
  const int blk = rb * 32 + c;
  u32x4 v[2][2];
  unsigned cdum; unsigned long long pc0;
  asm volatile("buffer_load_dwordx4 %0, %6, %7, 0 offen\n\tbuffer_load_dwordx4 %1, %6, %7, 0 offen offset:1024\n\t"
               "buffer_load_dwordx4 %2, %6, %7, 0 offen offset:2048\n\tbuffer_load_dwordx4 %3, %6, %7, 0 offen offset:3072\n\t"
               "s_getpc_b64 %5\n\tglobal_load_dword %4, %8, %5"
               : "=&v"(v[0][0]), "=&v"(v[0][1]), "=&v"(v[1][0]), "=&v"(v[1][1]), "=&v"(cdum), "=&s"(pc0)
               : "v"(lane * 16), "s"(make_srd(x + (size_t)(blk * 8 + w * 2) * DM, 4096)), "v"((unsigned)(((j * 4 + w) % 6) * 8192 + lane * 128)) : "memory");
  u32x4 srdQ = make_srd((const char*)wq + c * 256, DM * HD * 2 - c * 256);
  const unsigned guoff = c * 224 + (w & 1) * 32 * 7168;
  const u32x4 srdG = make_srd((const char*)((w >> 1) ? wu : wg) + guoff, DM * FF * 2 - guoff);
  unsigned vq_dma[4], vg_dma[7];
#pragma unroll
  for (int i = 0; i < 4; ++i) { const int row = 16 * w + 4 * i + g, sc = (((m16 >> 1) ^ (row & 7)) << 1) | (m16 & 1); vq_dma[i] = row * 8192 + sc * 16 - i * 1024; }
#pragma unroll
  for (int i = 0; i < 7; ++i) { const int b = 1024 * i + 16 * lane, row = b / 224, cb2 = (b % 224 + 32 * (c & 1)) % 224; vg_dma[i] = row * 7168 + cb2 - (i & 3) * 1024; }
  unsigned vtq = (8 * rb + 2 * w + (q4 & 1) + RAMPV * 64 * (lane >> 5) - RAMPB * 64) * 8192 + p * 64;
  unsigned vtg = (4 * rb + ((lane >> 3) & 3) + RAMPV * 64 * (lane >> 5) - RAMPB * 64) * 7168 + min((lane & 7) * 64, 220);
  unsigned soffQ = 0, soffG = 0;
  const unsigned m0q = ldsb + w * 4096;
  unsigned m0g = ldsb + 16384 + (w >> 1) * 14336 + (w & 1) * 7168;
  asm volatile(
    "s_nop 4\n\t"
    "s_mov_b32 s60, 118784\n\t"
    "s_add_u32 m0, s60, s62\n\t"
    "s_nop 0\n\t"
    "buffer_load_dwordx4 v237, s[36:39], s56 offen offset:0 lds\n\t"
    "buffer_load_dwordx4 v238, s[36:39], s56 offen offset:1024 lds\n\t"
    "buffer_load_dwordx4 v239, s[36:39], s56 offen offset:2048 lds\n\t"
    "buffer_load_dwordx4 v240, s[36:39], s56 offen offset:3072 lds\n\t"
    "s_add_u32 m0, s60, s63\n\t"
    "s_nop 0\n\t"
    "buffer_load_dwordx4 v241, s[40:43], s57 offen offset:0 lds\n\t"
    "buffer_load_dwordx4 v242, s[40:43], s57 offen offset:1024 lds\n\t"
    "buffer_load_dwordx4 v243, s[40:43], s57 offen offset:2048 lds\n\t"
    "buffer_load_dwordx4 v244, s[40:43], s57 offen offset:3072 lds\n\t"
    "s_add_u32 m0, m0, 4096\n\t"
    "s_nop 0\n\t"
    "buffer_load_dwordx4 v245, s[40:43], s57 offen offset:0 lds\n\t"
    "buffer_load_dwordx4 v246, s[40:43], s57 offen offset:1024 lds\n\t"
    "buffer_load_dwordx4 v247, s[40:43], s57 offen offset:2048 lds\n\t"
    "s_add_u32 s56, s56, 524288\n\t"
    "s_add_u32 s57, s57, 458752\n\t"
    "s_waitcnt vmcnt(11)\n\t"
    : "+v"(v[0][0]), "+v"(v[0][1]), "+v"(v[1][0]), "+v"(v[1][1]), "+v"(cdum), "+{s56}"(soffQ), "+{s57}"(soffG), "+s"(tag)
    : "{v237}"(vq_dma[0]), "{v238}"(vq_dma[1]), "{v239}"(vq_dma[2]), "{v240}"(vq_dma[3]),
      "{v241}"(vg_dma[0]), "{v242}"(vg_dma[1]), "{v243}"(vg_dma[2]), "{v244}"(vg_dma[3]), "{v245}"(vg_dma[4]), "{v246}"(vg_dma[5]), "{v247}"(vg_dma[6]),
      "{v252}"(vtq), "{v253}"(vtg), "{s[36:39]}"(srdQ), "{s[40:43]}"(srdG), "{s62}"(m0q), "{s63}"(m0g)
    : "s60", "a190", "scc", "memory");
  __builtin_amdgcn_sched_barrier(0);
  {
#pragma unroll
    for (int r = 0; r < 2; ++r) {
      float f[16];
#pragma unroll
      for (int h = 0; h < 2; ++h)
#pragma unroll
        for (int e = 0; e < 4; ++e) { f[h * 8 + 2 * e] = bflo(v[r][h][e]); f[h * 8 + 2 * e + 1] = bfhi(v[r][h][e]); }
      float sm = 0;
#pragma unroll
      for (int e = 0; e < 16; ++e) sm += f[e];
      const float mean = wave_sum(sm) * (1.0f / DM);
      float qq = 0;
#pragma unroll
      for (int e = 0; e < 16; ++e) { f[e] -= mean; qq += f[e] * f[e]; }
      const float rs = XNS / sqrtf(wave_sum(qq) * (1.0f / DM) + 1e-5f);
      const int m = w * 2 + r;
#pragma unroll
      for (int h = 0; h < 2; ++h) {
        const int step = h * 16 + (lane >> 2);
#pragma unroll
        for (int hf = 0; hf < 2; ++hf) {
          const int kk = 8 * p + 4 * hf, gg = (kk & 15) >> 2, half = kk >> 4;
          u32x2 o; o[0] = cvtpk(f[h * 8 + 4 * hf] * rs, f[h * 8 + 4 * hf + 1] * rs); o[1] = cvtpk(f[h * 8 + 4 * hf + 2] * rs, f[h * 8 + 4 * hf + 3] * rs);
          *(u32x2*)(lds + step * 544 + (gg * 8 + m) * 16 + half * 8) = o;
        }
      }
    }
    asm volatile("s_waitcnt lgkmcnt(0)\n\ts_barrier" ::: "memory");
    __amdgpu_buffer_rsrc_t xrs = __builtin_amdgcn_make_buffer_rsrc(ws + WS_XN + (size_t)(blk >> 1) * 32768 + (blk & 1) * 128, (short)0, 32768, 0x00020000);
#pragma unroll
    for (int i = 0; i < 4; ++i) {
      const int qi = i * 256 + tid, step = qi >> 5, gg = (qi >> 3) & 3, m = qi & 7;
      __builtin_amdgcn_raw_buffer_store_b128(*(const u32x4*)(lds + step * 544 + (qi & 31) * 16), xrs, step * 1024 + gg * 256 + m * 16, 0, 16);
    }
  }
  __builtin_amdgcn_sched_barrier(0);
  {
    const u32x4 srdK = make_srd((const char*)Kc + (size_t)c * SEQ * DH * 2, SEQ * DH * 2), srdV = make_srd((const char*)Vc + (size_t)c * SEQ * DH * 2, SEQ * DH * 2);
    const u32x4 srdX = make_srd(ws + WS_XN + (size_t)rb * 16 * 32768, 16 * 32768);
    const u32x4 srdC = make_srd(ws + WS_G + (size_t)((tile * 4 + q) * 4 + w) * 24576 - (size_t)w * 4 * 32768, w * 4 * 32768 + 24576);
    const u32x4 srdH = make_srd(ws + WS_H + (size_t)(rb * 16 + w * 4) * 114688 - (size_t)w * 4 * 32768, w * 4 * 32768 + 4 * 114688);
    unsigned vq_rd[8], vx[4], pk[12];
#pragma unroll
    for (int nb = 0; nb < 8; ++nb) vq_rd[nb] = ldsb + 118784 + (4 * g + q4) * 256 + ((nb ^ (4 * (g & 1) + q4)) * 32) + 8 * p;
    unsigned vg_rd = ldsb + 118784 + 16384 + (4 * g + q4) * 224 + 8 * p;
#pragma unroll
    for (int mb = 0; mb < 4; ++mb) vx[mb] = (4 * w + mb) * 32768 + lane * 16;
#pragma unroll
    for (int i = 0; i < 4; ++i) pk[i] = (16 * w + g) * 256 + ((m16 ^ (4 * i + g)) * 16);
#pragma unroll
    for (int i = 0; i < 2; ++i) pk[4 + i] = (16 * w + g) * 256 + (((((m16 >> 1) ^ (4 * i + g)) << 1) | (m16 & 1)) * 16);
#pragma unroll
    for (int ds = 0; ds < 4; ++ds) pk[6 + ds] = ldsb + m16 * 256 + (((4 * ds + sg) ^ m16) * 16);
    pk[10] = (bid * 4 + w) * 8192 + p * 64; pk[11] = (bid * 4 + w) * 7168 + p * 64;
    unsigned fo = 40960 + ((lane & 8) ? (j >> 2) * 32 + ((xcd & 4) | (lane & 3)) * 4 + (j & 3) : rb3 * 32 + q * 8 + (lane & 7)) * FST;
    unsigned w3[4], sw3 = q * 14 * 131072 + cb * 256;
#pragma unroll
    for (int i = 0; i < 4; ++i) { const int row = 16 * w + 4 * i + g, sc = (((m16 >> 1) ^ (row & 7)) << 1) | (m16 & 1); w3[i] = row * 2048 + sc * 16 - i * 1024; }
    unsigned sown = w == 0 ? 8192 + (rb * 32 + c) * 128 : 0x7fffff00u, spoll = 8192 + (rb * 32 + w * 8) * 128;
    unsigned soffX = 0, scur = 118784, soffKV = 0;
    unsigned sh = ((7 * c + 1) >> 1) * 1024;
    const unsigned shi0 = (c & 1) ? ((7 * c) >> 1) * 1024 + 8 : ((7 * c + 6) >> 1) * 1024, shi1 = 0;
    asm volatile(
    "s_nop 4\n\t"
    "v_mov_b32 v227, s25\n\t"
    "v_lshlrev_b32 v226, 3, v248\n\t"
    "v_and_b32 v226, 0x380, v226\n\t"
    "s_mov_b32 s60, 28672\n\t"
    "v_mov_b32 v0, 0\n\t"
    "v_mov_b32 v1, 0\n\t"
    "v_mov_b32 v2, 0\n\t"
    "v_mov_b32 v3, 0\n\t"
    "v_mov_b32 v4, 0\n\t"
    "v_mov_b32 v5, 0\n\t"
    "v_mov_b32 v6, 0\n\t"
    "v_mov_b32 v7, 0\n\t"
    "v_mov_b32 v8, 0\n\t"
    "v_mov_b32 v9, 0\n\t"
    "v_mov_b32 v10, 0\n\t"
    "v_mov_b32 v11, 0\n\t"
    "v_mov_b32 v12, 0\n\t"
    "v_mov_b32 v13, 0\n\t"
    "v_mov_b32 v14, 0\n\t"
    "v_mov_b32 v15, 0\n\t"
    "v_mov_b32 v16, 0\n\t"
    "v_mov_b32 v17, 0\n\t"
    "v_mov_b32 v18, 0\n\t"
    "v_mov_b32 v19, 0\n\t"
    "v_mov_b32 v20, 0\n\t"
    "v_mov_b32 v21, 0\n\t"
    "v_mov_b32 v22, 0\n\t"
    "v_mov_b32 v23, 0\n\t"
    "v_mov_b32 v24, 0\n\t"
    "v_mov_b32 v25, 0\n\t"
    "v_mov_b32 v26, 0\n\t"
    "v_mov_b32 v27, 0\n\t"
    "v_mov_b32 v28, 0\n\t"
    "v_mov_b32 v29, 0\n\t"
    "v_mov_b32 v30, 0\n\t"
    "v_mov_b32 v31, 0\n\t"
    "v_mov_b32 v32, 0\n\t"
    "v_mov_b32 v33, 0\n\t"
    "v_mov_b32 v34, 0\n\t"
    "v_mov_b32 v35, 0\n\t"
    "v_mov_b32 v36, 0\n\t"
    "v_mov_b32 v37, 0\n\t"
    "v_mov_b32 v38, 0\n\t"
    "v_mov_b32 v39, 0\n\t"
    "v_mov_b32 v40, 0\n\t"
    "v_mov_b32 v41, 0\n\t"
    "v_mov_b32 v42, 0\n\t"
    "v_mov_b32 v43, 0\n\t"
    "v_mov_b32 v44, 0\n\t"
    "v_mov_b32 v45, 0\n\t"
    "v_mov_b32 v46, 0\n\t"
    "v_mov_b32 v47, 0\n\t"
    "v_mov_b32 v48, 0\n\t"
    "v_mov_b32 v49, 0\n\t"
    "v_mov_b32 v50, 0\n\t"
    "v_mov_b32 v51, 0\n\t"
    "v_mov_b32 v52, 0\n\t"
    "v_mov_b32 v53, 0\n\t"
    "v_mov_b32 v54, 0\n\t"
    "v_mov_b32 v55, 0\n\t"
    "v_mov_b32 v56, 0\n\t"
    "v_mov_b32 v57, 0\n\t"
    "v_mov_b32 v58, 0\n\t"
    "v_mov_b32 v59, 0\n\t"
    "v_mov_b32 v60, 0\n\t"
    "v_mov_b32 v61, 0\n\t"
    "v_mov_b32 v62, 0\n\t"
    "v_mov_b32 v63, 0\n\t"
    "v_mov_b32 v64, 0\n\t"
    "v_mov_b32 v65, 0\n\t"
    "v_mov_b32 v66, 0\n\t"
    "v_mov_b32 v67, 0\n\t"
    "v_mov_b32 v68, 0\n\t"
    "v_mov_b32 v69, 0\n\t"
    "v_mov_b32 v70, 0\n\t"
    "v_mov_b32 v71, 0\n\t"
    "v_mov_b32 v72, 0\n\t"
    "v_mov_b32 v73, 0\n\t"
    "v_mov_b32 v74, 0\n\t"
    "v_mov_b32 v75, 0\n\t"
    "v_mov_b32 v76, 0\n\t"
    "v_mov_b32 v77, 0\n\t"
    "v_mov_b32 v78, 0\n\t"
    "v_mov_b32 v79, 0\n\t"
    "v_mov_b32 v80, 0\n\t"
    "v_mov_b32 v81, 0\n\t"
    "v_mov_b32 v82, 0\n\t"
    "v_mov_b32 v83, 0\n\t"
    "v_mov_b32 v84, 0\n\t"
    "v_mov_b32 v85, 0\n\t"
    "v_mov_b32 v86, 0\n\t"
    "v_mov_b32 v87, 0\n\t"
    "v_mov_b32 v88, 0\n\t"
    "v_mov_b32 v89, 0\n\t"
    "v_mov_b32 v90, 0\n\t"
    "v_mov_b32 v91, 0\n\t"
    "v_mov_b32 v92, 0\n\t"
    "v_mov_b32 v93, 0\n\t"
    "v_mov_b32 v94, 0\n\t"
    "v_mov_b32 v95, 0\n\t"
    "v_mov_b32 v96, 0\n\t"
    "v_mov_b32 v97, 0\n\t"
    "v_mov_b32 v98, 0\n\t"
    "v_mov_b32 v99, 0\n\t"
    "v_mov_b32 v100, 0\n\t"
    "v_mov_b32 v101, 0\n\t"
    "v_mov_b32 v102, 0\n\t"
    "v_mov_b32 v103, 0\n\t"
    "v_mov_b32 v104, 0\n\t"
    "v_mov_b32 v105, 0\n\t"
    "v_mov_b32 v106, 0\n\t"
    "v_mov_b32 v107, 0\n\t"
    "v_mov_b32 v108, 0\n\t"
    "v_mov_b32 v109, 0\n\t"
    "v_mov_b32 v110, 0\n\t"
    "v_mov_b32 v111, 0\n\t"
    "v_mov_b32 v112, 0\n\t"
    "v_mov_b32 v113, 0\n\t"
    "v_mov_b32 v114, 0\n\t"
    "v_mov_b32 v115, 0\n\t"
    "v_mov_b32 v116, 0\n\t"
    "v_mov_b32 v117, 0\n\t"
    "v_mov_b32 v118, 0\n\t"
    "v_mov_b32 v119, 0\n\t"
    "s_waitcnt vmcnt(0)\n\t"
    "s_barrier\n\t"
    "buffer_store_dword v227, off, s[28:31], s67 sc1\n\t"
    "s_add_u32 m0, s60, s62\n\t"
    "s_nop 0\n\t"
    "buffer_load_dwordx4 v237, s[36:39], s56 offen offset:0 lds\n\t"
    "buffer_load_dwordx4 v238, s[36:39], s56 offen offset:1024 lds\n\t"
    "buffer_load_dwordx4 v239, s[36:39], s56 offen offset:2048 lds\n\t"
    "buffer_load_dwordx4 v240, s[36:39], s56 offen offset:3072 lds\n\t"
    "s_add_u32 m0, s60, s63\n\t"
    "s_nop 0\n\t"
    "buffer_load_dwordx4 v241, s[40:43], s57 offen offset:0 lds\n\t"
    "buffer_load_dwordx4 v242, s[40:43], s57 offen offset:1024 lds\n\t"
    "buffer_load_dwordx4 v243, s[40:43], s57 offen offset:2048 lds\n\t"
    "buffer_load_dwordx4 v244, s[40:43], s57 offen offset:3072 lds\n\t"
    "s_add_u32 m0, m0, 4096\n\t"
    "s_nop 0\n\t"
    "buffer_load_dwordx4 v245, s[40:43], s57 offen offset:0 lds\n\t"
    "buffer_load_dwordx4 v246, s[40:43], s57 offen offset:1024 lds\n\t"
    "buffer_load_dwordx4 v247, s[40:43], s57 offen offset:2048 lds\n\t"
    "s_add_u32 s56, s56, 524288\n\t"
    "s_add_u32 s57, s57, 458752\n\t"
    "ds_read_b64_tr_b16 a[128:129], v228 offset:0\n\t"
    "ds_read_b64_tr_b16 a[130:131], v228 offset:4096\n\t"
    "ds_read_b64_tr_b16 a[132:133], v228 offset:8192\n\t"
    "ds_read_b64_tr_b16 a[134:135], v228 offset:12288\n\t"
    "ds_read_b64_tr_b16 a[136:137], v229 offset:0\n\t"
    "ds_read_b64_tr_b16 a[138:139], v229 offset:4096\n\t"
    "ds_read_b64_tr_b16 a[140:141], v229 offset:8192\n\t"
    "ds_read_b64_tr_b16 a[142:143], v229 offset:12288\n\t"
    "ds_read_b64_tr_b16 a[144:145], v230 offset:0\n\t"
    "ds_read_b64_tr_b16 a[146:147], v230 offset:4096\n\t"
    "ds_read_b64_tr_b16 a[148:149], v230 offset:8192\n\t"
    "ds_read_b64_tr_b16 a[150:151], v230 offset:12288\n\t"
    "ds_read_b64_tr_b16 a[152:153], v231 offset:0\n\t"
    "ds_read_b64_tr_b16 a[154:155], v231 offset:4096\n\t"
    "ds_read_b64_tr_b16 a[156:157], v231 offset:8192\n\t"
    "ds_read_b64_tr_b16 a[158:159], v231 offset:12288\n\t"
    "ds_read_b64_tr_b16 a[160:161], v232 offset:0\n\t"
    "ds_read_b64_tr_b16 a[162:163], v232 offset:4096\n\t"
    "v_mov_b32 v120, 0\n\t"
    "v_mov_b32 v121, 0\n\t"
    "v_mov_b32 v122, 0\n\t"
    "v_mov_b32 v123, 0\n\t"
    "v_mov_b32 v124, 0\n\t"
    "v_mov_b32 v125, 0\n\t"
    "v_mov_b32 v126, 0\n\t"
    "v_mov_b32 v127, 0\n\t"
    "v_mov_b32 v128, 0\n\t"
    "v_mov_b32 v129, 0\n\t"
    "v_mov_b32 v130, 0\n\t"
    "v_mov_b32 v131, 0\n\t"
    "v_mov_b32 v132, 0\n\t"
    "v_mov_b32 v133, 0\n\t"
    "v_mov_b32 v134, 0\n\t"
    "v_mov_b32 v135, 0\n\t"
    "v_mov_b32 v136, 0\n\t"
    "v_mov_b32 v137, 0\n\t"
    "v_mov_b32 v138, 0\n\t"
    "v_mov_b32 v139, 0\n\t"
    "v_mov_b32 v140, 0\n\t"
    "v_mov_b32 v141, 0\n\t"
    "v_mov_b32 v142, 0\n\t"
    "v_mov_b32 v143, 0\n\t"
    "v_mov_b32 v144, 0\n\t"
    "v_mov_b32 v145, 0\n\t"
    "v_mov_b32 v146, 0\n\t"
    "v_mov_b32 v147, 0\n\t"
    "v_mov_b32 v148, 0\n\t"
    "v_mov_b32 v149, 0\n\t"
    "v_mov_b32 v150, 0\n\t"
    "v_mov_b32 v151, 0\n\t"
    "v_mov_b32 v152, 0\n\t"
    "v_mov_b32 v153, 0\n\t"
    "v_mov_b32 v154, 0\n\t"
    "v_mov_b32 v155, 0\n\t"
    "v_mov_b32 v156, 0\n\t"
    "v_mov_b32 v157, 0\n\t"
    "v_mov_b32 v158, 0\n\t"
    "v_mov_b32 v159, 0\n\t"
    "v_mov_b32 v160, 0\n\t"
    "v_mov_b32 v161, 0\n\t"
    "v_mov_b32 v162, 0\n\t"
    "v_mov_b32 v163, 0\n\t"
    "v_mov_b32 v164, 0\n\t"
    "v_mov_b32 v165, 0\n\t"
    "v_mov_b32 v166, 0\n\t"
    "v_mov_b32 v167, 0\n\t"
    "v_mov_b32 v168, 0\n\t"
    "v_mov_b32 v169, 0\n\t"
    "v_mov_b32 v170, 0\n\t"
    "v_mov_b32 v171, 0\n\t"
    "v_mov_b32 v172, 0\n\t"
    "v_mov_b32 v173, 0\n\t"
    "v_mov_b32 v174, 0\n\t"
    "v_mov_b32 v175, 0\n\t"
    "v_mov_b32 v176, 0\n\t"
    "v_mov_b32 v177, 0\n\t"
    "v_mov_b32 v178, 0\n\t"
    "v_mov_b32 v179, 0\n\t"
    "v_mov_b32 v180, 0\n\t"
    "v_mov_b32 v181, 0\n\t"
    "v_mov_b32 v182, 0\n\t"
    "v_mov_b32 v183, 0\n\t"
    "v_mov_b32 v184, 0\n\t"
    "v_mov_b32 v185, 0\n\t"
    "v_mov_b32 v186, 0\n\t"
    "v_mov_b32 v187, 0\n\t"
    "v_mov_b32 v188, 0\n\t"
    "v_mov_b32 v189, 0\n\t"
    "v_mov_b32 v190, 0\n\t"
    "v_mov_b32 v191, 0\n\t"
    "v_mov_b32 v192, 0\n\t"
    "v_mov_b32 v193, 0\n\t"
    "v_mov_b32 v194, 0\n\t"
    "v_mov_b32 v195, 0\n\t"
    "v_mov_b32 v196, 0\n\t"
    "v_mov_b32 v197, 0\n\t"
    "v_mov_b32 v198, 0\n\t"
    "v_mov_b32 v199, 0\n\t"
    "v_mov_b32 v200, 0\n\t"
    "v_mov_b32 v201, 0\n\t"
    "v_mov_b32 v202, 0\n\t"
    "v_mov_b32 v203, 0\n\t"
    "v_mov_b32 v204, 0\n\t"
    "v_mov_b32 v205, 0\n\t"
    "v_mov_b32 v206, 0\n\t"
    "v_mov_b32 v207, 0\n\t"
    "v_mov_b32 v208, 0\n\t"
    "v_mov_b32 v209, 0\n\t"
    "v_mov_b32 v210, 0\n\t"
    "v_mov_b32 v211, 0\n\t"
    "v_mov_b32 v212, 0\n\t"
    "v_mov_b32 v213, 0\n\t"
    "v_mov_b32 v214, 0\n\t"
    "v_mov_b32 v215, 0\n\t"
    "v_mov_b32 v216, 0\n\t"
    "v_mov_b32 v217, 0\n\t"
    "v_mov_b32 v218, 0\n\t"
    "v_mov_b32 v219, 0\n\t"
    "v_mov_b32 v220, 0\n\t"
    "v_mov_b32 v221, 0\n\t"
    "v_mov_b32 v222, 0\n\t"
    "v_mov_b32 v223, 0\n\t"
    "v_mov_b32 v224, 0\n\t"
    "v_accvgpr_write_b32 a0, 0\n\t"
    "v_accvgpr_write_b32 a1, 0\n\t"
    "v_accvgpr_write_b32 a2, 0\n\t"
    "v_accvgpr_write_b32 a3, 0\n\t"
    "v_accvgpr_write_b32 a4, 0\n\t"
    "v_accvgpr_write_b32 a5, 0\n\t"
    "v_accvgpr_write_b32 a6, 0\n\t"
    "v_accvgpr_write_b32 a7, 0\n\t"
    "v_accvgpr_write_b32 a8, 0\n\t"
    "v_accvgpr_write_b32 a9, 0\n\t"
    "v_accvgpr_write_b32 a10, 0\n\t"
    "v_accvgpr_write_b32 a11, 0\n\t"
    "v_accvgpr_write_b32 a12, 0\n\t"
    "v_accvgpr_write_b32 a13, 0\n\t"
    "v_accvgpr_write_b32 a14, 0\n\t"
    "buffer_load_dword v225, v226, s[28:31], s64 offen sc1\n\t"
    "v_accvgpr_write_b32 a15, 0\n\t"
    "v_accvgpr_write_b32 a16, 0\n\t"
    "v_accvgpr_write_b32 a17, 0\n\t"
    "v_accvgpr_write_b32 a18, 0\n\t"
    "v_accvgpr_write_b32 a19, 0\n\t"
    "v_accvgpr_write_b32 a20, 0\n\t"
    "v_accvgpr_write_b32 a21, 0\n\t"
    "v_accvgpr_write_b32 a22, 0\n\t"
    "v_accvgpr_write_b32 a23, 0\n\t"
    "v_accvgpr_write_b32 a24, 0\n\t"
    "v_accvgpr_write_b32 a25, 0\n\t"
    "v_accvgpr_write_b32 a26, 0\n\t"
    "v_accvgpr_write_b32 a27, 0\n\t"
    "v_accvgpr_write_b32 a28, 0\n\t"
    "v_accvgpr_write_b32 a29, 0\n\t"
    "v_accvgpr_write_b32 a30, 0\n\t"
    "v_accvgpr_write_b32 a31, 0\n\t"
    "v_accvgpr_write_b32 a32, 0\n\t"
    "v_accvgpr_write_b32 a33, 0\n\t"
    "v_accvgpr_write_b32 a34, 0\n\t"
    "v_accvgpr_write_b32 a35, 0\n\t"
    "v_accvgpr_write_b32 a36, 0\n\t"
    "v_accvgpr_write_b32 a37, 0\n\t"
    "v_accvgpr_write_b32 a38, 0\n\t"
    "v_accvgpr_write_b32 a39, 0\n\t"
    "v_accvgpr_write_b32 a40, 0\n\t"
    "v_accvgpr_write_b32 a41, 0\n\t"
    "v_accvgpr_write_b32 a42, 0\n\t"
    "v_accvgpr_write_b32 a43, 0\n\t"
    "v_accvgpr_write_b32 a44, 0\n\t"
    "v_accvgpr_write_b32 a45, 0\n\t"
    "v_accvgpr_write_b32 a46, 0\n\t"
    "v_accvgpr_write_b32 a47, 0\n\t"
    "v_accvgpr_write_b32 a48, 0\n\t"
    "v_accvgpr_write_b32 a49, 0\n\t"
    "v_accvgpr_write_b32 a50, 0\n\t"
    "v_accvgpr_write_b32 a51, 0\n\t"
    "v_accvgpr_write_b32 a52, 0\n\t"
    "v_accvgpr_write_b32 a53, 0\n\t"
    "v_accvgpr_write_b32 a54, 0\n\t"
    "v_accvgpr_write_b32 a55, 0\n\t"
    "v_accvgpr_write_b32 a56, 0\n\t"
    "v_accvgpr_write_b32 a57, 0\n\t"
    "v_accvgpr_write_b32 a58, 0\n\t"
    "v_accvgpr_write_b32 a59, 0\n\t"
    "v_accvgpr_write_b32 a60, 0\n\t"
    "v_accvgpr_write_b32 a61, 0\n\t"
    "v_accvgpr_write_b32 a62, 0\n\t"
    "v_accvgpr_write_b32 a63, 0\n\t"
    "v_accvgpr_write_b32 a64, 0\n\t"
    "v_accvgpr_write_b32 a65, 0\n\t"
    "v_accvgpr_write_b32 a66, 0\n\t"
    "v_accvgpr_write_b32 a67, 0\n\t"
    "v_accvgpr_write_b32 a68, 0\n\t"
    "v_accvgpr_write_b32 a69, 0\n\t"
    "v_accvgpr_write_b32 a70, 0\n\t"
    "v_accvgpr_write_b32 a71, 0\n\t"
    "v_accvgpr_write_b32 a72, 0\n\t"
    "v_accvgpr_write_b32 a73, 0\n\t"
    "v_accvgpr_write_b32 a74, 0\n\t"
    "v_accvgpr_write_b32 a75, 0\n\t"
    "v_accvgpr_write_b32 a76, 0\n\t"
    "v_accvgpr_write_b32 a77, 0\n\t"
    "v_accvgpr_write_b32 a78, 0\n\t"
    "v_accvgpr_write_b32 a79, 0\n\t"
    "v_accvgpr_write_b32 a80, 0\n\t"
    "v_accvgpr_write_b32 a81, 0\n\t"
    "v_accvgpr_write_b32 a82, 0\n\t"
    "v_accvgpr_write_b32 a83, 0\n\t"
    "v_accvgpr_write_b32 a84, 0\n\t"
    "v_accvgpr_write_b32 a85, 0\n\t"
    "v_accvgpr_write_b32 a86, 0\n\t"
    "v_accvgpr_write_b32 a87, 0\n\t"
    "v_accvgpr_write_b32 a88, 0\n\t"
    "v_accvgpr_write_b32 a89, 0\n\t"
    "v_accvgpr_write_b32 a90, 0\n\t"
    "v_accvgpr_write_b32 a91, 0\n\t"
    "v_accvgpr_write_b32 a92, 0\n\t"
    "v_accvgpr_write_b32 a93, 0\n\t"
    "v_accvgpr_write_b32 a94, 0\n\t"
    "v_accvgpr_write_b32 a95, 0\n\t"
    "v_accvgpr_write_b32 a96, 0\n\t"
    "v_accvgpr_write_b32 a97, 0\n\t"
    "v_accvgpr_write_b32 a98, 0\n\t"
    "v_accvgpr_write_b32 a99, 0\n\t"
    "v_accvgpr_write_b32 a100, 0\n\t"
    "v_accvgpr_write_b32 a101, 0\n\t"
    "v_accvgpr_write_b32 a102, 0\n\t"
    "v_accvgpr_write_b32 a103, 0\n\t"
    "v_accvgpr_write_b32 a104, 0\n\t"
    "v_accvgpr_write_b32 a105, 0\n\t"
    "v_accvgpr_write_b32 a106, 0\n\t"
    "v_accvgpr_write_b32 a107, 0\n\t"
    "v_accvgpr_write_b32 a108, 0\n\t"
    "v_accvgpr_write_b32 a109, 0\n\t"
    "v_accvgpr_write_b32 a110, 0\n\t"
    "v_accvgpr_write_b32 a111, 0\n\t"
    "v_accvgpr_write_b32 a112, 0\n\t"
    "v_accvgpr_write_b32 a113, 0\n\t"
    "v_accvgpr_write_b32 a114, 0\n\t"
    "v_accvgpr_write_b32 a115, 0\n\t"
    "v_accvgpr_write_b32 a116, 0\n\t"
    "v_accvgpr_write_b32 a117, 0\n\t"
    "v_accvgpr_write_b32 a118, 0\n\t"
    "v_accvgpr_write_b32 a119, 0\n\t"
    "v_accvgpr_write_b32 a120, 0\n\t"
    "v_accvgpr_write_b32 a121, 0\n\t"
    "v_accvgpr_write_b32 a122, 0\n\t"
    "v_accvgpr_write_b32 a123, 0\n\t"
    "v_accvgpr_write_b32 a124, -1\n\t"
    "v_accvgpr_write_b32 a125, -1\n\t"
    "v_accvgpr_write_b32 a126, -1\n\t"
    "v_accvgpr_write_b32 a127, -1\n\t"
    "s_mov_b32 s65, 0\n\t"
    "L_xf_chk%=:\n\t"
    "s_waitcnt vmcnt(0)\n\t"
    "v_cmp_ne_u32 vcc, s25, v225\n\t"
    "s_cbranch_vccz L_xf_ok%=\n\t"
    "s_add_u32 s65, s65, 1\n\t"
    "s_cmp_gt_u32 s65, 0x400000\n\t"
    "s_cbranch_scc1 L_xf_ok%=\n\t"
    "s_sleep 8\n\t"
    "buffer_load_dword v225, v226, s[28:31], s64 offen sc1\n\t"
    "s_branch L_xf_chk%=\n\t"
    "L_xf_ok%=:\n\t"
    "buffer_load_dwordx4 a[192:195], v248, s[44:47], s58 offen offset:0 sc1\n\t"
    "buffer_load_dwordx4 a[196:199], v249, s[44:47], s58 offen offset:0 sc1\n\t"
    "buffer_load_dwordx4 a[200:203], v250, s[44:47], s58 offen offset:0 sc1\n\t"
    "buffer_load_dwordx4 a[204:207], v251, s[44:47], s58 offen offset:0 sc1\n\t"
    "buffer_load_dwordx4 a[208:211], v248, s[44:47], s58 offen offset:1024 sc1\n\t"
    "buffer_load_dwordx4 a[212:215], v249, s[44:47], s58 offen offset:1024 sc1\n\t"
    "buffer_load_dwordx4 a[216:219], v250, s[44:47], s58 offen offset:1024 sc1\n\t"
    "buffer_load_dwordx4 a[220:223], v251, s[44:47], s58 offen offset:1024 sc1\n\t"
    "s_mov_b32 s60, 73728\n\t"
    "s_mov_b32 s67, 0\n\t"
    "s_mov_b32 s64, 4294967289\n\t"
    "v_mov_b32 v225, 0\n\t"
    "v_mov_b32 v226, 0\n\t"
    "v_mov_b32 v227, 0\n\t"
    "s_waitcnt vmcnt(0) lgkmcnt(12)\n\t"
    "L_g1_loop%=:\n\t"
    "v_mfma_f32_16x16x32_bf16 v[0:3], a[128:131], a[192:195], v[0:3]\n\t"
    "ds_read_b64_tr_b16 a[164:165], v232 offset:8192\n\t"
    "v_mfma_f32_16x16x32_bf16 v[0:3], a[132:135], a[208:211], v[0:3]\n\t"
    "s_add_u32 m0, s60, s62\n\t"
    "v_mfma_f32_16x16x32_bf16 v[32:35], a[128:131], a[196:199], v[32:35]\n\t"
    "ds_read_b64_tr_b16 a[166:167], v232 offset:12288\n\t"
    "v_mfma_f32_16x16x32_bf16 v[32:35], a[132:135], a[212:215], v[32:35]\n\t"
    "buffer_load_dwordx4 v237, s[36:39], s56 offen offset:0 lds\n\t"
    "v_mfma_f32_16x16x32_bf16 v[64:67], a[128:131], a[200:203], v[64:67]\n\t"
    "ds_read_b64_tr_b16 a[168:169], v233 offset:0\n\t"
    "v_mfma_f32_16x16x32_bf16 v[64:67], a[132:135], a[216:219], v[64:67]\n\t"
    "v_mfma_f32_16x16x32_bf16 v[96:99], a[128:131], a[204:207], v[96:99]\n\t"
    "ds_read_b64_tr_b16 a[170:171], v233 offset:4096\n\t"
    "v_mfma_f32_16x16x32_bf16 v[96:99], a[132:135], a[220:223], v[96:99]\n\t"
    "s_waitcnt lgkmcnt(12)\n\t"
    "v_mfma_f32_16x16x32_bf16 v[4:7], a[136:139], a[192:195], v[4:7]\n\t"
    "ds_read_b64_tr_b16 a[128:129], v233 offset:8192\n\t"
    "v_mfma_f32_16x16x32_bf16 v[4:7], a[140:143], a[208:211], v[4:7]\n\t"
    "v_mfma_f32_16x16x32_bf16 v[36:39], a[136:139], a[196:199], v[36:39]\n\t"
    "ds_read_b64_tr_b16 a[130:131], v233 offset:12288\n\t"
    "v_mfma_f32_16x16x32_bf16 v[36:39], a[140:143], a[212:215], v[36:39]\n\t"
    "buffer_load_dwordx4 v238, s[36:39], s56 offen offset:1024 lds\n\t"
    "v_mfma_f32_16x16x32_bf16 v[68:71], a[136:139], a[200:203], v[68:71]\n\t"
    "ds_read_b64_tr_b16 a[132:133], v234 offset:0\n\t"
    "v_mfma_f32_16x16x32_bf16 v[68:71], a[140:143], a[216:219], v[68:71]\n\t"
    "buffer_load_dwordx4 a[224:227], v248, s[44:47], s58 offen offset:2048 sc1\n\t"
    "v_mfma_f32_16x16x32_bf16 v[100:103], a[136:139], a[204:207], v[100:103]\n\t"
    "ds_read_b64_tr_b16 a[134:135], v234 offset:4096\n\t"
    "v_mfma_f32_16x16x32_bf16 v[100:103], a[140:143], a[220:223], v[100:103]\n\t"
    "s_waitcnt lgkmcnt(12)\n\t"
    "v_mfma_f32_16x16x32_bf16 v[8:11], a[144:147], a[192:195], v[8:11]\n\t"
    "ds_read_b64_tr_b16 a[136:137], v234 offset:8192\n\t"
    "v_mfma_f32_16x16x32_bf16 v[8:11], a[148:151], a[208:211], v[8:11]\n\t"
    "v_mfma_f32_16x16x32_bf16 v[40:43], a[144:147], a[196:199], v[40:43]\n\t"
    "ds_read_b64_tr_b16 a[138:139], v234 offset:12288\n\t"
    "v_mfma_f32_16x16x32_bf16 v[40:43], a[148:151], a[212:215], v[40:43]\n\t"
    "buffer_load_dwordx4 v239, s[36:39], s56 offen offset:2048 lds\n\t"
    "v_mfma_f32_16x16x32_bf16 v[72:75], a[144:147], a[200:203], v[72:75]\n\t"
    "ds_read_b64_tr_b16 a[140:141], v235 offset:0\n\t"
    "v_mfma_f32_16x16x32_bf16 v[72:75], a[148:151], a[216:219], v[72:75]\n\t"
    "buffer_load_dwordx4 a[228:231], v249, s[44:47], s58 offen offset:2048 sc1\n\t"
    "v_mfma_f32_16x16x32_bf16 v[104:107], a[144:147], a[204:207], v[104:107]\n\t"
    "ds_read_b64_tr_b16 a[142:143], v235 offset:4096\n\t"
    "v_mfma_f32_16x16x32_bf16 v[104:107], a[148:151], a[220:223], v[104:107]\n\t"
    "s_waitcnt lgkmcnt(12)\n\t"
    "v_mfma_f32_16x16x32_bf16 v[12:15], a[152:155], a[192:195], v[12:15]\n\t"
    "ds_read_b64_tr_b16 a[144:145], v235 offset:8192\n\t"
    "v_mfma_f32_16x16x32_bf16 v[12:15], a[156:159], a[208:211], v[12:15]\n\t"
    "v_mfma_f32_16x16x32_bf16 v[44:47], a[152:155], a[196:199], v[44:47]\n\t"
    "ds_read_b64_tr_b16 a[146:147], v235 offset:12288\n\t"
    "v_mfma_f32_16x16x32_bf16 v[44:47], a[156:159], a[212:215], v[44:47]\n\t"
    "buffer_load_dwordx4 v240, s[36:39], s56 offen offset:3072 lds\n\t"
    "v_mfma_f32_16x16x32_bf16 v[76:79], a[152:155], a[200:203], v[76:79]\n\t"
    "ds_read_b64_tr_b16 a[148:149], v236 offset:0\n\t"
    "v_mfma_f32_16x16x32_bf16 v[76:79], a[156:159], a[216:219], v[76:79]\n\t"
    "buffer_load_dwordx4 a[232:235], v250, s[44:47], s58 offen offset:2048 sc1\n\t"
    "v_mfma_f32_16x16x32_bf16 v[108:111], a[152:155], a[204:207], v[108:111]\n\t"
    "ds_read_b64_tr_b16 a[150:151], v236 offset:3584\n\t"
    "v_mfma_f32_16x16x32_bf16 v[108:111], a[156:159], a[220:223], v[108:111]\n\t"
    "s_waitcnt lgkmcnt(12)\n\t"
    "v_mfma_f32_16x16x32_bf16 v[16:19], a[160:163], a[192:195], v[16:19]\n\t"
    "ds_read_b64_tr_b16 a[152:153], v236 offset:7168\n\t"
    "v_mfma_f32_16x16x32_bf16 v[16:19], a[164:167], a[208:211], v[16:19]\n\t"
    "s_add_u32 m0, s60, s63\n\t"
    "v_mfma_f32_16x16x32_bf16 v[48:51], a[160:163], a[196:199], v[48:51]\n\t"
    "ds_read_b64_tr_b16 a[154:155], v236 offset:10752\n\t"
    "v_mfma_f32_16x16x32_bf16 v[48:51], a[164:167], a[212:215], v[48:51]\n\t"
    "buffer_load_dwordx4 v241, s[40:43], s57 offen offset:0 lds\n\t"
    "v_mfma_f32_16x16x32_bf16 v[80:83], a[160:163], a[200:203], v[80:83]\n\t"
    "ds_read_b64_tr_b16 a[156:157], v236 offset:32\n\t"
    "v_mfma_f32_16x16x32_bf16 v[80:83], a[164:167], a[216:219], v[80:83]\n\t"
    "buffer_load_dwordx4 a[236:239], v251, s[44:47], s58 offen offset:2048 sc1\n\t"
    "v_mfma_f32_16x16x32_bf16 v[112:115], a[160:163], a[204:207], v[112:115]\n\t"
    "ds_read_b64_tr_b16 a[158:159], v236 offset:3616\n\t"
    "v_mfma_f32_16x16x32_bf16 v[112:115], a[164:167], a[220:223], v[112:115]\n\t"
    "s_waitcnt lgkmcnt(12)\n\t"
    "v_mfma_f32_16x16x32_bf16 v[20:23], a[168:171], a[192:195], v[20:23]\n\t"
    "ds_read_b64_tr_b16 a[160:161], v236 offset:7200\n\t"
    "v_mfma_f32_16x16x32_bf16 v[20:23], a[128:131], a[208:211], v[20:23]\n\t"
    "v_mfma_f32_16x16x32_bf16 v[52:55], a[168:171], a[196:199], v[52:55]\n\t"
    "ds_read_b64_tr_b16 a[162:163], v236 offset:10784\n\t"
    "v_mfma_f32_16x16x32_bf16 v[52:55], a[128:131], a[212:215], v[52:55]\n\t"
    "buffer_load_dwordx4 v242, s[40:43], s57 offen offset:1024 lds\n\t"
    "v_mfma_f32_16x16x32_bf16 v[84:87], a[168:171], a[200:203], v[84:87]\n\t"
    "ds_read_b64_tr_b16 a[164:165], v236 offset:64\n\t"
    "v_mfma_f32_16x16x32_bf16 v[84:87], a[128:131], a[216:219], v[84:87]\n\t"
    "buffer_load_dwordx4 a[240:243], v248, s[44:47], s58 offen offset:3072 sc1\n\t"
    "v_mfma_f32_16x16x32_bf16 v[116:119], a[168:171], a[204:207], v[116:119]\n\t"
    "ds_read_b64_tr_b16 a[166:167], v236 offset:3648\n\t"
    "v_mfma_f32_16x16x32_bf16 v[116:119], a[128:131], a[220:223], v[116:119]\n\t"
    "s_waitcnt lgkmcnt(12)\n\t"
    "v_mfma_f32_16x16x32_bf16 v[24:27], a[132:135], a[192:195], v[24:27]\n\t"
    "ds_read_b64_tr_b16 a[168:169], v236 offset:7232\n\t"
    "v_mfma_f32_16x16x32_bf16 v[24:27], a[136:139], a[208:211], v[24:27]\n\t"
    "v_mfma_f32_16x16x32_bf16 v[56:59], a[132:135], a[196:199], v[56:59]\n\t"
    "ds_read_b64_tr_b16 a[170:171], v236 offset:10816\n\t"
    "v_mfma_f32_16x16x32_bf16 v[56:59], a[136:139], a[212:215], v[56:59]\n\t"
    "buffer_load_dwordx4 v243, s[40:43], s57 offen offset:2048 lds\n\t"
    "v_mfma_f32_16x16x32_bf16 v[88:91], a[132:135], a[200:203], v[88:91]\n\t"
    "ds_read_b64_tr_b16 a[128:129], v236 offset:96\n\t"
    "v_mfma_f32_16x16x32_bf16 v[88:91], a[136:139], a[216:219], v[88:91]\n\t"
    "buffer_load_dwordx4 a[244:247], v249, s[44:47], s58 offen offset:3072 sc1\n\t"
    "v_mfma_f32_16x16x32_bf16 v[120:123], a[132:135], a[204:207], v[120:123]\n\t"
    "ds_read_b64_tr_b16 a[130:131], v236 offset:3680\n\t"
    "v_mfma_f32_16x16x32_bf16 v[120:123], a[136:139], a[220:223], v[120:123]\n\t"
    "s_waitcnt lgkmcnt(12)\n\t"
    "v_mfma_f32_16x16x32_bf16 v[28:31], a[140:143], a[192:195], v[28:31]\n\t"
    "ds_read_b64_tr_b16 a[132:133], v236 offset:7264\n\t"
    "v_mfma_f32_16x16x32_bf16 v[28:31], a[144:147], a[208:211], v[28:31]\n\t"
    "v_mfma_f32_16x16x32_bf16 v[60:63], a[140:143], a[196:199], v[60:63]\n\t"
    "ds_read_b64_tr_b16 a[134:135], v236 offset:10848\n\t"
    "v_mfma_f32_16x16x32_bf16 v[60:63], a[144:147], a[212:215], v[60:63]\n\t"
    "buffer_load_dwordx4 v244, s[40:43], s57 offen offset:3072 lds\n\t"
    "v_mfma_f32_16x16x32_bf16 v[92:95], a[140:143], a[200:203], v[92:95]\n\t"
    "ds_read_b64_tr_b16 a[136:137], v236 offset:128\n\t"
    "v_mfma_f32_16x16x32_bf16 v[92:95], a[144:147], a[216:219], v[92:95]\n\t"
    "buffer_load_dwordx4 a[248:251], v250, s[44:47], s58 offen offset:3072 sc1\n\t"
    "v_mfma_f32_16x16x32_bf16 v[124:127], a[140:143], a[204:207], v[124:127]\n\t"
    "ds_read_b64_tr_b16 a[138:139], v236 offset:3712\n\t"
    "v_mfma_f32_16x16x32_bf16 v[124:127], a[144:147], a[220:223], v[124:127]\n\t"
    "s_waitcnt lgkmcnt(12)\n\t"
    "v_mfma_f32_16x16x32_bf16 v[128:131], a[148:151], a[192:195], v[128:131]\n\t"
    "ds_read_b64_tr_b16 a[140:141], v236 offset:7296\n\t"
    "v_mfma_f32_16x16x32_bf16 v[128:131], a[152:155], a[208:211], v[128:131]\n\t"
    "s_add_u32 m0, m0, 4096\n\t"
    "v_mfma_f32_16x16x32_bf16 v[132:135], a[148:151], a[196:199], v[132:135]\n\t"
    "ds_read_b64_tr_b16 a[142:143], v236 offset:10880\n\t"
    "v_mfma_f32_16x16x32_bf16 v[132:135], a[152:155], a[212:215], v[132:135]\n\t"
    "buffer_load_dwordx4 v245, s[40:43], s57 offen offset:0 lds\n\t"
    "v_mfma_f32_16x16x32_bf16 v[136:139], a[148:151], a[200:203], v[136:139]\n\t"
    "ds_read_b64_tr_b16 a[144:145], v236 offset:160\n\t"
    "v_mfma_f32_16x16x32_bf16 v[136:139], a[152:155], a[216:219], v[136:139]\n\t"
    "buffer_load_dwordx4 a[252:255], v251, s[44:47], s58 offen offset:3072 sc1\n\t"
    "v_mfma_f32_16x16x32_bf16 v[140:143], a[148:151], a[204:207], v[140:143]\n\t"
    "ds_read_b64_tr_b16 a[146:147], v236 offset:3744\n\t"
    "v_mfma_f32_16x16x32_bf16 v[140:143], a[152:155], a[220:223], v[140:143]\n\t"
    "s_waitcnt lgkmcnt(12)\n\t"
    "v_mfma_f32_16x16x32_bf16 v[144:147], a[156:159], a[192:195], v[144:147]\n\t"
    "ds_read_b64_tr_b16 a[148:149], v236 offset:7328\n\t"
    "v_mfma_f32_16x16x32_bf16 v[144:147], a[160:163], a[208:211], v[144:147]\n\t"
    "v_mfma_f32_16x16x32_bf16 v[148:151], a[156:159], a[196:199], v[148:151]\n\t"
    "ds_read_b64_tr_b16 a[150:151], v236 offset:10912\n\t"
    "s_add_u32 s58, s58, 4096\n\t"
    "v_mfma_f32_16x16x32_bf16 v[148:151], a[160:163], a[212:215], v[148:151]\n\t"
    "buffer_load_dwordx4 v246, s[40:43], s57 offen offset:1024 lds\n\t"
    "v_mfma_f32_16x16x32_bf16 v[152:155], a[156:159], a[200:203], v[152:155]\n\t"
    "ds_read_b64_tr_b16 a[152:153], v236 offset:192\n\t"
    "v_mfma_f32_16x16x32_bf16 v[152:155], a[160:163], a[216:219], v[152:155]\n\t"
    "v_mfma_f32_16x16x32_bf16 v[156:159], a[156:159], a[204:207], v[156:159]\n\t"
    "ds_read_b64_tr_b16 a[154:155], v236 offset:3776\n\t"
    "v_mfma_f32_16x16x32_bf16 v[156:159], a[160:163], a[220:223], v[156:159]\n\t"
    "s_waitcnt lgkmcnt(12)\n\t"
    "v_mfma_f32_16x16x32_bf16 v[160:163], a[164:167], a[192:195], v[160:163]\n\t"
    "ds_read_b64_tr_b16 a[156:157], v236 offset:7360\n\t"
    "v_mfma_f32_16x16x32_bf16 v[160:163], a[168:171], a[208:211], v[160:163]\n\t"
    "v_mfma_f32_16x16x32_bf16 v[164:167], a[164:167], a[196:199], v[164:167]\n\t"
    "ds_read_b64_tr_b16 a[158:159], v236 offset:10944\n\t"
    "v_mfma_f32_16x16x32_bf16 v[164:167], a[168:171], a[212:215], v[164:167]\n\t"
    "buffer_load_dwordx4 v247, s[40:43], s57 offen offset:2048 lds\n\t"
    "v_mfma_f32_16x16x32_bf16 v[168:171], a[164:167], a[200:203], v[168:171]\n\t"
    "ds_read_b64_tr_b16 a[160:161], v236 offset:14336\n\t"
    "v_mfma_f32_16x16x32_bf16 v[168:171], a[168:171], a[216:219], v[168:171]\n\t"
    "v_mfma_f32_16x16x32_bf16 v[172:175], a[164:167], a[204:207], v[172:175]\n\t"
    "ds_read_b64_tr_b16 a[162:163], v236 offset:17920\n\t"
    "v_mfma_f32_16x16x32_bf16 v[172:175], a[168:171], a[220:223], v[172:175]\n\t"
    "s_waitcnt lgkmcnt(12)\n\t"
    "v_mfma_f32_16x16x32_bf16 v[176:179], a[128:131], a[192:195], v[176:179]\n\t"
    "ds_read_b64_tr_b16 a[164:165], v236 offset:21504\n\t"
    "v_mfma_f32_16x16x32_bf16 v[176:179], a[132:135], a[208:211], v[176:179]\n\t"
    "v_mfma_f32_16x16x32_bf16 v[180:183], a[128:131], a[196:199], v[180:183]\n\t"
    "ds_read_b64_tr_b16 a[166:167], v236 offset:25088\n\t"
    "v_mfma_f32_16x16x32_bf16 v[180:183], a[132:135], a[212:215], v[180:183]\n\t"
    "v_mfma_f32_16x16x32_bf16 v[184:187], a[128:131], a[200:203], v[184:187]\n\t"
    "ds_read_b64_tr_b16 a[168:169], v236 offset:14368\n\t"
    "v_mfma_f32_16x16x32_bf16 v[184:187], a[132:135], a[216:219], v[184:187]\n\t"
    "v_mfma_f32_16x16x32_bf16 v[188:191], a[128:131], a[204:207], v[188:191]\n\t"
    "ds_read_b64_tr_b16 a[170:171], v236 offset:17952\n\t"
    "v_mfma_f32_16x16x32_bf16 v[188:191], a[132:135], a[220:223], v[188:191]\n\t"
    "s_waitcnt lgkmcnt(12)\n\t"
    "v_mfma_f32_16x16x32_bf16 v[192:195], a[136:139], a[192:195], v[192:195]\n\t"
    "ds_read_b64_tr_b16 a[128:129], v236 offset:21536\n\t"
    "v_mfma_f32_16x16x32_bf16 v[192:195], a[140:143], a[208:211], v[192:195]\n\t"
    "v_mfma_f32_16x16x32_bf16 v[196:199], a[136:139], a[196:199], v[196:199]\n\t"
    "ds_read_b64_tr_b16 a[130:131], v236 offset:25120\n\t"
    "v_mfma_f32_16x16x32_bf16 v[196:199], a[140:143], a[212:215], v[196:199]\n\t"
    "s_add_u32 s65, s56, 1572864\n\t"
    "v_mfma_f32_16x16x32_bf16 v[200:203], a[136:139], a[200:203], v[200:203]\n\t"
    "ds_read_b64_tr_b16 a[132:133], v236 offset:14400\n\t"
    "v_mfma_f32_16x16x32_bf16 v[200:203], a[140:143], a[216:219], v[200:203]\n\t"
    "buffer_load_dword a190, v252, s[36:39], s65 offen\n\t"
    "v_mfma_f32_16x16x32_bf16 v[204:207], a[136:139], a[204:207], v[204:207]\n\t"
    "ds_read_b64_tr_b16 a[134:135], v236 offset:17984\n\t"
    "v_mfma_f32_16x16x32_bf16 v[204:207], a[140:143], a[220:223], v[204:207]\n\t"
    "s_waitcnt lgkmcnt(12)\n\t"
    "s_add_u32 s65, s57, 1376256\n\t"
    "v_mfma_f32_16x16x32_bf16 v[208:211], a[144:147], a[192:195], v[208:211]\n\t"
    "ds_read_b64_tr_b16 a[136:137], v236 offset:21568\n\t"
    "v_mfma_f32_16x16x32_bf16 v[208:211], a[148:151], a[208:211], v[208:211]\n\t"
    "buffer_load_dword a190, v253, s[40:43], s65 offen\n\t"
    "v_mfma_f32_16x16x32_bf16 v[212:215], a[144:147], a[196:199], v[212:215]\n\t"
    "ds_read_b64_tr_b16 a[138:139], v236 offset:25152\n\t"
    "v_mfma_f32_16x16x32_bf16 v[212:215], a[148:151], a[212:215], v[212:215]\n\t"
    "v_mfma_f32_16x16x32_bf16 v[216:219], a[144:147], a[200:203], v[216:219]\n\t"
    "ds_read_b64_tr_b16 a[140:141], v236 offset:14432\n\t"
    "v_mfma_f32_16x16x32_bf16 v[216:219], a[148:151], a[216:219], v[216:219]\n\t"
    "s_add_u32 s56, s56, 524288\n\t"
    "v_mfma_f32_16x16x32_bf16 v[220:223], a[144:147], a[204:207], v[220:223]\n\t"
    "ds_read_b64_tr_b16 a[142:143], v236 offset:18016\n\t"
    "v_mfma_f32_16x16x32_bf16 v[220:223], a[148:151], a[220:223], v[220:223]\n\t"
    "s_waitcnt lgkmcnt(12)\n\t"
    "v_mfma_f32_16x16x32_bf16 v[224:227], a[152:155], a[192:195], v[224:227]\n\t"
    "ds_read_b64_tr_b16 a[144:145], v236 offset:21600\n\t"
    "v_mfma_f32_16x16x32_bf16 v[224:227], a[156:159], a[208:211], v[224:227]\n\t"
    "s_add_u32 s57, s57, 458752\n\t"
    "v_mfma_f32_16x16x32_bf16 a[112:115], a[152:155], a[196:199], a[112:115]\n\t"
    "ds_read_b64_tr_b16 a[146:147], v236 offset:25184\n\t"
    "v_mfma_f32_16x16x32_bf16 a[112:115], a[156:159], a[212:215], a[112:115]\n\t"
    "v_mfma_f32_16x16x32_bf16 a[116:119], a[152:155], a[200:203], a[116:119]\n\t"
    "ds_read_b64_tr_b16 a[148:149], v236 offset:14464\n\t"
    "v_mfma_f32_16x16x32_bf16 a[116:119], a[156:159], a[216:219], a[116:119]\n\t"
    "buffer_store_dwordx4 a[124:127], v248, s[80:83], s67 offen sc1\n\t"
    "v_mfma_f32_16x16x32_bf16 a[120:123], a[152:155], a[204:207], a[120:123]\n\t"
    "ds_read_b64_tr_b16 a[150:151], v236 offset:18048\n\t"
    "v_mfma_f32_16x16x32_bf16 a[120:123], a[156:159], a[220:223], a[120:123]\n\t"
    "s_waitcnt lgkmcnt(12)\n\t"
    "v_mfma_f32_16x16x32_bf16 a[0:3], a[160:163], a[192:195], a[0:3]\n\t"
    "ds_read_b64_tr_b16 a[152:153], v236 offset:21632\n\t"
    "v_mfma_f32_16x16x32_bf16 a[0:3], a[164:167], a[208:211], a[0:3]\n\t"
    "v_mfma_f32_16x16x32_bf16 a[4:7], a[160:163], a[196:199], a[4:7]\n\t"
    "ds_read_b64_tr_b16 a[154:155], v236 offset:25216\n\t"
    "v_mfma_f32_16x16x32_bf16 a[4:7], a[164:167], a[212:215], a[4:7]\n\t"
    "v_mfma_f32_16x16x32_bf16 a[8:11], a[160:163], a[200:203], a[8:11]\n\t"
    "ds_read_b64_tr_b16 a[156:157], v236 offset:14496\n\t"
    "v_mfma_f32_16x16x32_bf16 a[8:11], a[164:167], a[216:219], a[8:11]\n\t"
    "buffer_store_dwordx4 a[124:127], v248, s[80:83], s67 offen offset:1024 sc1\n\t"
    "v_mfma_f32_16x16x32_bf16 a[12:15], a[160:163], a[204:207], a[12:15]\n\t"
    "ds_read_b64_tr_b16 a[158:159], v236 offset:18080\n\t"
    "s_add_u32 s67, s67, 2048\n\t"
    "v_mfma_f32_16x16x32_bf16 a[12:15], a[164:167], a[220:223], a[12:15]\n\t"
    "s_waitcnt lgkmcnt(12)\n\t"
    "s_mov_b32 s60, s59\n\t"
    "v_mfma_f32_16x16x32_bf16 a[16:19], a[168:171], a[192:195], a[16:19]\n\t"
    "ds_read_b64_tr_b16 a[160:161], v236 offset:21664\n\t"
    "v_mfma_f32_16x16x32_bf16 a[16:19], a[128:131], a[208:211], a[16:19]\n\t"
    "s_add_u32 s65, s59, 45056\n\t"
    "v_mfma_f32_16x16x32_bf16 a[20:23], a[168:171], a[196:199], a[20:23]\n\t"
    "ds_read_b64_tr_b16 a[162:163], v236 offset:25248\n\t"
    "v_mfma_f32_16x16x32_bf16 a[20:23], a[128:131], a[212:215], a[20:23]\n\t"
    "s_cmp_eq_u32 s59, 118784\n\t"
    "s_cselect_b32 s59, 28672, s65\n\t"
    "v_mfma_f32_16x16x32_bf16 a[24:27], a[168:171], a[200:203], a[24:27]\n\t"
    "ds_read_b64_tr_b16 a[164:165], v236 offset:14528\n\t"
    "v_mfma_f32_16x16x32_bf16 a[24:27], a[128:131], a[216:219], a[24:27]\n\t"
    "s_sub_u32 s61, s59, s60\n\t"
    "v_mfma_f32_16x16x32_bf16 a[28:31], a[168:171], a[204:207], a[28:31]\n\t"
    "ds_read_b64_tr_b16 a[166:167], v236 offset:18112\n\t"
    "v_mfma_f32_16x16x32_bf16 a[28:31], a[128:131], a[220:223], a[28:31]\n\t"
    "s_waitcnt lgkmcnt(12)\n\t"
    "v_mfma_f32_16x16x32_bf16 a[32:35], a[132:135], a[192:195], a[32:35]\n\t"
    "ds_read_b64_tr_b16 a[168:169], v236 offset:21696\n\t"
    "v_mfma_f32_16x16x32_bf16 a[32:35], a[136:139], a[208:211], a[32:35]\n\t"
    "v_mfma_f32_16x16x32_bf16 a[36:39], a[132:135], a[196:199], a[36:39]\n\t"
    "ds_read_b64_tr_b16 a[170:171], v236 offset:25280\n\t"
    "v_mfma_f32_16x16x32_bf16 a[36:39], a[136:139], a[212:215], a[36:39]\n\t"
    "v_add_u32 v228, s61, v228\n\t"
    "v_add_u32 v229, s61, v229\n\t"
    "v_mfma_f32_16x16x32_bf16 a[40:43], a[132:135], a[200:203], a[40:43]\n\t"
    "v_add_u32 v230, s61, v230\n\t"
    "v_mfma_f32_16x16x32_bf16 a[40:43], a[136:139], a[216:219], a[40:43]\n\t"
    "v_add_u32 v231, s61, v231\n\t"
    "v_add_u32 v232, s61, v232\n\t"
    "v_mfma_f32_16x16x32_bf16 a[44:47], a[132:135], a[204:207], a[44:47]\n\t"
    "v_add_u32 v233, s61, v233\n\t"
    "v_mfma_f32_16x16x32_bf16 a[44:47], a[136:139], a[220:223], a[44:47]\n\t"
    "s_waitcnt lgkmcnt(12)\n\t"
    "v_add_u32 v234, s61, v234\n\t"
    "v_add_u32 v235, s61, v235\n\t"
    "v_mfma_f32_16x16x32_bf16 a[48:51], a[140:143], a[192:195], a[48:51]\n\t"
    "v_add_u32 v236, s61, v236\n\t"
    "v_mfma_f32_16x16x32_bf16 a[48:51], a[144:147], a[208:211], a[48:51]\n\t"
    "v_mfma_f32_16x16x32_bf16 a[52:55], a[140:143], a[196:199], a[52:55]\n\t"
    "s_waitcnt vmcnt(27) lgkmcnt(0)\n\t"
    "s_barrier\n\t"
    "v_mfma_f32_16x16x32_bf16 a[52:55], a[144:147], a[212:215], a[52:55]\n\t"
    "ds_read_b64_tr_b16 a[128:129], v228 offset:0\n\t"
    "v_mfma_f32_16x16x32_bf16 a[56:59], a[140:143], a[200:203], a[56:59]\n\t"
    "ds_read_b64_tr_b16 a[130:131], v228 offset:4096\n\t"
    "v_mfma_f32_16x16x32_bf16 a[56:59], a[144:147], a[216:219], a[56:59]\n\t"
    "ds_read_b64_tr_b16 a[132:133], v228 offset:8192\n\t"
    "v_mfma_f32_16x16x32_bf16 a[60:63], a[140:143], a[204:207], a[60:63]\n\t"
    "ds_read_b64_tr_b16 a[134:135], v228 offset:12288\n\t"
    "v_mfma_f32_16x16x32_bf16 a[60:63], a[144:147], a[220:223], a[60:63]\n\t"
    "ds_read_b64_tr_b16 a[136:137], v229 offset:0\n\t"
    "s_waitcnt lgkmcnt(12)\n\t"
    "v_mfma_f32_16x16x32_bf16 a[64:67], a[148:151], a[192:195], a[64:67]\n\t"
    "ds_read_b64_tr_b16 a[138:139], v229 offset:4096\n\t"
    "ds_read_b64_tr_b16 a[140:141], v229 offset:8192\n\t"
    "v_mfma_f32_16x16x32_bf16 a[64:67], a[152:155], a[208:211], a[64:67]\n\t"
    "v_mfma_f32_16x16x32_bf16 a[68:71], a[148:151], a[196:199], a[68:71]\n\t"
    "ds_read_b64_tr_b16 a[142:143], v229 offset:12288\n\t"
    "v_mfma_f32_16x16x32_bf16 a[68:71], a[152:155], a[212:215], a[68:71]\n\t"
    "v_mfma_f32_16x16x32_bf16 a[72:75], a[148:151], a[200:203], a[72:75]\n\t"
    "ds_read_b64_tr_b16 a[144:145], v230 offset:0\n\t"
    "v_mfma_f32_16x16x32_bf16 a[72:75], a[152:155], a[216:219], a[72:75]\n\t"
    "v_mfma_f32_16x16x32_bf16 a[76:79], a[148:151], a[204:207], a[76:79]\n\t"
    "ds_read_b64_tr_b16 a[146:147], v230 offset:4096\n\t"
    "v_mfma_f32_16x16x32_bf16 a[76:79], a[152:155], a[220:223], a[76:79]\n\t"
    "s_waitcnt lgkmcnt(12)\n\t"
    "v_mfma_f32_16x16x32_bf16 a[80:83], a[156:159], a[192:195], a[80:83]\n\t"
    "ds_read_b64_tr_b16 a[148:149], v230 offset:8192\n\t"
    "v_mfma_f32_16x16x32_bf16 a[80:83], a[160:163], a[208:211], a[80:83]\n\t"
    "v_mfma_f32_16x16x32_bf16 a[84:87], a[156:159], a[196:199], a[84:87]\n\t"
    "ds_read_b64_tr_b16 a[150:151], v230 offset:12288\n\t"
    "v_mfma_f32_16x16x32_bf16 a[84:87], a[160:163], a[212:215], a[84:87]\n\t"
    "v_mfma_f32_16x16x32_bf16 a[88:91], a[156:159], a[200:203], a[88:91]\n\t"
    "ds_read_b64_tr_b16 a[152:153], v231 offset:0\n\t"
    "v_mfma_f32_16x16x32_bf16 a[88:91], a[160:163], a[216:219], a[88:91]\n\t"
    "v_mfma_f32_16x16x32_bf16 a[92:95], a[156:159], a[204:207], a[92:95]\n\t"
    "ds_read_b64_tr_b16 a[154:155], v231 offset:4096\n\t"
    "v_mfma_f32_16x16x32_bf16 a[92:95], a[160:163], a[220:223], a[92:95]\n\t"
    "s_waitcnt lgkmcnt(12)\n\t"
    "v_mfma_f32_16x16x32_bf16 a[96:99], a[164:167], a[192:195], a[96:99]\n\t"
    "ds_read_b64_tr_b16 a[156:157], v231 offset:8192\n\t"
    "v_mfma_f32_16x16x32_bf16 a[96:99], a[168:171], a[208:211], a[96:99]\n\t"
    "v_mfma_f32_16x16x32_bf16 a[100:103], a[164:167], a[196:199], a[100:103]\n\t"
    "ds_read_b64_tr_b16 a[158:159], v231 offset:12288\n\t"
    "v_mfma_f32_16x16x32_bf16 a[100:103], a[168:171], a[212:215], a[100:103]\n\t"
    "v_mfma_f32_16x16x32_bf16 a[104:107], a[164:167], a[200:203], a[104:107]\n\t"
    "ds_read_b64_tr_b16 a[160:161], v232 offset:0\n\t"
    "v_mfma_f32_16x16x32_bf16 a[104:107], a[168:171], a[216:219], a[104:107]\n\t"
    "v_mfma_f32_16x16x32_bf16 a[108:111], a[164:167], a[204:207], a[108:111]\n\t"
    "ds_read_b64_tr_b16 a[162:163], v232 offset:4096\n\t"
    "v_mfma_f32_16x16x32_bf16 a[108:111], a[168:171], a[220:223], a[108:111]\n\t"
    "s_waitcnt lgkmcnt(12) vmcnt(6)\n\t"
    "v_mfma_f32_16x16x32_bf16 v[0:3], a[128:131], a[224:227], v[0:3]\n\t"
    "ds_read_b64_tr_b16 a[164:165], v232 offset:8192\n\t"
    "v_mfma_f32_16x16x32_bf16 v[0:3], a[132:135], a[240:243], v[0:3]\n\t"
    "s_add_u32 m0, s60, s62\n\t"
    "v_mfma_f32_16x16x32_bf16 v[32:35], a[128:131], a[228:231], v[32:35]\n\t"
    "ds_read_b64_tr_b16 a[166:167], v232 offset:12288\n\t"
    "v_mfma_f32_16x16x32_bf16 v[32:35], a[132:135], a[244:247], v[32:35]\n\t"
    "buffer_load_dwordx4 v237, s[36:39], s56 offen offset:0 lds\n\t"
    "v_mfma_f32_16x16x32_bf16 v[64:67], a[128:131], a[232:235], v[64:67]\n\t"
    "ds_read_b64_tr_b16 a[168:169], v233 offset:0\n\t"
    "v_mfma_f32_16x16x32_bf16 v[64:67], a[132:135], a[248:251], v[64:67]\n\t"
    "v_mfma_f32_16x16x32_bf16 v[96:99], a[128:131], a[236:239], v[96:99]\n\t"
    "ds_read_b64_tr_b16 a[170:171], v233 offset:4096\n\t"
    "v_mfma_f32_16x16x32_bf16 v[96:99], a[132:135], a[252:255], v[96:99]\n\t"
    "s_waitcnt lgkmcnt(12)\n\t"
    "v_mfma_f32_16x16x32_bf16 v[4:7], a[136:139], a[224:227], v[4:7]\n\t"
    "ds_read_b64_tr_b16 a[128:129], v233 offset:8192\n\t"
    "v_mfma_f32_16x16x32_bf16 v[4:7], a[140:143], a[240:243], v[4:7]\n\t"
    "v_mfma_f32_16x16x32_bf16 v[36:39], a[136:139], a[228:231], v[36:39]\n\t"
    "ds_read_b64_tr_b16 a[130:131], v233 offset:12288\n\t"
    "v_mfma_f32_16x16x32_bf16 v[36:39], a[140:143], a[244:247], v[36:39]\n\t"
    "buffer_load_dwordx4 v238, s[36:39], s56 offen offset:1024 lds\n\t"
    "v_mfma_f32_16x16x32_bf16 v[68:71], a[136:139], a[232:235], v[68:71]\n\t"
    "ds_read_b64_tr_b16 a[132:133], v234 offset:0\n\t"
    "v_mfma_f32_16x16x32_bf16 v[68:71], a[140:143], a[248:251], v[68:71]\n\t"
    "buffer_load_dwordx4 a[192:195], v248, s[44:47], s58 offen offset:0 sc1\n\t"
    "v_mfma_f32_16x16x32_bf16 v[100:103], a[136:139], a[236:239], v[100:103]\n\t"
    "ds_read_b64_tr_b16 a[134:135], v234 offset:4096\n\t"
    "v_mfma_f32_16x16x32_bf16 v[100:103], a[140:143], a[252:255], v[100:103]\n\t"
    "s_waitcnt lgkmcnt(12)\n\t"
    "v_mfma_f32_16x16x32_bf16 v[8:11], a[144:147], a[224:227], v[8:11]\n\t"
    "ds_read_b64_tr_b16 a[136:137], v234 offset:8192\n\t"
    "v_mfma_f32_16x16x32_bf16 v[8:11], a[148:151], a[240:243], v[8:11]\n\t"
    "v_mfma_f32_16x16x32_bf16 v[40:43], a[144:147], a[228:231], v[40:43]\n\t"
    "ds_read_b64_tr_b16 a[138:139], v234 offset:12288\n\t"
    "v_mfma_f32_16x16x32_bf16 v[40:43], a[148:151], a[244:247], v[40:43]\n\t"
    "buffer_load_dwordx4 v239, s[36:39], s56 offen offset:2048 lds\n\t"
    "v_mfma_f32_16x16x32_bf16 v[72:75], a[144:147], a[232:235], v[72:75]\n\t"
    "ds_read_b64_tr_b16 a[140:141], v235 offset:0\n\t"
    "v_mfma_f32_16x16x32_bf16 v[72:75], a[148:151], a[248:251], v[72:75]\n\t"
    "buffer_load_dwordx4 a[196:199], v249, s[44:47], s58 offen offset:0 sc1\n\t"
    "v_mfma_f32_16x16x32_bf16 v[104:107], a[144:147], a[236:239], v[104:107]\n\t"
    "ds_read_b64_tr_b16 a[142:143], v235 offset:4096\n\t"
    "v_mfma_f32_16x16x32_bf16 v[104:107], a[148:151], a[252:255], v[104:107]\n\t"
    "s_waitcnt lgkmcnt(12)\n\t"
    "v_mfma_f32_16x16x32_bf16 v[12:15], a[152:155], a[224:227], v[12:15]\n\t"
    "ds_read_b64_tr_b16 a[144:145], v235 offset:8192\n\t"
    "v_mfma_f32_16x16x32_bf16 v[12:15], a[156:159], a[240:243], v[12:15]\n\t"
    "v_mfma_f32_16x16x32_bf16 v[44:47], a[152:155], a[228:231], v[44:47]\n\t"
    "ds_read_b64_tr_b16 a[146:147], v235 offset:12288\n\t"
    "v_mfma_f32_16x16x32_bf16 v[44:47], a[156:159], a[244:247], v[44:47]\n\t"
    "buffer_load_dwordx4 v240, s[36:39], s56 offen offset:3072 lds\n\t"
    "v_mfma_f32_16x16x32_bf16 v[76:79], a[152:155], a[232:235], v[76:79]\n\t"
    "ds_read_b64_tr_b16 a[148:149], v236 offset:0\n\t"
    "v_mfma_f32_16x16x32_bf16 v[76:79], a[156:159], a[248:251], v[76:79]\n\t"
    "buffer_load_dwordx4 a[200:203], v250, s[44:47], s58 offen offset:0 sc1\n\t"
    "v_mfma_f32_16x16x32_bf16 v[108:111], a[152:155], a[236:239], v[108:111]\n\t"
    "ds_read_b64_tr_b16 a[150:151], v236 offset:3584\n\t"
    "v_mfma_f32_16x16x32_bf16 v[108:111], a[156:159], a[252:255], v[108:111]\n\t"
    "s_waitcnt lgkmcnt(12)\n\t"
    "v_mfma_f32_16x16x32_bf16 v[16:19], a[160:163], a[224:227], v[16:19]\n\t"
    "ds_read_b64_tr_b16 a[152:153], v236 offset:7168\n\t"
    "v_mfma_f32_16x16x32_bf16 v[16:19], a[164:167], a[240:243], v[16:19]\n\t"
    "s_add_u32 m0, s60, s63\n\t"
    "v_mfma_f32_16x16x32_bf16 v[48:51], a[160:163], a[228:231], v[48:51]\n\t"
    "ds_read_b64_tr_b16 a[154:155], v236 offset:10752\n\t"
    "v_mfma_f32_16x16x32_bf16 v[48:51], a[164:167], a[244:247], v[48:51]\n\t"
    "buffer_load_dwordx4 v241, s[40:43], s57 offen offset:0 lds\n\t"
    "v_mfma_f32_16x16x32_bf16 v[80:83], a[160:163], a[232:235], v[80:83]\n\t"
    "ds_read_b64_tr_b16 a[156:157], v236 offset:32\n\t"
    "v_mfma_f32_16x16x32_bf16 v[80:83], a[164:167], a[248:251], v[80:83]\n\t"
    "buffer_load_dwordx4 a[204:207], v251, s[44:47], s58 offen offset:0 sc1\n\t"
    "v_mfma_f32_16x16x32_bf16 v[112:115], a[160:163], a[236:239], v[112:115]\n\t"
    "ds_read_b64_tr_b16 a[158:159], v236 offset:3616\n\t"
    "v_mfma_f32_16x16x32_bf16 v[112:115], a[164:167], a[252:255], v[112:115]\n\t"
    "s_waitcnt lgkmcnt(12)\n\t"
    "v_mfma_f32_16x16x32_bf16 v[20:23], a[168:171], a[224:227], v[20:23]\n\t"
    "ds_read_b64_tr_b16 a[160:161], v236 offset:7200\n\t"
    "v_mfma_f32_16x16x32_bf16 v[20:23], a[128:131], a[240:243], v[20:23]\n\t"
    "v_mfma_f32_16x16x32_bf16 v[52:55], a[168:171], a[228:231], v[52:55]\n\t"
    "ds_read_b64_tr_b16 a[162:163], v236 offset:10784\n\t"
    "v_mfma_f32_16x16x32_bf16 v[52:55], a[128:131], a[244:247], v[52:55]\n\t"
    "buffer_load_dwordx4 v242, s[40:43], s57 offen offset:1024 lds\n\t"
    "v_mfma_f32_16x16x32_bf16 v[84:87], a[168:171], a[232:235], v[84:87]\n\t"
    "ds_read_b64_tr_b16 a[164:165], v236 offset:64\n\t"
    "v_mfma_f32_16x16x32_bf16 v[84:87], a[128:131], a[248:251], v[84:87]\n\t"
    "buffer_load_dwordx4 a[208:211], v248, s[44:47], s58 offen offset:1024 sc1\n\t"
    "v_mfma_f32_16x16x32_bf16 v[116:119], a[168:171], a[236:239], v[116:119]\n\t"
    "ds_read_b64_tr_b16 a[166:167], v236 offset:3648\n\t"
    "v_mfma_f32_16x16x32_bf16 v[116:119], a[128:131], a[252:255], v[116:119]\n\t"
    "s_waitcnt lgkmcnt(12)\n\t"
    "v_mfma_f32_16x16x32_bf16 v[24:27], a[132:135], a[224:227], v[24:27]\n\t"
    "ds_read_b64_tr_b16 a[168:169], v236 offset:7232\n\t"
    "v_mfma_f32_16x16x32_bf16 v[24:27], a[136:139], a[240:243], v[24:27]\n\t"
    "v_mfma_f32_16x16x32_bf16 v[56:59], a[132:135], a[228:231], v[56:59]\n\t"
    "ds_read_b64_tr_b16 a[170:171], v236 offset:10816\n\t"
    "v_mfma_f32_16x16x32_bf16 v[56:59], a[136:139], a[244:247], v[56:59]\n\t"
    "buffer_load_dwordx4 v243, s[40:43], s57 offen offset:2048 lds\n\t"
    "v_mfma_f32_16x16x32_bf16 v[88:91], a[132:135], a[232:235], v[88:91]\n\t"
    "ds_read_b64_tr_b16 a[128:129], v236 offset:96\n\t"
    "v_mfma_f32_16x16x32_bf16 v[88:91], a[136:139], a[248:251], v[88:91]\n\t"
    "buffer_load_dwordx4 a[212:215], v249, s[44:47], s58 offen offset:1024 sc1\n\t"
    "v_mfma_f32_16x16x32_bf16 v[120:123], a[132:135], a[236:239], v[120:123]\n\t"
    "ds_read_b64_tr_b16 a[130:131], v236 offset:3680\n\t"
    "v_mfma_f32_16x16x32_bf16 v[120:123], a[136:139], a[252:255], v[120:123]\n\t"
    "s_waitcnt lgkmcnt(12)\n\t"
    "v_mfma_f32_16x16x32_bf16 v[28:31], a[140:143], a[224:227], v[28:31]\n\t"
    "ds_read_b64_tr_b16 a[132:133], v236 offset:7264\n\t"
    "v_mfma_f32_16x16x32_bf16 v[28:31], a[144:147], a[240:243], v[28:31]\n\t"
    "v_mfma_f32_16x16x32_bf16 v[60:63], a[140:143], a[228:231], v[60:63]\n\t"
    "ds_read_b64_tr_b16 a[134:135], v236 offset:10848\n\t"
    "v_mfma_f32_16x16x32_bf16 v[60:63], a[144:147], a[244:247], v[60:63]\n\t"
    "buffer_load_dwordx4 v244, s[40:43], s57 offen offset:3072 lds\n\t"
    "v_mfma_f32_16x16x32_bf16 v[92:95], a[140:143], a[232:235], v[92:95]\n\t"
    "ds_read_b64_tr_b16 a[136:137], v236 offset:128\n\t"
    "v_mfma_f32_16x16x32_bf16 v[92:95], a[144:147], a[248:251], v[92:95]\n\t"
    "buffer_load_dwordx4 a[216:219], v250, s[44:47], s58 offen offset:1024 sc1\n\t"
    "v_mfma_f32_16x16x32_bf16 v[124:127], a[140:143], a[236:239], v[124:127]\n\t"
    "ds_read_b64_tr_b16 a[138:139], v236 offset:3712\n\t"
    "v_mfma_f32_16x16x32_bf16 v[124:127], a[144:147], a[252:255], v[124:127]\n\t"
    "s_waitcnt lgkmcnt(12)\n\t"
    "v_mfma_f32_16x16x32_bf16 v[128:131], a[148:151], a[224:227], v[128:131]\n\t"
    "ds_read_b64_tr_b16 a[140:141], v236 offset:7296\n\t"
    "v_mfma_f32_16x16x32_bf16 v[128:131], a[152:155], a[240:243], v[128:131]\n\t"
    "s_add_u32 m0, m0, 4096\n\t"
    "v_mfma_f32_16x16x32_bf16 v[132:135], a[148:151], a[228:231], v[132:135]\n\t"
    "ds_read_b64_tr_b16 a[142:143], v236 offset:10880\n\t"
    "v_mfma_f32_16x16x32_bf16 v[132:135], a[152:155], a[244:247], v[132:135]\n\t"
    "buffer_load_dwordx4 v245, s[40:43], s57 offen offset:0 lds\n\t"
    "v_mfma_f32_16x16x32_bf16 v[136:139], a[148:151], a[232:235], v[136:139]\n\t"
    "ds_read_b64_tr_b16 a[144:145], v236 offset:160\n\t"
    "v_mfma_f32_16x16x32_bf16 v[136:139], a[152:155], a[248:251], v[136:139]\n\t"
    "buffer_load_dwordx4 a[220:223], v251, s[44:47], s58 offen offset:1024 sc1\n\t"
    "v_mfma_f32_16x16x32_bf16 v[140:143], a[148:151], a[236:239], v[140:143]\n\t"
    "ds_read_b64_tr_b16 a[146:147], v236 offset:3744\n\t"
    "v_mfma_f32_16x16x32_bf16 v[140:143], a[152:155], a[252:255], v[140:143]\n\t"
    "s_waitcnt lgkmcnt(12)\n\t"
    "v_mfma_f32_16x16x32_bf16 v[144:147], a[156:159], a[224:227], v[144:147]\n\t"
    "ds_read_b64_tr_b16 a[148:149], v236 offset:7328\n\t"
    "v_mfma_f32_16x16x32_bf16 v[144:147], a[160:163], a[240:243], v[144:147]\n\t"
    "v_mfma_f32_16x16x32_bf16 v[148:151], a[156:159], a[228:231], v[148:151]\n\t"
    "ds_read_b64_tr_b16 a[150:151], v236 offset:10912\n\t"
    "v_mfma_f32_16x16x32_bf16 v[148:151], a[160:163], a[244:247], v[148:151]\n\t"
    "buffer_load_dwordx4 v246, s[40:43], s57 offen offset:1024 lds\n\t"
    "v_mfma_f32_16x16x32_bf16 v[152:155], a[156:159], a[232:235], v[152:155]\n\t"
    "ds_read_b64_tr_b16 a[152:153], v236 offset:192\n\t"
    "v_mfma_f32_16x16x32_bf16 v[152:155], a[160:163], a[248:251], v[152:155]\n\t"
    "v_mfma_f32_16x16x32_bf16 v[156:159], a[156:159], a[236:239], v[156:159]\n\t"
    "ds_read_b64_tr_b16 a[154:155], v236 offset:3776\n\t"
    "v_mfma_f32_16x16x32_bf16 v[156:159], a[160:163], a[252:255], v[156:159]\n\t"
    "s_waitcnt lgkmcnt(12)\n\t"
    "v_mfma_f32_16x16x32_bf16 v[160:163], a[164:167], a[224:227], v[160:163]\n\t"
    "ds_read_b64_tr_b16 a[156:157], v236 offset:7360\n\t"
    "v_mfma_f32_16x16x32_bf16 v[160:163], a[168:171], a[240:243], v[160:163]\n\t"
    "v_mfma_f32_16x16x32_bf16 v[164:167], a[164:167], a[228:231], v[164:167]\n\t"
    "ds_read_b64_tr_b16 a[158:159], v236 offset:10944\n\t"
    "v_mfma_f32_16x16x32_bf16 v[164:167], a[168:171], a[244:247], v[164:167]\n\t"
    "buffer_load_dwordx4 v247, s[40:43], s57 offen offset:2048 lds\n\t"
    "v_mfma_f32_16x16x32_bf16 v[168:171], a[164:167], a[232:235], v[168:171]\n\t"
    "ds_read_b64_tr_b16 a[160:161], v236 offset:14336\n\t"
    "v_mfma_f32_16x16x32_bf16 v[168:171], a[168:171], a[248:251], v[168:171]\n\t"
    "v_mfma_f32_16x16x32_bf16 v[172:175], a[164:167], a[236:239], v[172:175]\n\t"
    "ds_read_b64_tr_b16 a[162:163], v236 offset:17920\n\t"
    "v_mfma_f32_16x16x32_bf16 v[172:175], a[168:171], a[252:255], v[172:175]\n\t"
    "s_waitcnt lgkmcnt(12)\n\t"
    "v_mfma_f32_16x16x32_bf16 v[176:179], a[128:131], a[224:227], v[176:179]\n\t"
    "ds_read_b64_tr_b16 a[164:165], v236 offset:21504\n\t"
    "v_mfma_f32_16x16x32_bf16 v[176:179], a[132:135], a[240:243], v[176:179]\n\t"
    "v_mfma_f32_16x16x32_bf16 v[180:183], a[128:131], a[228:231], v[180:183]\n\t"
    "ds_read_b64_tr_b16 a[166:167], v236 offset:25088\n\t"
    "v_mfma_f32_16x16x32_bf16 v[180:183], a[132:135], a[244:247], v[180:183]\n\t"
    "v_mfma_f32_16x16x32_bf16 v[184:187], a[128:131], a[232:235], v[184:187]\n\t"
    "ds_read_b64_tr_b16 a[168:169], v236 offset:14368\n\t"
    "v_mfma_f32_16x16x32_bf16 v[184:187], a[132:135], a[248:251], v[184:187]\n\t"
    "v_mfma_f32_16x16x32_bf16 v[188:191], a[128:131], a[236:239], v[188:191]\n\t"
    "ds_read_b64_tr_b16 a[170:171], v236 offset:17952\n\t"
    "v_mfma_f32_16x16x32_bf16 v[188:191], a[132:135], a[252:255], v[188:191]\n\t"
    "s_waitcnt lgkmcnt(12)\n\t"
    "v_mfma_f32_16x16x32_bf16 v[192:195], a[136:139], a[224:227], v[192:195]\n\t"
    "ds_read_b64_tr_b16 a[128:129], v236 offset:21536\n\t"
    "v_mfma_f32_16x16x32_bf16 v[192:195], a[140:143], a[240:243], v[192:195]\n\t"
    "v_mfma_f32_16x16x32_bf16 v[196:199], a[136:139], a[228:231], v[196:199]\n\t"
    "ds_read_b64_tr_b16 a[130:131], v236 offset:25120\n\t"
    "v_mfma_f32_16x16x32_bf16 v[196:199], a[140:143], a[244:247], v[196:199]\n\t"
    "s_add_u32 s65, s56, 1572864\n\t"
    "v_mfma_f32_16x16x32_bf16 v[200:203], a[136:139], a[232:235], v[200:203]\n\t"
    "ds_read_b64_tr_b16 a[132:133], v236 offset:14400\n\t"
    "v_mfma_f32_16x16x32_bf16 v[200:203], a[140:143], a[248:251], v[200:203]\n\t"
    "buffer_load_dword a190, v252, s[36:39], s65 offen\n\t"
    "v_mfma_f32_16x16x32_bf16 v[204:207], a[136:139], a[236:239], v[204:207]\n\t"
    "ds_read_b64_tr_b16 a[134:135], v236 offset:17984\n\t"
    "v_mfma_f32_16x16x32_bf16 v[204:207], a[140:143], a[252:255], v[204:207]\n\t"
    "s_waitcnt lgkmcnt(12)\n\t"
    "s_add_u32 s65, s57, 1376256\n\t"
    "v_mfma_f32_16x16x32_bf16 v[208:211], a[144:147], a[224:227], v[208:211]\n\t"
    "ds_read_b64_tr_b16 a[136:137], v236 offset:21568\n\t"
    "v_mfma_f32_16x16x32_bf16 v[208:211], a[148:151], a[240:243], v[208:211]\n\t"
    "buffer_load_dword a190, v253, s[40:43], s65 offen\n\t"
    "v_mfma_f32_16x16x32_bf16 v[212:215], a[144:147], a[228:231], v[212:215]\n\t"
    "ds_read_b64_tr_b16 a[138:139], v236 offset:25152\n\t"
    "v_mfma_f32_16x16x32_bf16 v[212:215], a[148:151], a[244:247], v[212:215]\n\t"
    "v_mfma_f32_16x16x32_bf16 v[216:219], a[144:147], a[232:235], v[216:219]\n\t"
    "ds_read_b64_tr_b16 a[140:141], v236 offset:14432\n\t"
    "v_mfma_f32_16x16x32_bf16 v[216:219], a[148:151], a[248:251], v[216:219]\n\t"
    "s_add_u32 s56, s56, 524288\n\t"
    "v_mfma_f32_16x16x32_bf16 v[220:223], a[144:147], a[236:239], v[220:223]\n\t"
    "ds_read_b64_tr_b16 a[142:143], v236 offset:18016\n\t"
    "v_mfma_f32_16x16x32_bf16 v[220:223], a[148:151], a[252:255], v[220:223]\n\t"
    "s_waitcnt lgkmcnt(12)\n\t"
    "v_mfma_f32_16x16x32_bf16 v[224:227], a[152:155], a[224:227], v[224:227]\n\t"
    "ds_read_b64_tr_b16 a[144:145], v236 offset:21600\n\t"
    "v_mfma_f32_16x16x32_bf16 v[224:227], a[156:159], a[240:243], v[224:227]\n\t"
    "s_add_u32 s57, s57, 458752\n\t"
    "v_mfma_f32_16x16x32_bf16 a[112:115], a[152:155], a[228:231], a[112:115]\n\t"
    "ds_read_b64_tr_b16 a[146:147], v236 offset:25184\n\t"
    "v_mfma_f32_16x16x32_bf16 a[112:115], a[156:159], a[244:247], a[112:115]\n\t"
    "v_mfma_f32_16x16x32_bf16 a[116:119], a[152:155], a[232:235], a[116:119]\n\t"
    "ds_read_b64_tr_b16 a[148:149], v236 offset:14464\n\t"
    "v_mfma_f32_16x16x32_bf16 a[116:119], a[156:159], a[248:251], a[116:119]\n\t"
    "buffer_store_dwordx4 a[124:127], v248, s[80:83], s67 offen sc1\n\t"
    "v_mfma_f32_16x16x32_bf16 a[120:123], a[152:155], a[236:239], a[120:123]\n\t"
    "ds_read_b64_tr_b16 a[150:151], v236 offset:18048\n\t"
    "v_mfma_f32_16x16x32_bf16 a[120:123], a[156:159], a[252:255], a[120:123]\n\t"
    "s_waitcnt lgkmcnt(12)\n\t"
    "v_mfma_f32_16x16x32_bf16 a[0:3], a[160:163], a[224:227], a[0:3]\n\t"
    "ds_read_b64_tr_b16 a[152:153], v236 offset:21632\n\t"
    "v_mfma_f32_16x16x32_bf16 a[0:3], a[164:167], a[240:243], a[0:3]\n\t"
    "v_mfma_f32_16x16x32_bf16 a[4:7], a[160:163], a[228:231], a[4:7]\n\t"
    "ds_read_b64_tr_b16 a[154:155], v236 offset:25216\n\t"
    "v_mfma_f32_16x16x32_bf16 a[4:7], a[164:167], a[244:247], a[4:7]\n\t"
    "v_mfma_f32_16x16x32_bf16 a[8:11], a[160:163], a[232:235], a[8:11]\n\t"
    "ds_read_b64_tr_b16 a[156:157], v236 offset:14496\n\t"
    "v_mfma_f32_16x16x32_bf16 a[8:11], a[164:167], a[248:251], a[8:11]\n\t"
    "buffer_store_dwordx4 a[124:127], v248, s[80:83], s67 offen offset:1024 sc1\n\t"
    "v_mfma_f32_16x16x32_bf16 a[12:15], a[160:163], a[236:239], a[12:15]\n\t"
    "ds_read_b64_tr_b16 a[158:159], v236 offset:18080\n\t"
    "s_add_u32 s67, s67, 2048\n\t"
    "v_mfma_f32_16x16x32_bf16 a[12:15], a[164:167], a[252:255], a[12:15]\n\t"
    "s_waitcnt lgkmcnt(12)\n\t"
    "s_mov_b32 s60, s59\n\t"
    "v_mfma_f32_16x16x32_bf16 a[16:19], a[168:171], a[224:227], a[16:19]\n\t"
    "ds_read_b64_tr_b16 a[160:161], v236 offset:21664\n\t"
    "v_mfma_f32_16x16x32_bf16 a[16:19], a[128:131], a[240:243], a[16:19]\n\t"
    "s_add_u32 s65, s59, 45056\n\t"
    "v_mfma_f32_16x16x32_bf16 a[20:23], a[168:171], a[228:231], a[20:23]\n\t"
    "ds_read_b64_tr_b16 a[162:163], v236 offset:25248\n\t"
    "v_mfma_f32_16x16x32_bf16 a[20:23], a[128:131], a[244:247], a[20:23]\n\t"
    "s_cmp_eq_u32 s59, 118784\n\t"
    "s_cselect_b32 s59, 28672, s65\n\t"
    "v_mfma_f32_16x16x32_bf16 a[24:27], a[168:171], a[232:235], a[24:27]\n\t"
    "ds_read_b64_tr_b16 a[164:165], v236 offset:14528\n\t"
    "v_mfma_f32_16x16x32_bf16 a[24:27], a[128:131], a[248:251], a[24:27]\n\t"
    "s_sub_u32 s61, s59, s60\n\t"
    "v_mfma_f32_16x16x32_bf16 a[28:31], a[168:171], a[236:239], a[28:31]\n\t"
    "ds_read_b64_tr_b16 a[166:167], v236 offset:18112\n\t"
    "v_mfma_f32_16x16x32_bf16 a[28:31], a[128:131], a[252:255], a[28:31]\n\t"
    "s_waitcnt lgkmcnt(12)\n\t"
    "v_mfma_f32_16x16x32_bf16 a[32:35], a[132:135], a[224:227], a[32:35]\n\t"
    "ds_read_b64_tr_b16 a[168:169], v236 offset:21696\n\t"
    "v_mfma_f32_16x16x32_bf16 a[32:35], a[136:139], a[240:243], a[32:35]\n\t"
    "v_mfma_f32_16x16x32_bf16 a[36:39], a[132:135], a[228:231], a[36:39]\n\t"
    "ds_read_b64_tr_b16 a[170:171], v236 offset:25280\n\t"
    "v_mfma_f32_16x16x32_bf16 a[36:39], a[136:139], a[244:247], a[36:39]\n\t"
    "v_add_u32 v228, s61, v228\n\t"
    "v_add_u32 v229, s61, v229\n\t"
    "v_mfma_f32_16x16x32_bf16 a[40:43], a[132:135], a[232:235], a[40:43]\n\t"
    "v_add_u32 v230, s61, v230\n\t"
    "v_mfma_f32_16x16x32_bf16 a[40:43], a[136:139], a[248:251], a[40:43]\n\t"
    "v_add_u32 v231, s61, v231\n\t"
    "v_add_u32 v232, s61, v232\n\t"
    "v_mfma_f32_16x16x32_bf16 a[44:47], a[132:135], a[236:239], a[44:47]\n\t"
    "v_add_u32 v233, s61, v233\n\t"
    "v_mfma_f32_16x16x32_bf16 a[44:47], a[136:139], a[252:255], a[44:47]\n\t"
    "s_waitcnt lgkmcnt(12)\n\t"
    "v_add_u32 v234, s61, v234\n\t"
    "v_add_u32 v235, s61, v235\n\t"
    "v_mfma_f32_16x16x32_bf16 a[48:51], a[140:143], a[224:227], a[48:51]\n\t"
    "v_add_u32 v236, s61, v236\n\t"
    "v_mfma_f32_16x16x32_bf16 a[48:51], a[144:147], a[240:243], a[48:51]\n\t"
    "v_mfma_f32_16x16x32_bf16 a[52:55], a[140:143], a[228:231], a[52:55]\n\t"
    "s_waitcnt vmcnt(27) lgkmcnt(0)\n\t"
    "s_barrier\n\t"
    "v_mfma_f32_16x16x32_bf16 a[52:55], a[144:147], a[244:247], a[52:55]\n\t"
    "ds_read_b64_tr_b16 a[128:129], v228 offset:0\n\t"
    "v_mfma_f32_16x16x32_bf16 a[56:59], a[140:143], a[232:235], a[56:59]\n\t"
    "ds_read_b64_tr_b16 a[130:131], v228 offset:4096\n\t"
    "v_mfma_f32_16x16x32_bf16 a[56:59], a[144:147], a[248:251], a[56:59]\n\t"
    "ds_read_b64_tr_b16 a[132:133], v228 offset:8192\n\t"
    "v_mfma_f32_16x16x32_bf16 a[60:63], a[140:143], a[236:239], a[60:63]\n\t"
    "ds_read_b64_tr_b16 a[134:135], v228 offset:12288\n\t"
    "v_mfma_f32_16x16x32_bf16 a[60:63], a[144:147], a[252:255], a[60:63]\n\t"
    "ds_read_b64_tr_b16 a[136:137], v229 offset:0\n\t"
    "s_waitcnt lgkmcnt(12)\n\t"
    "v_mfma_f32_16x16x32_bf16 a[64:67], a[148:151], a[224:227], a[64:67]\n\t"
    "ds_read_b64_tr_b16 a[138:139], v229 offset:4096\n\t"
    "ds_read_b64_tr_b16 a[140:141], v229 offset:8192\n\t"
    "v_mfma_f32_16x16x32_bf16 a[64:67], a[152:155], a[240:243], a[64:67]\n\t"
    "v_mfma_f32_16x16x32_bf16 a[68:71], a[148:151], a[228:231], a[68:71]\n\t"
    "ds_read_b64_tr_b16 a[142:143], v229 offset:12288\n\t"
    "v_mfma_f32_16x16x32_bf16 a[68:71], a[152:155], a[244:247], a[68:71]\n\t"
    "v_mfma_f32_16x16x32_bf16 a[72:75], a[148:151], a[232:235], a[72:75]\n\t"
    "ds_read_b64_tr_b16 a[144:145], v230 offset:0\n\t"
    "v_mfma_f32_16x16x32_bf16 a[72:75], a[152:155], a[248:251], a[72:75]\n\t"
    "v_mfma_f32_16x16x32_bf16 a[76:79], a[148:151], a[236:239], a[76:79]\n\t"
    "ds_read_b64_tr_b16 a[146:147], v230 offset:4096\n\t"
    "v_mfma_f32_16x16x32_bf16 a[76:79], a[152:155], a[252:255], a[76:79]\n\t"
    "s_waitcnt lgkmcnt(12)\n\t"
    "v_mfma_f32_16x16x32_bf16 a[80:83], a[156:159], a[224:227], a[80:83]\n\t"
    "ds_read_b64_tr_b16 a[148:149], v230 offset:8192\n\t"
    "v_mfma_f32_16x16x32_bf16 a[80:83], a[160:163], a[240:243], a[80:83]\n\t"
    "v_mfma_f32_16x16x32_bf16 a[84:87], a[156:159], a[228:231], a[84:87]\n\t"
    "ds_read_b64_tr_b16 a[150:151], v230 offset:12288\n\t"
    "v_mfma_f32_16x16x32_bf16 a[84:87], a[160:163], a[244:247], a[84:87]\n\t"
    "v_mfma_f32_16x16x32_bf16 a[88:91], a[156:159], a[232:235], a[88:91]\n\t"
    "ds_read_b64_tr_b16 a[152:153], v231 offset:0\n\t"
    "v_mfma_f32_16x16x32_bf16 a[88:91], a[160:163], a[248:251], a[88:91]\n\t"
    "v_mfma_f32_16x16x32_bf16 a[92:95], a[156:159], a[236:239], a[92:95]\n\t"
    "ds_read_b64_tr_b16 a[154:155], v231 offset:4096\n\t"
    "v_mfma_f32_16x16x32_bf16 a[92:95], a[160:163], a[252:255], a[92:95]\n\t"
    "s_waitcnt lgkmcnt(12)\n\t"
    "v_mfma_f32_16x16x32_bf16 a[96:99], a[164:167], a[224:227], a[96:99]\n\t"
    "ds_read_b64_tr_b16 a[156:157], v231 offset:8192\n\t"
    "v_mfma_f32_16x16x32_bf16 a[96:99], a[168:171], a[240:243], a[96:99]\n\t"
    "v_mfma_f32_16x16x32_bf16 a[100:103], a[164:167], a[228:231], a[100:103]\n\t"
    "ds_read_b64_tr_b16 a[158:159], v231 offset:12288\n\t"
    "v_mfma_f32_16x16x32_bf16 a[100:103], a[168:171], a[244:247], a[100:103]\n\t"
    "v_mfma_f32_16x16x32_bf16 a[104:107], a[164:167], a[232:235], a[104:107]\n\t"
    "ds_read_b64_tr_b16 a[160:161], v232 offset:0\n\t"
    "v_mfma_f32_16x16x32_bf16 a[104:107], a[168:171], a[248:251], a[104:107]\n\t"
    "v_mfma_f32_16x16x32_bf16 a[108:111], a[164:167], a[236:239], a[108:111]\n\t"
    "ds_read_b64_tr_b16 a[162:163], v232 offset:4096\n\t"
    "v_mfma_f32_16x16x32_bf16 a[108:111], a[168:171], a[252:255], a[108:111]\n\t"
    "s_waitcnt lgkmcnt(12) vmcnt(6)\n\t"
    "s_add_u32 s64, s64, 1\n\t"
    "s_cbranch_scc0 L_g1_loop%=\n\t"
    "v_mfma_f32_16x16x32_bf16 v[0:3], a[128:131], a[192:195], v[0:3]\n\t"
    "ds_read_b64_tr_b16 a[164:165], v232 offset:8192\n\t"
    "v_mfma_f32_16x16x32_bf16 v[0:3], a[132:135], a[208:211], v[0:3]\n\t"
    "buffer_load_dwordx4 a[224:227], v248, s[44:47], s58 offen offset:2048 sc1\n\t"
    "v_mfma_f32_16x16x32_bf16 v[32:35], a[128:131], a[196:199], v[32:35]\n\t"
    "ds_read_b64_tr_b16 a[166:167], v232 offset:12288\n\t"
    "v_mfma_f32_16x16x32_bf16 v[32:35], a[132:135], a[212:215], v[32:35]\n\t"
    "buffer_load_dwordx4 a[228:231], v249, s[44:47], s58 offen offset:2048 sc1\n\t"
    "v_mfma_f32_16x16x32_bf16 v[64:67], a[128:131], a[200:203], v[64:67]\n\t"
    "ds_read_b64_tr_b16 a[168:169], v233 offset:0\n\t"
    "v_mfma_f32_16x16x32_bf16 v[64:67], a[132:135], a[216:219], v[64:67]\n\t"
    "buffer_load_dwordx4 a[232:235], v250, s[44:47], s58 offen offset:2048 sc1\n\t"
    "v_mfma_f32_16x16x32_bf16 v[96:99], a[128:131], a[204:207], v[96:99]\n\t"
    "ds_read_b64_tr_b16 a[170:171], v233 offset:4096\n\t"
    "v_mfma_f32_16x16x32_bf16 v[96:99], a[132:135], a[220:223], v[96:99]\n\t"
    "buffer_load_dwordx4 a[236:239], v251, s[44:47], s58 offen offset:2048 sc1\n\t"
    "s_waitcnt lgkmcnt(12)\n\t"
    "v_mfma_f32_16x16x32_bf16 v[4:7], a[136:139], a[192:195], v[4:7]\n\t"
    "ds_read_b64_tr_b16 a[128:129], v233 offset:8192\n\t"
    "v_mfma_f32_16x16x32_bf16 v[4:7], a[140:143], a[208:211], v[4:7]\n\t"
    "buffer_load_dwordx4 a[240:243], v248, s[44:47], s58 offen offset:3072 sc1\n\t"
    "v_mfma_f32_16x16x32_bf16 v[36:39], a[136:139], a[196:199], v[36:39]\n\t"
    "ds_read_b64_tr_b16 a[130:131], v233 offset:12288\n\t"
    "v_mfma_f32_16x16x32_bf16 v[36:39], a[140:143], a[212:215], v[36:39]\n\t"
    "buffer_load_dwordx4 a[244:247], v249, s[44:47], s58 offen offset:3072 sc1\n\t"
    "v_mfma_f32_16x16x32_bf16 v[68:71], a[136:139], a[200:203], v[68:71]\n\t"
    "ds_read_b64_tr_b16 a[132:133], v234 offset:0\n\t"
    "v_mfma_f32_16x16x32_bf16 v[68:71], a[140:143], a[216:219], v[68:71]\n\t"
    "buffer_load_dwordx4 a[248:251], v250, s[44:47], s58 offen offset:3072 sc1\n\t"
    "v_mfma_f32_16x16x32_bf16 v[100:103], a[136:139], a[204:207], v[100:103]\n\t"
    "ds_read_b64_tr_b16 a[134:135], v234 offset:4096\n\t"
    "v_mfma_f32_16x16x32_bf16 v[100:103], a[140:143], a[220:223], v[100:103]\n\t"
    "buffer_load_dwordx4 a[252:255], v251, s[44:47], s58 offen offset:3072 sc1\n\t"
    "s_waitcnt lgkmcnt(12)\n\t"
    "v_add_u32 v241, 45056, v236\n\t"
    "v_mfma_f32_16x16x32_bf16 v[8:11], a[144:147], a[192:195], v[8:11]\n\t"
    "ds_read_b64_tr_b16 a[136:137], v234 offset:8192\n\t"
    "v_accvgpr_read_b32 v242, a172\n\t"
    "v_mfma_f32_16x16x32_bf16 v[8:11], a[148:151], a[208:211], v[8:11]\n\t"
    "v_accvgpr_read_b32 v243, a173\n\t"
    "v_accvgpr_read_b32 v244, a174\n\t"
    "v_mfma_f32_16x16x32_bf16 v[40:43], a[144:147], a[196:199], v[40:43]\n\t"
    "ds_read_b64_tr_b16 a[138:139], v234 offset:12288\n\t"
    "v_accvgpr_read_b32 v245, a175\n\t"
    "v_mfma_f32_16x16x32_bf16 v[40:43], a[148:151], a[212:215], v[40:43]\n\t"
    "v_accvgpr_read_b32 v246, a176\n\t"
    "v_accvgpr_read_b32 v247, a177\n\t"
    "v_mfma_f32_16x16x32_bf16 v[72:75], a[144:147], a[200:203], v[72:75]\n\t"
    "ds_read_b64_tr_b16 a[140:141], v235 offset:0\n\t"
    "v_accvgpr_read_b32 v237, a178\n\t"
    "v_mfma_f32_16x16x32_bf16 v[72:75], a[148:151], a[216:219], v[72:75]\n\t"
    "v_accvgpr_read_b32 v238, a179\n\t"
    "v_accvgpr_read_b32 v239, a180\n\t"
    "v_mfma_f32_16x16x32_bf16 v[104:107], a[144:147], a[204:207], v[104:107]\n\t"
    "ds_read_b64_tr_b16 a[142:143], v235 offset:4096\n\t"
    "v_accvgpr_read_b32 v240, a181\n\t"
    "v_mfma_f32_16x16x32_bf16 v[104:107], a[148:151], a[220:223], v[104:107]\n\t"
    "s_waitcnt lgkmcnt(12)\n\t"
    "v_accvgpr_read_b32 v252, a182\n\t"
    "v_accvgpr_read_b32 v253, a183\n\t"
    "v_mfma_f32_16x16x32_bf16 v[12:15], a[152:155], a[192:195], v[12:15]\n\t"
    "ds_read_b64_tr_b16 a[144:145], v235 offset:8192\n\t"
    "v_mfma_f32_16x16x32_bf16 v[12:15], a[156:159], a[208:211], v[12:15]\n\t"
    "v_mfma_f32_16x16x32_bf16 v[44:47], a[152:155], a[196:199], v[44:47]\n\t"
    "ds_read_b64_tr_b16 a[146:147], v235 offset:12288\n\t"
    "v_mfma_f32_16x16x32_bf16 v[44:47], a[156:159], a[212:215], v[44:47]\n\t"
    "v_mfma_f32_16x16x32_bf16 v[76:79], a[152:155], a[200:203], v[76:79]\n\t"
    "ds_read_b64_tr_b16 a[148:149], v236 offset:0\n\t"
    "v_mfma_f32_16x16x32_bf16 v[76:79], a[156:159], a[216:219], v[76:79]\n\t"
    "v_mfma_f32_16x16x32_bf16 v[108:111], a[152:155], a[204:207], v[108:111]\n\t"
    "ds_read_b64_tr_b16 a[150:151], v236 offset:3584\n\t"
    "v_mfma_f32_16x16x32_bf16 v[108:111], a[156:159], a[220:223], v[108:111]\n\t"
    "s_waitcnt lgkmcnt(12)\n\t"
    "v_mfma_f32_16x16x32_bf16 v[16:19], a[160:163], a[192:195], v[16:19]\n\t"
    "ds_read_b64_tr_b16 a[152:153], v236 offset:7168\n\t"
    "s_add_u32 m0, s62, 0\n\t"
    "v_mfma_f32_16x16x32_bf16 v[16:19], a[164:167], a[208:211], v[16:19]\n\t"
    "buffer_load_dwordx4 v242, s[48:51], s66 offen offset:0 lds\n\t"
    "v_mfma_f32_16x16x32_bf16 v[48:51], a[160:163], a[196:199], v[48:51]\n\t"
    "ds_read_b64_tr_b16 a[154:155], v236 offset:10752\n\t"
    "v_mfma_f32_16x16x32_bf16 v[48:51], a[164:167], a[212:215], v[48:51]\n\t"
    "v_mfma_f32_16x16x32_bf16 v[80:83], a[160:163], a[200:203], v[80:83]\n\t"
    "ds_read_b64_tr_b16 a[156:157], v236 offset:14336\n\t"
    "v_mfma_f32_16x16x32_bf16 v[80:83], a[164:167], a[216:219], v[80:83]\n\t"
    "v_mfma_f32_16x16x32_bf16 v[112:115], a[160:163], a[204:207], v[112:115]\n\t"
    "ds_read_b64_tr_b16 a[158:159], v236 offset:17920\n\t"
    "v_mfma_f32_16x16x32_bf16 v[112:115], a[164:167], a[220:223], v[112:115]\n\t"
    "buffer_load_dwordx4 v243, s[48:51], s66 offen offset:1024 lds\n\t"
    "s_waitcnt lgkmcnt(12)\n\t"
    "v_mfma_f32_16x16x32_bf16 v[20:23], a[168:171], a[192:195], v[20:23]\n\t"
    "ds_read_b64_tr_b16 a[160:161], v236 offset:21504\n\t"
    "v_mfma_f32_16x16x32_bf16 v[20:23], a[128:131], a[208:211], v[20:23]\n\t"
    "v_mfma_f32_16x16x32_bf16 v[52:55], a[168:171], a[196:199], v[52:55]\n\t"
    "ds_read_b64_tr_b16 a[162:163], v236 offset:25088\n\t"
    "v_mfma_f32_16x16x32_bf16 v[52:55], a[128:131], a[212:215], v[52:55]\n\t"
    "v_mfma_f32_16x16x32_bf16 v[84:87], a[168:171], a[200:203], v[84:87]\n\t"
    "ds_read_b64_tr_b16 a[164:165], v236 offset:32\n\t"
    "v_mfma_f32_16x16x32_bf16 v[84:87], a[128:131], a[216:219], v[84:87]\n\t"
    "buffer_load_dwordx4 v244, s[48:51], s66 offen offset:2048 lds\n\t"
    "v_mfma_f32_16x16x32_bf16 v[116:119], a[168:171], a[204:207], v[116:119]\n\t"
    "ds_read_b64_tr_b16 a[166:167], v236 offset:3616\n\t"
    "v_mfma_f32_16x16x32_bf16 v[116:119], a[128:131], a[220:223], v[116:119]\n\t"
    "s_waitcnt lgkmcnt(12)\n\t"
    "v_mfma_f32_16x16x32_bf16 v[24:27], a[132:135], a[192:195], v[24:27]\n\t"
    "ds_read_b64_tr_b16 a[168:169], v236 offset:7200\n\t"
    "v_mfma_f32_16x16x32_bf16 v[24:27], a[136:139], a[208:211], v[24:27]\n\t"
    "v_mfma_f32_16x16x32_bf16 v[56:59], a[132:135], a[196:199], v[56:59]\n\t"
    "ds_read_b64_tr_b16 a[170:171], v236 offset:10784\n\t"
    "v_mfma_f32_16x16x32_bf16 v[56:59], a[136:139], a[212:215], v[56:59]\n\t"
    "buffer_load_dwordx4 v245, s[48:51], s66 offen offset:3072 lds\n\t"
    "v_mfma_f32_16x16x32_bf16 v[88:91], a[132:135], a[200:203], v[88:91]\n\t"
    "ds_read_b64_tr_b16 a[128:129], v236 offset:14368\n\t"
    "v_mfma_f32_16x16x32_bf16 v[88:91], a[136:139], a[216:219], v[88:91]\n\t"
    "v_mfma_f32_16x16x32_bf16 v[120:123], a[132:135], a[204:207], v[120:123]\n\t"
    "ds_read_b64_tr_b16 a[130:131], v236 offset:17952\n\t"
    "v_mfma_f32_16x16x32_bf16 v[120:123], a[136:139], a[220:223], v[120:123]\n\t"
    "s_waitcnt lgkmcnt(12)\n\t"
    "v_mfma_f32_16x16x32_bf16 v[28:31], a[140:143], a[192:195], v[28:31]\n\t"
    "ds_read_b64_tr_b16 a[132:133], v236 offset:21536\n\t"
    "s_add_u32 m0, m0, 16384\n\t"
    "v_mfma_f32_16x16x32_bf16 v[28:31], a[144:147], a[208:211], v[28:31]\n\t"
    "buffer_load_dwordx4 v246, s[52:55], s66 offen offset:0 lds\n\t"
    "v_mfma_f32_16x16x32_bf16 v[60:63], a[140:143], a[196:199], v[60:63]\n\t"
    "ds_read_b64_tr_b16 a[134:135], v236 offset:25120\n\t"
    "v_mfma_f32_16x16x32_bf16 v[60:63], a[144:147], a[212:215], v[60:63]\n\t"
    "s_waitcnt vmcnt(17)\n\t"
    "s_barrier\n\t"
    "v_mfma_f32_16x16x32_bf16 v[92:95], a[140:143], a[200:203], v[92:95]\n\t"
    "ds_read_b64_tr_b16 a[136:137], v228 offset:45056\n\t"
    "v_mfma_f32_16x16x32_bf16 v[92:95], a[144:147], a[216:219], v[92:95]\n\t"
    "v_mfma_f32_16x16x32_bf16 v[124:127], a[140:143], a[204:207], v[124:127]\n\t"
    "ds_read_b64_tr_b16 a[138:139], v228 offset:49152\n\t"
    "v_mfma_f32_16x16x32_bf16 v[124:127], a[144:147], a[220:223], v[124:127]\n\t"
    "buffer_load_dwordx4 v247, s[52:55], s66 offen offset:1024 lds\n\t"
    "s_waitcnt lgkmcnt(12)\n\t"
    "v_mfma_f32_16x16x32_bf16 v[128:131], a[148:151], a[192:195], v[128:131]\n\t"
    "ds_read_b64_tr_b16 a[140:141], v228 offset:53248\n\t"
    "v_mfma_f32_16x16x32_bf16 v[128:131], a[152:155], a[208:211], v[128:131]\n\t"
    "v_mfma_f32_16x16x32_bf16 v[132:135], a[148:151], a[196:199], v[132:135]\n\t"
    "ds_read_b64_tr_b16 a[142:143], v228 offset:57344\n\t"
    "v_mfma_f32_16x16x32_bf16 v[132:135], a[152:155], a[212:215], v[132:135]\n\t"
    "v_mfma_f32_16x16x32_bf16 v[136:139], a[148:151], a[200:203], v[136:139]\n\t"
    "ds_read_b64_tr_b16 a[144:145], v229 offset:45056\n\t"
    "v_mfma_f32_16x16x32_bf16 v[136:139], a[152:155], a[216:219], v[136:139]\n\t"
    "buffer_load_dwordx4 v246, s[52:55], s66 offen offset:2048 lds\n\t"
    "v_mfma_f32_16x16x32_bf16 v[140:143], a[148:151], a[204:207], v[140:143]\n\t"
    "ds_read_b64_tr_b16 a[146:147], v229 offset:49152\n\t"
    "v_mfma_f32_16x16x32_bf16 v[140:143], a[152:155], a[220:223], v[140:143]\n\t"
    "s_waitcnt lgkmcnt(12)\n\t"
    "v_mfma_f32_16x16x32_bf16 a[0:3], a[156:159], a[192:195], a[0:3]\n\t"
    "ds_read_b64_tr_b16 a[148:149], v229 offset:53248\n\t"
    "v_mfma_f32_16x16x32_bf16 a[0:3], a[160:163], a[208:211], a[0:3]\n\t"
    "v_mfma_f32_16x16x32_bf16 a[4:7], a[156:159], a[196:199], a[4:7]\n\t"
    "ds_read_b64_tr_b16 a[150:151], v229 offset:57344\n\t"
    "v_mfma_f32_16x16x32_bf16 a[4:7], a[160:163], a[212:215], a[4:7]\n\t"
    "buffer_load_dwordx4 v247, s[52:55], s66 offen offset:3072 lds\n\t"
    "v_mfma_f32_16x16x32_bf16 a[8:11], a[156:159], a[200:203], a[8:11]\n\t"
    "ds_read_b64_tr_b16 a[152:153], v230 offset:45056\n\t"
    "v_mfma_f32_16x16x32_bf16 a[8:11], a[160:163], a[216:219], a[8:11]\n\t"
    "v_mfma_f32_16x16x32_bf16 a[12:15], a[156:159], a[204:207], a[12:15]\n\t"
    "ds_read_b64_tr_b16 a[154:155], v230 offset:49152\n\t"
    "v_mfma_f32_16x16x32_bf16 a[12:15], a[160:163], a[220:223], a[12:15]\n\t"
    "s_waitcnt lgkmcnt(12)\n\t"
    "v_mfma_f32_16x16x32_bf16 v[144:147], a[164:167], a[192:195], v[144:147]\n\t"
    "ds_read_b64_tr_b16 a[156:157], v230 offset:53248\n\t"
    "s_add_u32 s66, s66, 16384\n\t"
    "s_add_u32 m0, s62, 32768\n\t"
    "v_mfma_f32_16x16x32_bf16 v[144:147], a[168:171], a[208:211], v[144:147]\n\t"
    "buffer_load_dwordx4 v242, s[48:51], s66 offen offset:0 lds\n\t"
    "v_mfma_f32_16x16x32_bf16 v[148:151], a[164:167], a[196:199], v[148:151]\n\t"
    "ds_read_b64_tr_b16 a[158:159], v230 offset:57344\n\t"
    "v_mfma_f32_16x16x32_bf16 v[148:151], a[168:171], a[212:215], v[148:151]\n\t"
    "v_mfma_f32_16x16x32_bf16 v[152:155], a[164:167], a[200:203], v[152:155]\n\t"
    "ds_read_b64_tr_b16 a[160:161], v231 offset:45056\n\t"
    "v_mfma_f32_16x16x32_bf16 v[152:155], a[168:171], a[216:219], v[152:155]\n\t"
    "v_mfma_f32_16x16x32_bf16 v[156:159], a[164:167], a[204:207], v[156:159]\n\t"
    "ds_read_b64_tr_b16 a[162:163], v231 offset:49152\n\t"
    "v_mfma_f32_16x16x32_bf16 v[156:159], a[168:171], a[220:223], v[156:159]\n\t"
    "buffer_load_dwordx4 v243, s[48:51], s66 offen offset:1024 lds\n\t"
    "s_waitcnt lgkmcnt(12)\n\t"
    "v_mfma_f32_16x16x32_bf16 a[16:19], a[128:131], a[192:195], a[16:19]\n\t"
    "ds_read_b64_tr_b16 a[164:165], v231 offset:53248\n\t"
    "v_mfma_f32_16x16x32_bf16 a[16:19], a[132:135], a[208:211], a[16:19]\n\t"
    "v_mfma_f32_16x16x32_bf16 a[20:23], a[128:131], a[196:199], a[20:23]\n\t"
    "ds_read_b64_tr_b16 a[166:167], v231 offset:57344\n\t"
    "v_mfma_f32_16x16x32_bf16 a[20:23], a[132:135], a[212:215], a[20:23]\n\t"
    "v_mfma_f32_16x16x32_bf16 a[24:27], a[128:131], a[200:203], a[24:27]\n\t"
    "ds_read_b64_tr_b16 a[168:169], v232 offset:45056\n\t"
    "v_mfma_f32_16x16x32_bf16 a[24:27], a[132:135], a[216:219], a[24:27]\n\t"
    "buffer_load_dwordx4 v244, s[48:51], s66 offen offset:2048 lds\n\t"
    "v_mfma_f32_16x16x32_bf16 a[28:31], a[128:131], a[204:207], a[28:31]\n\t"
    "ds_read_b64_tr_b16 a[170:171], v232 offset:49152\n\t"
    "v_mfma_f32_16x16x32_bf16 a[28:31], a[132:135], a[220:223], a[28:31]\n\t"
    "s_waitcnt vmcnt(11) lgkmcnt(12)\n\t"
    "v_mfma_f32_16x16x32_bf16 v[0:3], a[136:139], a[224:227], v[0:3]\n\t"
    "ds_read_b64_tr_b16 a[128:129], v232 offset:53248\n\t"
    "v_mfma_f32_16x16x32_bf16 v[0:3], a[140:143], a[240:243], v[0:3]\n\t"
    "v_mfma_f32_16x16x32_bf16 v[32:35], a[136:139], a[228:231], v[32:35]\n\t"
    "ds_read_b64_tr_b16 a[130:131], v232 offset:57344\n\t"
    "v_mfma_f32_16x16x32_bf16 v[32:35], a[140:143], a[244:247], v[32:35]\n\t"
    "buffer_load_dwordx4 v245, s[48:51], s66 offen offset:3072 lds\n\t"
    "v_mfma_f32_16x16x32_bf16 v[64:67], a[136:139], a[232:235], v[64:67]\n\t"
    "ds_read_b64_tr_b16 a[132:133], v233 offset:45056\n\t"
    "v_mfma_f32_16x16x32_bf16 v[64:67], a[140:143], a[248:251], v[64:67]\n\t"
    "v_mfma_f32_16x16x32_bf16 v[96:99], a[136:139], a[236:239], v[96:99]\n\t"
    "ds_read_b64_tr_b16 a[134:135], v233 offset:49152\n\t"
    "v_mfma_f32_16x16x32_bf16 v[96:99], a[140:143], a[252:255], v[96:99]\n\t"
    "s_waitcnt lgkmcnt(12)\n\t"
    "v_mfma_f32_16x16x32_bf16 v[4:7], a[144:147], a[224:227], v[4:7]\n\t"
    "ds_read_b64_tr_b16 a[136:137], v233 offset:53248\n\t"
    "s_add_u32 m0, m0, 16384\n\t"
    "v_mfma_f32_16x16x32_bf16 v[4:7], a[148:151], a[240:243], v[4:7]\n\t"
    "buffer_load_dwordx4 v246, s[52:55], s66 offen offset:0 lds\n\t"
    "v_mfma_f32_16x16x32_bf16 v[36:39], a[144:147], a[228:231], v[36:39]\n\t"
    "ds_read_b64_tr_b16 a[138:139], v233 offset:57344\n\t"
    "v_mfma_f32_16x16x32_bf16 v[36:39], a[148:151], a[244:247], v[36:39]\n\t"
    "v_mfma_f32_16x16x32_bf16 v[68:71], a[144:147], a[232:235], v[68:71]\n\t"
    "ds_read_b64_tr_b16 a[140:141], v234 offset:45056\n\t"
    "v_mfma_f32_16x16x32_bf16 v[68:71], a[148:151], a[248:251], v[68:71]\n\t"
    "v_mfma_f32_16x16x32_bf16 v[100:103], a[144:147], a[236:239], v[100:103]\n\t"
    "ds_read_b64_tr_b16 a[142:143], v234 offset:49152\n\t"
    "v_mfma_f32_16x16x32_bf16 v[100:103], a[148:151], a[252:255], v[100:103]\n\t"
    "buffer_load_dwordx4 v247, s[52:55], s66 offen offset:1024 lds\n\t"
    "s_waitcnt lgkmcnt(12)\n\t"
    "v_mfma_f32_16x16x32_bf16 v[8:11], a[152:155], a[224:227], v[8:11]\n\t"
    "ds_read_b64_tr_b16 a[144:145], v234 offset:53248\n\t"
    "v_mfma_f32_16x16x32_bf16 v[8:11], a[156:159], a[240:243], v[8:11]\n\t"
    "v_mfma_f32_16x16x32_bf16 v[40:43], a[152:155], a[228:231], v[40:43]\n\t"
    "ds_read_b64_tr_b16 a[146:147], v234 offset:57344\n\t"
    "v_mfma_f32_16x16x32_bf16 v[40:43], a[156:159], a[244:247], v[40:43]\n\t"
    "v_mfma_f32_16x16x32_bf16 v[72:75], a[152:155], a[232:235], v[72:75]\n\t"
    "ds_read_b64_tr_b16 a[148:149], v235 offset:45056\n\t"
    "v_mfma_f32_16x16x32_bf16 v[72:75], a[156:159], a[248:251], v[72:75]\n\t"
    "buffer_load_dwordx4 v246, s[52:55], s66 offen offset:2048 lds\n\t"
    "v_mfma_f32_16x16x32_bf16 v[104:107], a[152:155], a[236:239], v[104:107]\n\t"
    "ds_read_b64_tr_b16 a[150:151], v235 offset:49152\n\t"
    "v_mfma_f32_16x16x32_bf16 v[104:107], a[156:159], a[252:255], v[104:107]\n\t"
    "s_waitcnt lgkmcnt(12)\n\t"
    "v_mfma_f32_16x16x32_bf16 v[12:15], a[160:163], a[224:227], v[12:15]\n\t"
    "ds_read_b64_tr_b16 a[152:153], v235 offset:53248\n\t"
    "v_mfma_f32_16x16x32_bf16 v[12:15], a[164:167], a[240:243], v[12:15]\n\t"
    "v_mfma_f32_16x16x32_bf16 v[44:47], a[160:163], a[228:231], v[44:47]\n\t"
    "ds_read_b64_tr_b16 a[154:155], v235 offset:57344\n\t"
    "v_mfma_f32_16x16x32_bf16 v[44:47], a[164:167], a[244:247], v[44:47]\n\t"
    "buffer_load_dwordx4 v247, s[52:55], s66 offen offset:3072 lds\n\t"
    "v_mfma_f32_16x16x32_bf16 v[76:79], a[160:163], a[232:235], v[76:79]\n\t"
    "ds_read_b64_tr_b16 a[156:157], v241 offset:0\n\t"
    "v_mfma_f32_16x16x32_bf16 v[76:79], a[164:167], a[248:251], v[76:79]\n\t"
    "v_mfma_f32_16x16x32_bf16 v[108:111], a[160:163], a[236:239], v[108:111]\n\t"
    "ds_read_b64_tr_b16 a[158:159], v241 offset:3584\n\t"
    "v_mfma_f32_16x16x32_bf16 v[108:111], a[164:167], a[252:255], v[108:111]\n\t"
    "s_waitcnt lgkmcnt(12)\n\t"
    "v_mfma_f32_16x16x32_bf16 v[16:19], a[168:171], a[224:227], v[16:19]\n\t"
    "ds_read_b64_tr_b16 a[160:161], v241 offset:7168\n\t"
    "s_add_u32 s66, s66, 16384\n\t"
    "v_add_u32 v228, 4294909952, v228\n\t"
    "v_mfma_f32_16x16x32_bf16 v[16:19], a[128:131], a[240:243], v[16:19]\n\t"
    "v_add_u32 v229, 4294909952, v229\n\t"
    "v_add_u32 v230, 4294909952, v230\n\t"
    "v_mfma_f32_16x16x32_bf16 v[48:51], a[168:171], a[228:231], v[48:51]\n\t"
    "ds_read_b64_tr_b16 a[162:163], v241 offset:10752\n\t"
    "v_add_u32 v231, 4294909952, v231\n\t"
    "v_mfma_f32_16x16x32_bf16 v[48:51], a[128:131], a[244:247], v[48:51]\n\t"
    "v_add_u32 v232, 4294909952, v232\n\t"
    "v_add_u32 v233, 4294909952, v233\n\t"
    "v_mfma_f32_16x16x32_bf16 v[80:83], a[168:171], a[232:235], v[80:83]\n\t"
    "ds_read_b64_tr_b16 a[164:165], v241 offset:14336\n\t"
    "v_add_u32 v234, 4294909952, v234\n\t"
    "v_mfma_f32_16x16x32_bf16 v[80:83], a[128:131], a[248:251], v[80:83]\n\t"
    "v_add_u32 v235, 4294909952, v235\n\t"
    "v_mfma_f32_16x16x32_bf16 v[112:115], a[168:171], a[236:239], v[112:115]\n\t"
    "ds_read_b64_tr_b16 a[166:167], v241 offset:17920\n\t"
    "v_mfma_f32_16x16x32_bf16 v[112:115], a[128:131], a[252:255], v[112:115]\n\t"
    "s_waitcnt lgkmcnt(12)\n\t"
    "v_mfma_f32_16x16x32_bf16 v[20:23], a[132:135], a[224:227], v[20:23]\n\t"
    "ds_read_b64_tr_b16 a[168:169], v241 offset:21504\n\t"
    "v_mfma_f32_16x16x32_bf16 v[20:23], a[136:139], a[240:243], v[20:23]\n\t"
    "v_mfma_f32_16x16x32_bf16 v[52:55], a[132:135], a[228:231], v[52:55]\n\t"
    "ds_read_b64_tr_b16 a[170:171], v241 offset:25088\n\t"
    "v_mfma_f32_16x16x32_bf16 v[52:55], a[136:139], a[244:247], v[52:55]\n\t"
    "v_mfma_f32_16x16x32_bf16 v[84:87], a[132:135], a[232:235], v[84:87]\n\t"
    "ds_read_b64_tr_b16 a[128:129], v241 offset:32\n\t"
    "v_mfma_f32_16x16x32_bf16 v[84:87], a[136:139], a[248:251], v[84:87]\n\t"
    "v_mfma_f32_16x16x32_bf16 v[116:119], a[132:135], a[236:239], v[116:119]\n\t"
    "ds_read_b64_tr_b16 a[130:131], v241 offset:3616\n\t"
    "v_mfma_f32_16x16x32_bf16 v[116:119], a[136:139], a[252:255], v[116:119]\n\t"
    "s_waitcnt lgkmcnt(12)\n\t"
    "v_mfma_f32_16x16x32_bf16 v[24:27], a[140:143], a[224:227], v[24:27]\n\t"
    "ds_read_b64_tr_b16 a[132:133], v241 offset:7200\n\t"
    "v_mfma_f32_16x16x32_bf16 v[24:27], a[144:147], a[240:243], v[24:27]\n\t"
    "v_mfma_f32_16x16x32_bf16 v[56:59], a[140:143], a[228:231], v[56:59]\n\t"
    "ds_read_b64_tr_b16 a[134:135], v241 offset:10784\n\t"
    "v_mfma_f32_16x16x32_bf16 v[56:59], a[144:147], a[244:247], v[56:59]\n\t"
    "v_mfma_f32_16x16x32_bf16 v[88:91], a[140:143], a[232:235], v[88:91]\n\t"
    "ds_read_b64_tr_b16 a[136:137], v241 offset:14368\n\t"
    "v_mfma_f32_16x16x32_bf16 v[88:91], a[144:147], a[248:251], v[88:91]\n\t"
    "v_mfma_f32_16x16x32_bf16 v[120:123], a[140:143], a[236:239], v[120:123]\n\t"
    "ds_read_b64_tr_b16 a[138:139], v241 offset:17952\n\t"
    "v_mfma_f32_16x16x32_bf16 v[120:123], a[144:147], a[252:255], v[120:123]\n\t"
    "s_waitcnt lgkmcnt(12)\n\t"
    "v_mfma_f32_16x16x32_bf16 v[28:31], a[148:151], a[224:227], v[28:31]\n\t"
    "ds_read_b64_tr_b16 a[140:141], v241 offset:21536\n\t"
    "v_mfma_f32_16x16x32_bf16 v[28:31], a[152:155], a[240:243], v[28:31]\n\t"
    "v_mfma_f32_16x16x32_bf16 v[60:63], a[148:151], a[228:231], v[60:63]\n\t"
    "ds_read_b64_tr_b16 a[142:143], v241 offset:25120\n\t"
    "v_mfma_f32_16x16x32_bf16 v[60:63], a[152:155], a[244:247], v[60:63]\n\t"
    "v_mfma_f32_16x16x32_bf16 v[92:95], a[148:151], a[232:235], v[92:95]\n\t"
    "ds_read_b64_tr_b16 a[144:145], v236 offset:64\n\t"
    "v_mfma_f32_16x16x32_bf16 v[92:95], a[152:155], a[248:251], v[92:95]\n\t"
    "v_mfma_f32_16x16x32_bf16 v[124:127], a[148:151], a[236:239], v[124:127]\n\t"
    "ds_read_b64_tr_b16 a[146:147], v236 offset:3648\n\t"
    "v_mfma_f32_16x16x32_bf16 v[124:127], a[152:155], a[252:255], v[124:127]\n\t"
    "s_waitcnt lgkmcnt(12)\n\t"
    "v_mfma_f32_16x16x32_bf16 v[128:131], a[156:159], a[224:227], v[128:131]\n\t"
    "ds_read_b64_tr_b16 a[148:149], v236 offset:7232\n\t"
    "v_mfma_f32_16x16x32_bf16 v[128:131], a[160:163], a[240:243], v[128:131]\n\t"
    "v_mfma_f32_16x16x32_bf16 v[132:135], a[156:159], a[228:231], v[132:135]\n\t"
    "ds_read_b64_tr_b16 a[150:151], v236 offset:10816\n\t"
    "v_mfma_f32_16x16x32_bf16 v[132:135], a[160:163], a[244:247], v[132:135]\n\t"
    "v_mfma_f32_16x16x32_bf16 v[136:139], a[156:159], a[232:235], v[136:139]\n\t"
    "ds_read_b64_tr_b16 a[152:153], v241 offset:64\n\t"
    "v_mfma_f32_16x16x32_bf16 v[136:139], a[160:163], a[248:251], v[136:139]\n\t"
    "v_mfma_f32_16x16x32_bf16 v[140:143], a[156:159], a[236:239], v[140:143]\n\t"
    "ds_read_b64_tr_b16 a[154:155], v241 offset:3648\n\t"
    "v_mfma_f32_16x16x32_bf16 v[140:143], a[160:163], a[252:255], v[140:143]\n\t"
    "s_waitcnt lgkmcnt(12)\n\t"
    "v_mfma_f32_16x16x32_bf16 a[0:3], a[164:167], a[224:227], a[0:3]\n\t"
    "ds_read_b64_tr_b16 a[156:157], v241 offset:7232\n\t"
    "v_mfma_f32_16x16x32_bf16 a[0:3], a[168:171], a[240:243], a[0:3]\n\t"
    "v_cvt_pk_bf16_f32 v0, v0, v1\n\t"
    "v_cvt_pk_bf16_f32 v1, v2, v3\n\t"
    "v_mfma_f32_16x16x32_bf16 a[4:7], a[164:167], a[228:231], a[4:7]\n\t"
    "ds_read_b64_tr_b16 a[158:159], v241 offset:10816\n\t"
    "v_cvt_pk_bf16_f32 v2, v4, v5\n\t"
    "v_mfma_f32_16x16x32_bf16 a[4:7], a[168:171], a[244:247], a[4:7]\n\t"
    "v_cvt_pk_bf16_f32 v3, v6, v7\n\t"
    "v_cvt_pk_bf16_f32 v4, v8, v9\n\t"
    "v_mfma_f32_16x16x32_bf16 a[8:11], a[164:167], a[232:235], a[8:11]\n\t"
    "ds_read_b64_tr_b16 a[160:161], v236 offset:14400\n\t"
    "v_cvt_pk_bf16_f32 v5, v10, v11\n\t"
    "v_mfma_f32_16x16x32_bf16 a[8:11], a[168:171], a[248:251], a[8:11]\n\t"
    "v_cvt_pk_bf16_f32 v6, v12, v13\n\t"
    "v_cvt_pk_bf16_f32 v7, v14, v15\n\t"
    "v_mfma_f32_16x16x32_bf16 a[12:15], a[164:167], a[236:239], a[12:15]\n\t"
    "ds_read_b64_tr_b16 a[162:163], v236 offset:17984\n\t"
    "v_permlane16_swap_b32 v0, v2\n\t"
    "v_mfma_f32_16x16x32_bf16 a[12:15], a[168:171], a[252:255], a[12:15]\n\t"
    "s_waitcnt lgkmcnt(12)\n\t"
    "v_permlane16_swap_b32 v1, v3\n\t"
    "v_cvt_pk_bf16_f32 v8, v16, v17\n\t"
    "v_mfma_f32_16x16x32_bf16 v[144:147], a[128:131], a[224:227], v[144:147]\n\t"
    "ds_read_b64_tr_b16 a[164:165], v236 offset:21568\n\t"
    "v_cvt_pk_bf16_f32 v9, v18, v19\n\t"
    "v_mfma_f32_16x16x32_bf16 v[144:147], a[132:135], a[240:243], v[144:147]\n\t"
    "v_cvt_pk_bf16_f32 v10, v20, v21\n\t"
    "v_cvt_pk_bf16_f32 v11, v22, v23\n\t"
    "v_mfma_f32_16x16x32_bf16 v[148:151], a[128:131], a[228:231], v[148:151]\n\t"
    "ds_read_b64_tr_b16 a[166:167], v236 offset:25152\n\t"
    "v_permlane16_swap_b32 v4, v6\n\t"
    "v_mfma_f32_16x16x32_bf16 v[148:151], a[132:135], a[244:247], v[148:151]\n\t"
    "v_permlane16_swap_b32 v5, v7\n\t"
    "v_cvt_pk_bf16_f32 v12, v24, v25\n\t"
    "v_mfma_f32_16x16x32_bf16 v[152:155], a[128:131], a[232:235], v[152:155]\n\t"
    "ds_read_b64_tr_b16 a[168:169], v241 offset:14400\n\t"
    "v_cvt_pk_bf16_f32 v13, v26, v27\n\t"
    "v_mfma_f32_16x16x32_bf16 v[152:155], a[132:135], a[248:251], v[152:155]\n\t"
    "v_cvt_pk_bf16_f32 v14, v28, v29\n\t"
    "v_cvt_pk_bf16_f32 v15, v30, v31\n\t"
    "v_mfma_f32_16x16x32_bf16 v[156:159], a[128:131], a[236:239], v[156:159]\n\t"
    "ds_read_b64_tr_b16 a[170:171], v241 offset:17984\n\t"
    "v_permlane16_swap_b32 v8, v10\n\t"
    "v_mfma_f32_16x16x32_bf16 v[156:159], a[132:135], a[252:255], v[156:159]\n\t"
    "s_waitcnt lgkmcnt(12)\n\t"
    "v_permlane16_swap_b32 v9, v11\n\t"
    "v_cvt_pk_bf16_f32 v16, v32, v33\n\t"
    "v_mfma_f32_16x16x32_bf16 a[16:19], a[136:139], a[224:227], a[16:19]\n\t"
    "ds_read_b64_tr_b16 a[128:129], v241 offset:21568\n\t"
    "v_cvt_pk_bf16_f32 v17, v34, v35\n\t"
    "v_mfma_f32_16x16x32_bf16 a[16:19], a[140:143], a[240:243], a[16:19]\n\t"
    "v_cvt_pk_bf16_f32 v18, v36, v37\n\t"
    "v_cvt_pk_bf16_f32 v19, v38, v39\n\t"
    "v_mfma_f32_16x16x32_bf16 a[20:23], a[136:139], a[228:231], a[20:23]\n\t"
    "ds_read_b64_tr_b16 a[130:131], v241 offset:25152\n\t"
    "v_permlane16_swap_b32 v12, v14\n\t"
    "v_mfma_f32_16x16x32_bf16 a[20:23], a[140:143], a[244:247], a[20:23]\n\t"
    "v_permlane16_swap_b32 v13, v15\n\t"
    "v_cvt_pk_bf16_f32 v20, v40, v41\n\t"
    "v_mfma_f32_16x16x32_bf16 a[24:27], a[136:139], a[232:235], a[24:27]\n\t"
    "ds_read_b64_tr_b16 a[132:133], v236 offset:96\n\t"
    "v_cvt_pk_bf16_f32 v21, v42, v43\n\t"
    "v_mfma_f32_16x16x32_bf16 a[24:27], a[140:143], a[248:251], a[24:27]\n\t"
    "v_cvt_pk_bf16_f32 v22, v44, v45\n\t"
    "v_cvt_pk_bf16_f32 v23, v46, v47\n\t"
    "v_mfma_f32_16x16x32_bf16 a[28:31], a[136:139], a[236:239], a[28:31]\n\t"
    "ds_read_b64_tr_b16 a[134:135], v236 offset:3680\n\t"
    "v_permlane16_swap_b32 v16, v18\n\t"
    "v_mfma_f32_16x16x32_bf16 a[28:31], a[140:143], a[252:255], a[28:31]\n\t"
    "s_waitcnt lgkmcnt(10)\n\t"
    "v_permlane16_swap_b32 v17, v19\n\t"
    "v_cvt_pk_bf16_f32 v24, v48, v49\n\t"
    "v_mfma_f32_16x16x32_bf16 v[160:163], a[144:147], a[192:195], v[160:163]\n\t"
    "ds_read_b64_tr_b16 a[136:137], v236 offset:7264\n\t"
    "v_cvt_pk_bf16_f32 v25, v50, v51\n\t"
    "v_mfma_f32_16x16x32_bf16 v[160:163], a[148:151], a[208:211], v[160:163]\n\t"
    "v_cvt_pk_bf16_f32 v26, v52, v53\n\t"
    "v_cvt_pk_bf16_f32 v27, v54, v55\n\t"
    "v_mfma_f32_16x16x32_bf16 v[160:163], a[152:155], a[224:227], v[160:163]\n\t"
    "ds_read_b64_tr_b16 a[138:139], v236 offset:10848\n\t"
    "v_permlane16_swap_b32 v20, v22\n\t"
    "v_mfma_f32_16x16x32_bf16 v[160:163], a[156:159], a[240:243], v[160:163]\n\t"
    "v_permlane16_swap_b32 v21, v23\n\t"
    "v_cvt_pk_bf16_f32 v28, v56, v57\n\t"
    "v_mfma_f32_16x16x32_bf16 v[164:167], a[144:147], a[196:199], v[164:167]\n\t"
    "ds_read_b64_tr_b16 a[140:141], v241 offset:96\n\t"
    "v_cvt_pk_bf16_f32 v29, v58, v59\n\t"
    "v_mfma_f32_16x16x32_bf16 v[164:167], a[148:151], a[212:215], v[164:167]\n\t"
    "v_cvt_pk_bf16_f32 v30, v60, v61\n\t"
    "v_cvt_pk_bf16_f32 v31, v62, v63\n\t"
    "v_mfma_f32_16x16x32_bf16 v[164:167], a[152:155], a[228:231], v[164:167]\n\t"
    "ds_read_b64_tr_b16 a[142:143], v241 offset:3680\n\t"
    "v_permlane16_swap_b32 v24, v26\n\t"
    "v_mfma_f32_16x16x32_bf16 v[164:167], a[156:159], a[244:247], v[164:167]\n\t"
    "v_permlane16_swap_b32 v25, v27\n\t"
    "v_cvt_pk_bf16_f32 v32, v64, v65\n\t"
    "v_mfma_f32_16x16x32_bf16 v[168:171], a[144:147], a[200:203], v[168:171]\n\t"
    "v_cvt_pk_bf16_f32 v33, v66, v67\n\t"
    "v_cvt_pk_bf16_f32 v34, v68, v69\n\t"
    "v_mfma_f32_16x16x32_bf16 v[168:171], a[148:151], a[216:219], v[168:171]\n\t"
    "v_cvt_pk_bf16_f32 v35, v70, v71\n\t"
    "v_permlane16_swap_b32 v28, v30\n\t"
    "v_mfma_f32_16x16x32_bf16 v[168:171], a[152:155], a[232:235], v[168:171]\n\t"
    "v_permlane16_swap_b32 v29, v31\n\t"
    "v_cvt_pk_bf16_f32 v36, v72, v73\n\t"
    "v_mfma_f32_16x16x32_bf16 v[168:171], a[156:159], a[248:251], v[168:171]\n\t"
    "v_cvt_pk_bf16_f32 v37, v74, v75\n\t"
    "v_cvt_pk_bf16_f32 v38, v76, v77\n\t"
    "v_mfma_f32_16x16x32_bf16 v[172:175], a[144:147], a[204:207], v[172:175]\n\t"
    "v_cvt_pk_bf16_f32 v39, v78, v79\n\t"
    "v_permlane16_swap_b32 v32, v34\n\t"
    "v_mfma_f32_16x16x32_bf16 v[172:175], a[148:151], a[220:223], v[172:175]\n\t"
    "v_permlane16_swap_b32 v33, v35\n\t"
    "v_cvt_pk_bf16_f32 v40, v80, v81\n\t"
    "v_mfma_f32_16x16x32_bf16 v[172:175], a[152:155], a[236:239], v[172:175]\n\t"
    "v_cvt_pk_bf16_f32 v41, v82, v83\n\t"
    "v_cvt_pk_bf16_f32 v42, v84, v85\n\t"
    "v_mfma_f32_16x16x32_bf16 v[172:175], a[156:159], a[252:255], v[172:175]\n\t"
    "s_waitcnt lgkmcnt(6)\n\t"
    "v_cvt_pk_bf16_f32 v43, v86, v87\n\t"
    "v_permlane16_swap_b32 v36, v38\n\t"
    "v_mfma_f32_16x16x32_bf16 a[32:35], a[160:163], a[192:195], a[32:35]\n\t"
    "ds_read_b64_tr_b16 a[144:145], v241 offset:7264\n\t"
    "v_permlane16_swap_b32 v37, v39\n\t"
    "v_mfma_f32_16x16x32_bf16 a[32:35], a[164:167], a[208:211], a[32:35]\n\t"
    "v_cvt_pk_bf16_f32 v44, v88, v89\n\t"
    "v_cvt_pk_bf16_f32 v45, v90, v91\n\t"
    "v_mfma_f32_16x16x32_bf16 a[32:35], a[168:171], a[224:227], a[32:35]\n\t"
    "ds_read_b64_tr_b16 a[146:147], v241 offset:10848\n\t"
    "v_cvt_pk_bf16_f32 v46, v92, v93\n\t"
    "v_mfma_f32_16x16x32_bf16 a[32:35], a[128:131], a[240:243], a[32:35]\n\t"
    "v_cvt_pk_bf16_f32 v47, v94, v95\n\t"
    "v_permlane16_swap_b32 v40, v42\n\t"
    "v_mfma_f32_16x16x32_bf16 a[36:39], a[160:163], a[196:199], a[36:39]\n\t"
    "ds_read_b64_tr_b16 a[148:149], v236 offset:14432\n\t"
    "v_permlane16_swap_b32 v41, v43\n\t"
    "v_mfma_f32_16x16x32_bf16 a[36:39], a[164:167], a[212:215], a[36:39]\n\t"
    "v_cvt_pk_bf16_f32 v48, v96, v97\n\t"
    "v_cvt_pk_bf16_f32 v49, v98, v99\n\t"
    "v_mfma_f32_16x16x32_bf16 a[36:39], a[168:171], a[228:231], a[36:39]\n\t"
    "ds_read_b64_tr_b16 a[150:151], v236 offset:18016\n\t"
    "v_cvt_pk_bf16_f32 v50, v100, v101\n\t"
    "v_mfma_f32_16x16x32_bf16 a[36:39], a[128:131], a[244:247], a[36:39]\n\t"
    "v_cvt_pk_bf16_f32 v51, v102, v103\n\t"
    "v_permlane16_swap_b32 v44, v46\n\t"
    "v_mfma_f32_16x16x32_bf16 a[40:43], a[160:163], a[200:203], a[40:43]\n\t"
    "ds_read_b64_tr_b16 a[152:153], v236 offset:21600\n\t"
    "v_permlane16_swap_b32 v45, v47\n\t"
    "v_mfma_f32_16x16x32_bf16 a[40:43], a[164:167], a[216:219], a[40:43]\n\t"
    "v_cvt_pk_bf16_f32 v52, v104, v105\n\t"
    "v_cvt_pk_bf16_f32 v53, v106, v107\n\t"
    "v_mfma_f32_16x16x32_bf16 a[40:43], a[168:171], a[232:235], a[40:43]\n\t"
    "ds_read_b64_tr_b16 a[154:155], v236 offset:25184\n\t"
    "v_cvt_pk_bf16_f32 v54, v108, v109\n\t"
    "v_mfma_f32_16x16x32_bf16 a[40:43], a[128:131], a[248:251], a[40:43]\n\t"
    "v_cvt_pk_bf16_f32 v55, v110, v111\n\t"
    "v_permlane16_swap_b32 v48, v50\n\t"
    "v_mfma_f32_16x16x32_bf16 a[44:47], a[160:163], a[204:207], a[44:47]\n\t"
    "ds_read_b64_tr_b16 a[156:157], v241 offset:14432\n\t"
    "v_permlane16_swap_b32 v49, v51\n\t"
    "v_mfma_f32_16x16x32_bf16 a[44:47], a[164:167], a[220:223], a[44:47]\n\t"
    "v_cvt_pk_bf16_f32 v56, v112, v113\n\t"
    "v_cvt_pk_bf16_f32 v57, v114, v115\n\t"
    "v_mfma_f32_16x16x32_bf16 a[44:47], a[168:171], a[236:239], a[44:47]\n\t"
    "ds_read_b64_tr_b16 a[158:159], v241 offset:18016\n\t"
    "v_cvt_pk_bf16_f32 v58, v116, v117\n\t"
    "v_mfma_f32_16x16x32_bf16 a[44:47], a[128:131], a[252:255], a[44:47]\n\t"
    "s_waitcnt lgkmcnt(6)\n\t"
    "v_cvt_pk_bf16_f32 v59, v118, v119\n\t"
    "v_permlane16_swap_b32 v52, v54\n\t"
    "v_mfma_f32_16x16x32_bf16 v[176:179], a[132:135], a[192:195], v[176:179]\n\t"
    "ds_read_b64_tr_b16 a[160:161], v241 offset:21600\n\t"
    "v_permlane16_swap_b32 v53, v55\n\t"
    "v_mfma_f32_16x16x32_bf16 v[176:179], a[136:139], a[208:211], v[176:179]\n\t"
    "v_cvt_pk_bf16_f32 v60, v120, v121\n\t"
    "v_cvt_pk_bf16_f32 v61, v122, v123\n\t"
    "v_mfma_f32_16x16x32_bf16 v[176:179], a[140:143], a[224:227], v[176:179]\n\t"
    "ds_read_b64_tr_b16 a[162:163], v241 offset:25184\n\t"
    "v_cvt_pk_bf16_f32 v62, v124, v125\n\t"
    "v_mfma_f32_16x16x32_bf16 v[176:179], a[144:147], a[240:243], v[176:179]\n\t"
    "v_cvt_pk_bf16_f32 v63, v126, v127\n\t"
    "v_permlane16_swap_b32 v56, v58\n\t"
    "v_mfma_f32_16x16x32_bf16 v[180:183], a[132:135], a[196:199], v[180:183]\n\t"
    "ds_read_b64_tr_b16 a[164:165], v236 offset:128\n\t"
    "v_permlane16_swap_b32 v57, v59\n\t"
    "s_nop 1\n\t"
    "v_mfma_f32_16x16x32_bf16 v[180:183], a[136:139], a[212:215], v[180:183]\n\t"
    "v_permlane16_swap_b32 v60, v62\n\t"
    "v_permlane16_swap_b32 v61, v63\n\t"
    "v_mfma_f32_16x16x32_bf16 v[180:183], a[140:143], a[228:231], v[180:183]\n\t"
    "ds_read_b64_tr_b16 a[166:167], v236 offset:3712\n\t"
    "v_accvgpr_read_b32 v80, a0\n\t"
    "v_mfma_f32_16x16x32_bf16 v[180:183], a[144:147], a[244:247], v[180:183]\n\t"
    "v_accvgpr_read_b32 v81, a1\n\t"
    "v_accvgpr_read_b32 v82, a2\n\t"
    "v_mfma_f32_16x16x32_bf16 v[184:187], a[132:135], a[200:203], v[184:187]\n\t"
    "ds_read_b64_tr_b16 a[168:169], v236 offset:7296\n\t"
    "v_accvgpr_read_b32 v83, a3\n\t"
    "v_mfma_f32_16x16x32_bf16 v[184:187], a[136:139], a[216:219], v[184:187]\n\t"
    "v_max_f32 v128, 0, v128\n\t"
    "v_max_f32 v129, 0, v129\n\t"
    "v_mfma_f32_16x16x32_bf16 v[184:187], a[140:143], a[232:235], v[184:187]\n\t"
    "ds_read_b64_tr_b16 a[170:171], v236 offset:10880\n\t"
    "v_max_f32 v130, 0, v130\n\t"
    "v_mfma_f32_16x16x32_bf16 v[184:187], a[144:147], a[248:251], v[184:187]\n\t"
    "v_max_f32 v131, 0, v131\n\t"
    "v_mul_f32 v128, v128, v80\n\t"
    "v_mfma_f32_16x16x32_bf16 v[188:191], a[132:135], a[204:207], v[188:191]\n\t"
    "ds_read_b64_tr_b16 a[128:129], v241 offset:128\n\t"
    "v_mul_f32 v129, v129, v81\n\t"
    "v_mfma_f32_16x16x32_bf16 v[188:191], a[136:139], a[220:223], v[188:191]\n\t"
    "v_mul_f32 v130, v130, v82\n\t"
    "v_mul_f32 v131, v131, v83\n\t"
    "v_mfma_f32_16x16x32_bf16 v[188:191], a[140:143], a[236:239], v[188:191]\n\t"
    "ds_read_b64_tr_b16 a[130:131], v241 offset:3712\n\t"
    "v_cvt_pk_bf16_f32 v128, v128, v129\n\t"
    "v_mfma_f32_16x16x32_bf16 v[188:191], a[144:147], a[252:255], v[188:191]\n\t"
    "s_waitcnt lgkmcnt(6)\n\t"
    "v_cvt_pk_bf16_f32 v129, v130, v131\n\t"
    "v_accvgpr_read_b32 v84, a4\n\t"
    "v_mfma_f32_16x16x32_bf16 a[48:51], a[148:151], a[192:195], a[48:51]\n\t"
    "ds_read_b64_tr_b16 a[132:133], v241 offset:7296\n\t"
    "v_accvgpr_read_b32 v85, a5\n\t"
    "v_mfma_f32_16x16x32_bf16 a[48:51], a[152:155], a[208:211], a[48:51]\n\t"
    "v_accvgpr_read_b32 v86, a6\n\t"
    "v_accvgpr_read_b32 v87, a7\n\t"
    "v_mfma_f32_16x16x32_bf16 a[48:51], a[156:159], a[224:227], a[48:51]\n\t"
    "ds_read_b64_tr_b16 a[134:135], v241 offset:10880\n\t"
    "v_max_f32 v132, 0, v132\n\t"
    "v_mfma_f32_16x16x32_bf16 a[48:51], a[160:163], a[240:243], a[48:51]\n\t"
    "v_max_f32 v133, 0, v133\n\t"
    "v_max_f32 v134, 0, v134\n\t"
    "v_mfma_f32_16x16x32_bf16 a[52:55], a[148:151], a[196:199], a[52:55]\n\t"
    "ds_read_b64_tr_b16 a[136:137], v236 offset:14464\n\t"
    "v_max_f32 v135, 0, v135\n\t"
    "v_mfma_f32_16x16x32_bf16 a[52:55], a[152:155], a[212:215], a[52:55]\n\t"
    "v_mul_f32 v132, v132, v84\n\t"
    "v_mul_f32 v133, v133, v85\n\t"
    "v_mfma_f32_16x16x32_bf16 a[52:55], a[156:159], a[228:231], a[52:55]\n\t"
    "ds_read_b64_tr_b16 a[138:139], v236 offset:18048\n\t"
    "v_mul_f32 v134, v134, v86\n\t"
    "v_mfma_f32_16x16x32_bf16 a[52:55], a[160:163], a[244:247], a[52:55]\n\t"
    "v_mul_f32 v135, v135, v87\n\t"
    "v_cvt_pk_bf16_f32 v132, v132, v133\n\t"
    "v_mfma_f32_16x16x32_bf16 a[56:59], a[148:151], a[200:203], a[56:59]\n\t"
    "ds_read_b64_tr_b16 a[140:141], v236 offset:21632\n\t"
    "v_cvt_pk_bf16_f32 v133, v134, v135\n\t"
    "v_mfma_f32_16x16x32_bf16 a[56:59], a[152:155], a[216:219], a[56:59]\n\t"
    "v_accvgpr_read_b32 v80, a8\n\t"
    "v_accvgpr_read_b32 v81, a9\n\t"
    "v_mfma_f32_16x16x32_bf16 a[56:59], a[156:159], a[232:235], a[56:59]\n\t"
    "ds_read_b64_tr_b16 a[142:143], v236 offset:25216\n\t"
    "v_accvgpr_read_b32 v82, a10\n\t"
    "v_mfma_f32_16x16x32_bf16 a[56:59], a[160:163], a[248:251], a[56:59]\n\t"
    "v_accvgpr_read_b32 v83, a11\n\t"
    "v_max_f32 v136, 0, v136\n\t"
    "v_mfma_f32_16x16x32_bf16 a[60:63], a[148:151], a[204:207], a[60:63]\n\t"
    "ds_read_b64_tr_b16 a[144:145], v241 offset:14464\n\t"
    "v_max_f32 v137, 0, v137\n\t"
    "v_mfma_f32_16x16x32_bf16 a[60:63], a[152:155], a[220:223], a[60:63]\n\t"
    "v_max_f32 v138, 0, v138\n\t"
    "v_max_f32 v139, 0, v139\n\t"
    "v_mfma_f32_16x16x32_bf16 a[60:63], a[156:159], a[236:239], a[60:63]\n\t"
    "ds_read_b64_tr_b16 a[146:147], v241 offset:18048\n\t"
    "v_mul_f32 v136, v136, v80\n\t"
    "v_mfma_f32_16x16x32_bf16 a[60:63], a[160:163], a[252:255], a[60:63]\n\t"
    "s_waitcnt lgkmcnt(6)\n\t"
    "v_mul_f32 v137, v137, v81\n\t"
    "v_mul_f32 v138, v138, v82\n\t"
    "v_mfma_f32_16x16x32_bf16 v[192:195], a[164:167], a[192:195], v[192:195]\n\t"
    "ds_read_b64_tr_b16 a[148:149], v241 offset:21632\n\t"
    "v_mul_f32 v139, v139, v83\n\t"
    "v_mfma_f32_16x16x32_bf16 v[192:195], a[168:171], a[208:211], v[192:195]\n\t"
    "v_cvt_pk_bf16_f32 v136, v136, v137\n\t"
    "v_cvt_pk_bf16_f32 v137, v138, v139\n\t"
    "v_mfma_f32_16x16x32_bf16 v[192:195], a[128:131], a[224:227], v[192:195]\n\t"
    "ds_read_b64_tr_b16 a[150:151], v241 offset:25216\n\t"
    "v_accvgpr_read_b32 v84, a12\n\t"
    "v_mfma_f32_16x16x32_bf16 v[192:195], a[132:135], a[240:243], v[192:195]\n\t"
    "v_accvgpr_read_b32 v85, a13\n\t"
    "v_accvgpr_read_b32 v86, a14\n\t"
    "v_mfma_f32_16x16x32_bf16 v[196:199], a[164:167], a[196:199], v[196:199]\n\t"
    "ds_read_b64_tr_b16 a[152:153], v236 offset:160\n\t"
    "v_accvgpr_read_b32 v87, a15\n\t"
    "v_mfma_f32_16x16x32_bf16 v[196:199], a[168:171], a[212:215], v[196:199]\n\t"
    "v_max_f32 v140, 0, v140\n\t"
    "v_max_f32 v141, 0, v141\n\t"
    "v_mfma_f32_16x16x32_bf16 v[196:199], a[128:131], a[228:231], v[196:199]\n\t"
    "ds_read_b64_tr_b16 a[154:155], v236 offset:3744\n\t"
    "v_max_f32 v142, 0, v142\n\t"
    "v_mfma_f32_16x16x32_bf16 v[196:199], a[132:135], a[244:247], v[196:199]\n\t"
    "v_max_f32 v143, 0, v143\n\t"
    "v_mul_f32 v140, v140, v84\n\t"
    "v_mfma_f32_16x16x32_bf16 v[200:203], a[164:167], a[200:203], v[200:203]\n\t"
    "ds_read_b64_tr_b16 a[156:157], v236 offset:7328\n\t"
    "v_mul_f32 v141, v141, v85\n\t"
    "v_mfma_f32_16x16x32_bf16 v[200:203], a[168:171], a[216:219], v[200:203]\n\t"
    "v_mul_f32 v142, v142, v86\n\t"
    "v_mul_f32 v143, v143, v87\n\t"
    "v_mfma_f32_16x16x32_bf16 v[200:203], a[128:131], a[232:235], v[200:203]\n\t"
    "ds_read_b64_tr_b16 a[158:159], v236 offset:10912\n\t"
    "v_cvt_pk_bf16_f32 v140, v140, v141\n\t"
    "v_mfma_f32_16x16x32_bf16 v[200:203], a[132:135], a[248:251], v[200:203]\n\t"
    "v_cvt_pk_bf16_f32 v141, v142, v143\n\t"
    "v_accvgpr_read_b32 v80, a16\n\t"
    "v_mfma_f32_16x16x32_bf16 v[204:207], a[164:167], a[204:207], v[204:207]\n\t"
    "ds_read_b64_tr_b16 a[160:161], v241 offset:160\n\t"
    "v_accvgpr_read_b32 v81, a17\n\t"
    "v_mfma_f32_16x16x32_bf16 v[204:207], a[168:171], a[220:223], v[204:207]\n\t"
    "v_accvgpr_read_b32 v82, a18\n\t"
    "v_accvgpr_read_b32 v83, a19\n\t"
    "v_mfma_f32_16x16x32_bf16 v[204:207], a[128:131], a[236:239], v[204:207]\n\t"
    "ds_read_b64_tr_b16 a[162:163], v241 offset:3744\n\t"
    "v_max_f32 v144, 0, v144\n\t"
    "v_mfma_f32_16x16x32_bf16 v[204:207], a[132:135], a[252:255], v[204:207]\n\t"
    "s_waitcnt lgkmcnt(6)\n\t"
    "v_max_f32 v145, 0, v145\n\t"
    "v_max_f32 v146, 0, v146\n\t"
    "v_mfma_f32_16x16x32_bf16 a[64:67], a[136:139], a[192:195], a[64:67]\n\t"
    "ds_read_b64_tr_b16 a[164:165], v241 offset:7328\n\t"
    "v_max_f32 v147, 0, v147\n\t"
    "v_mfma_f32_16x16x32_bf16 a[64:67], a[140:143], a[208:211], a[64:67]\n\t"
    "v_mul_f32 v144, v144, v80\n\t"
    "v_mul_f32 v145, v145, v81\n\t"
    "v_mfma_f32_16x16x32_bf16 a[64:67], a[144:147], a[224:227], a[64:67]\n\t"
    "ds_read_b64_tr_b16 a[166:167], v241 offset:10912\n\t"
    "v_mul_f32 v146, v146, v82\n\t"
    "v_mfma_f32_16x16x32_bf16 a[64:67], a[148:151], a[240:243], a[64:67]\n\t"
    "v_mul_f32 v147, v147, v83\n\t"
    "v_cvt_pk_bf16_f32 v130, v144, v145\n\t"
    "v_mfma_f32_16x16x32_bf16 a[68:71], a[136:139], a[196:199], a[68:71]\n\t"
    "ds_read_b64_tr_b16 a[168:169], v236 offset:14496\n\t"
    "v_cvt_pk_bf16_f32 v131, v146, v147\n\t"
    "s_add_u32 s35, s68, 0\n\t"
    "v_mfma_f32_16x16x32_bf16 a[68:71], a[140:143], a[212:215], a[68:71]\n\t"
    "buffer_store_dwordx4 v[128:131], v248, s[84:87], s35 offen sc1\n\t"
    "v_accvgpr_read_b32 v84, a20\n\t"
    "v_mfma_f32_16x16x32_bf16 a[68:71], a[144:147], a[228:231], a[68:71]\n\t"
    "ds_read_b64_tr_b16 a[170:171], v236 offset:18080\n\t"
    "v_accvgpr_read_b32 v85, a21\n\t"
    "v_mfma_f32_16x16x32_bf16 a[68:71], a[148:151], a[244:247], a[68:71]\n\t"
    "v_accvgpr_read_b32 v86, a22\n\t"
    "v_accvgpr_read_b32 v87, a23\n\t"
    "v_mfma_f32_16x16x32_bf16 a[72:75], a[136:139], a[200:203], a[72:75]\n\t"
    "ds_read_b64_tr_b16 a[128:129], v236 offset:21664\n\t"
    "v_max_f32 v148, 0, v148\n\t"
    "v_mfma_f32_16x16x32_bf16 a[72:75], a[140:143], a[216:219], a[72:75]\n\t"
    "v_max_f32 v149, 0, v149\n\t"
    "v_max_f32 v150, 0, v150\n\t"
    "v_mfma_f32_16x16x32_bf16 a[72:75], a[144:147], a[232:235], a[72:75]\n\t"
    "ds_read_b64_tr_b16 a[130:131], v236 offset:25248\n\t"
    "v_max_f32 v151, 0, v151\n\t"
    "v_mfma_f32_16x16x32_bf16 a[72:75], a[148:151], a[248:251], a[72:75]\n\t"
    "v_mul_f32 v148, v148, v84\n\t"
    "v_mul_f32 v149, v149, v85\n\t"
    "v_mfma_f32_16x16x32_bf16 a[76:79], a[136:139], a[204:207], a[76:79]\n\t"
    "ds_read_b64_tr_b16 a[132:133], v241 offset:14496\n\t"
    "v_mul_f32 v150, v150, v86\n\t"
    "v_mfma_f32_16x16x32_bf16 a[76:79], a[140:143], a[220:223], a[76:79]\n\t"
    "v_mul_f32 v151, v151, v87\n\t"
    "v_cvt_pk_bf16_f32 v134, v148, v149\n\t"
    "v_mfma_f32_16x16x32_bf16 a[76:79], a[144:147], a[236:239], a[76:79]\n\t"
    "ds_read_b64_tr_b16 a[134:135], v241 offset:18080\n\t"
    "v_cvt_pk_bf16_f32 v135, v150, v151\n\t"
    "s_add_u32 s35, s68, 114688\n\t"
    "v_mfma_f32_16x16x32_bf16 a[76:79], a[148:151], a[252:255], a[76:79]\n\t"
    "buffer_store_dwordx4 v[132:135], v248, s[84:87], s35 offen sc1\n\t"
    "s_waitcnt lgkmcnt(6)\n\t"
    "v_accvgpr_read_b32 v80, a24\n\t"
    "v_mfma_f32_16x16x32_bf16 v[208:211], a[152:155], a[192:195], v[208:211]\n\t"
    "ds_read_b64_tr_b16 a[136:137], v241 offset:21664\n\t"
    "v_accvgpr_read_b32 v81, a25\n\t"
    "v_mfma_f32_16x16x32_bf16 v[208:211], a[156:159], a[208:211], v[208:211]\n\t"
    "v_accvgpr_read_b32 v82, a26\n\t"
    "v_accvgpr_read_b32 v83, a27\n\t"
    "v_mfma_f32_16x16x32_bf16 v[208:211], a[160:163], a[224:227], v[208:211]\n\t"
    "ds_read_b64_tr_b16 a[138:139], v241 offset:25248\n\t"
    "v_max_f32 v152, 0, v152\n\t"
    "v_mfma_f32_16x16x32_bf16 v[208:211], a[164:167], a[240:243], v[208:211]\n\t"
    "v_max_f32 v153, 0, v153\n\t"
    "v_max_f32 v154, 0, v154\n\t"
    "v_mfma_f32_16x16x32_bf16 v[212:215], a[152:155], a[196:199], v[212:215]\n\t"
    "ds_read_b64_tr_b16 a[140:141], v236 offset:192\n\t"
    "v_max_f32 v155, 0, v155\n\t"
    "v_mfma_f32_16x16x32_bf16 v[212:215], a[156:159], a[212:215], v[212:215]\n\t"
    "v_mul_f32 v152, v152, v80\n\t"
    "v_mul_f32 v153, v153, v81\n\t"
    "v_mfma_f32_16x16x32_bf16 v[212:215], a[160:163], a[228:231], v[212:215]\n\t"
    "ds_read_b64_tr_b16 a[142:143], v236 offset:3776\n\t"
    "v_mul_f32 v154, v154, v82\n\t"
    "v_mfma_f32_16x16x32_bf16 v[212:215], a[164:167], a[244:247], v[212:215]\n\t"
    "v_mul_f32 v155, v155, v83\n\t"
    "v_cvt_pk_bf16_f32 v138, v152, v153\n\t"
    "v_mfma_f32_16x16x32_bf16 v[216:219], a[152:155], a[200:203], v[216:219]\n\t"
    "ds_read_b64_tr_b16 a[144:145], v236 offset:7360\n\t"
    "v_cvt_pk_bf16_f32 v139, v154, v155\n\t"
    "s_add_u32 s35, s68, 229376\n\t"
    "v_mfma_f32_16x16x32_bf16 v[216:219], a[156:159], a[216:219], v[216:219]\n\t"
    "buffer_store_dwordx4 v[136:139], v248, s[84:87], s35 offen sc1\n\t"
    "v_accvgpr_read_b32 v84, a28\n\t"
    "v_mfma_f32_16x16x32_bf16 v[216:219], a[160:163], a[232:235], v[216:219]\n\t"
    "ds_read_b64_tr_b16 a[146:147], v236 offset:10944\n\t"
    "v_accvgpr_read_b32 v85, a29\n\t"
    "v_mfma_f32_16x16x32_bf16 v[216:219], a[164:167], a[248:251], v[216:219]\n\t"
    "v_accvgpr_read_b32 v86, a30\n\t"
    "v_accvgpr_read_b32 v87, a31\n\t"
    "v_mfma_f32_16x16x32_bf16 v[220:223], a[152:155], a[204:207], v[220:223]\n\t"
    "ds_read_b64_tr_b16 a[148:149], v241 offset:192\n\t"
    "v_max_f32 v156, 0, v156\n\t"
    "v_mfma_f32_16x16x32_bf16 v[220:223], a[156:159], a[220:223], v[220:223]\n\t"
    "v_max_f32 v157, 0, v157\n\t"
    "v_max_f32 v158, 0, v158\n\t"
    "v_mfma_f32_16x16x32_bf16 v[220:223], a[160:163], a[236:239], v[220:223]\n\t"
    "ds_read_b64_tr_b16 a[150:151], v241 offset:3776\n\t"
    "v_max_f32 v159, 0, v159\n\t"
    "v_mfma_f32_16x16x32_bf16 v[220:223], a[164:167], a[252:255], v[220:223]\n\t"
    "s_waitcnt lgkmcnt(6)\n\t"
    "v_mul_f32 v156, v156, v84\n\t"
    "v_mul_f32 v157, v157, v85\n\t"
    "v_mfma_f32_16x16x32_bf16 a[80:83], a[168:171], a[192:195], a[80:83]\n\t"
    "ds_read_b64_tr_b16 a[152:153], v241 offset:7360\n\t"
    "v_mul_f32 v158, v158, v86\n\t"
    "v_mfma_f32_16x16x32_bf16 a[80:83], a[128:131], a[208:211], a[80:83]\n\t"
    "v_mul_f32 v159, v159, v87\n\t"
    "v_cvt_pk_bf16_f32 v142, v156, v157\n\t"
    "v_mfma_f32_16x16x32_bf16 a[80:83], a[132:135], a[224:227], a[80:83]\n\t"
    "ds_read_b64_tr_b16 a[154:155], v241 offset:10944\n\t"
    "v_cvt_pk_bf16_f32 v143, v158, v159\n\t"
    "s_add_u32 s35, s68, 344064\n\t"
    "v_mfma_f32_16x16x32_bf16 a[80:83], a[136:139], a[240:243], a[80:83]\n\t"
    "buffer_store_dwordx4 v[140:143], v248, s[84:87], s35 offen sc1\n\t"
    "v_accvgpr_read_b32 v80, a32\n\t"
    "v_mfma_f32_16x16x32_bf16 a[84:87], a[168:171], a[196:199], a[84:87]\n\t"
    "ds_read_b64_tr_b16 a[156:157], v236 offset:14528\n\t"
    "v_accvgpr_read_b32 v81, a33\n\t"
    "v_mfma_f32_16x16x32_bf16 a[84:87], a[128:131], a[212:215], a[84:87]\n\t"
    "v_accvgpr_read_b32 v82, a34\n\t"
    "v_accvgpr_read_b32 v83, a35\n\t"
    "v_mfma_f32_16x16x32_bf16 a[84:87], a[132:135], a[228:231], a[84:87]\n\t"
    "ds_read_b64_tr_b16 a[158:159], v236 offset:18112\n\t"
    "v_max_f32 v160, 0, v160\n\t"
    "v_mfma_f32_16x16x32_bf16 a[84:87], a[136:139], a[244:247], a[84:87]\n\t"
    "v_max_f32 v161, 0, v161\n\t"
    "v_max_f32 v162, 0, v162\n\t"
    "v_mfma_f32_16x16x32_bf16 a[88:91], a[168:171], a[200:203], a[88:91]\n\t"
    "ds_read_b64_tr_b16 a[160:161], v236 offset:21696\n\t"
    "v_max_f32 v163, 0, v163\n\t"
    "v_mfma_f32_16x16x32_bf16 a[88:91], a[128:131], a[216:219], a[88:91]\n\t"
    "v_mul_f32 v160, v160, v80\n\t"
    "v_mul_f32 v161, v161, v81\n\t"
    "v_mfma_f32_16x16x32_bf16 a[88:91], a[132:135], a[232:235], a[88:91]\n\t"
    "ds_read_b64_tr_b16 a[162:163], v236 offset:25280\n\t"
    "v_mul_f32 v162, v162, v82\n\t"
    "v_mfma_f32_16x16x32_bf16 a[88:91], a[136:139], a[248:251], a[88:91]\n\t"
    "v_mul_f32 v163, v163, v83\n\t"
    "v_cvt_pk_bf16_f32 v160, v160, v161\n\t"
    "v_mfma_f32_16x16x32_bf16 a[92:95], a[168:171], a[204:207], a[92:95]\n\t"
    "ds_read_b64_tr_b16 a[164:165], v241 offset:14528\n\t"
    "v_cvt_pk_bf16_f32 v161, v162, v163\n\t"
    "v_mfma_f32_16x16x32_bf16 a[92:95], a[128:131], a[220:223], a[92:95]\n\t"
    "v_accvgpr_read_b32 v84, a36\n\t"
    "v_accvgpr_read_b32 v85, a37\n\t"
    "v_mfma_f32_16x16x32_bf16 a[92:95], a[132:135], a[236:239], a[92:95]\n\t"
    "ds_read_b64_tr_b16 a[166:167], v241 offset:18112\n\t"
    "v_accvgpr_read_b32 v86, a38\n\t"
    "v_mfma_f32_16x16x32_bf16 a[92:95], a[136:139], a[252:255], a[92:95]\n\t"
    "s_waitcnt lgkmcnt(6)\n\t"
    "v_accvgpr_read_b32 v87, a39\n\t"
    "v_max_f32 v164, 0, v164\n\t"
    "v_mfma_f32_16x16x32_bf16 v[224:227], a[140:143], a[192:195], v[224:227]\n\t"
    "ds_read_b64_tr_b16 a[168:169], v241 offset:21696\n\t"
    "v_max_f32 v165, 0, v165\n\t"
    "v_mfma_f32_16x16x32_bf16 v[224:227], a[144:147], a[208:211], v[224:227]\n\t"
    "v_max_f32 v166, 0, v166\n\t"
    "v_max_f32 v167, 0, v167\n\t"
    "v_mfma_f32_16x16x32_bf16 v[224:227], a[148:151], a[224:227], v[224:227]\n\t"
    "ds_read_b64_tr_b16 a[170:171], v241 offset:25280\n\t"
    "v_mul_f32 v164, v164, v84\n\t"
    "v_mfma_f32_16x16x32_bf16 v[224:227], a[152:155], a[240:243], v[224:227]\n\t"
    "v_mul_f32 v165, v165, v85\n\t"
    "v_mul_f32 v166, v166, v86\n\t"
    "v_mfma_f32_16x16x32_bf16 a[112:115], a[140:143], a[196:199], a[112:115]\n\t"
    "v_mul_f32 v167, v167, v87\n\t"
    "v_cvt_pk_bf16_f32 v164, v164, v165\n\t"
    "v_mfma_f32_16x16x32_bf16 a[112:115], a[144:147], a[212:215], a[112:115]\n\t"
    "v_cvt_pk_bf16_f32 v165, v166, v167\n\t"
    "v_accvgpr_read_b32 v80, a40\n\t"
    "v_mfma_f32_16x16x32_bf16 a[112:115], a[148:151], a[228:231], a[112:115]\n\t"
    "v_accvgpr_read_b32 v81, a41\n\t"
    "v_accvgpr_read_b32 v82, a42\n\t"
    "v_mfma_f32_16x16x32_bf16 a[112:115], a[152:155], a[244:247], a[112:115]\n\t"
    "v_accvgpr_read_b32 v83, a43\n\t"
    "v_max_f32 v168, 0, v168\n\t"
    "v_mfma_f32_16x16x32_bf16 a[116:119], a[140:143], a[200:203], a[116:119]\n\t"
    "v_max_f32 v169, 0, v169\n\t"
    "v_max_f32 v170, 0, v170\n\t"
    "v_mfma_f32_16x16x32_bf16 a[116:119], a[144:147], a[216:219], a[116:119]\n\t"
    "v_max_f32 v171, 0, v171\n\t"
    "v_mul_f32 v168, v168, v80\n\t"
    "v_mfma_f32_16x16x32_bf16 a[116:119], a[148:151], a[232:235], a[116:119]\n\t"
    "v_mul_f32 v169, v169, v81\n\t"
    "v_mul_f32 v170, v170, v82\n\t"
    "v_mfma_f32_16x16x32_bf16 a[116:119], a[152:155], a[248:251], a[116:119]\n\t"
    "v_mul_f32 v171, v171, v83\n\t"
    "v_cvt_pk_bf16_f32 v168, v168, v169\n\t"
    "v_mfma_f32_16x16x32_bf16 a[120:123], a[140:143], a[204:207], a[120:123]\n\t"
    "v_cvt_pk_bf16_f32 v169, v170, v171\n\t"
    "v_accvgpr_read_b32 v84, a44\n\t"
    "v_mfma_f32_16x16x32_bf16 a[120:123], a[144:147], a[220:223], a[120:123]\n\t"
    "v_accvgpr_read_b32 v85, a45\n\t"
    "v_accvgpr_read_b32 v86, a46\n\t"
    "v_mfma_f32_16x16x32_bf16 a[120:123], a[148:151], a[236:239], a[120:123]\n\t"
    "v_accvgpr_read_b32 v87, a47\n\t"
    "v_max_f32 v172, 0, v172\n\t"
    "v_mfma_f32_16x16x32_bf16 a[120:123], a[152:155], a[252:255], a[120:123]\n\t"
    "s_waitcnt lgkmcnt(0)\n\t"
    "v_max_f32 v173, 0, v173\n\t"
    "v_max_f32 v174, 0, v174\n\t"
    "v_mfma_f32_16x16x32_bf16 a[96:99], a[156:159], a[192:195], a[96:99]\n\t"
    "v_max_f32 v175, 0, v175\n\t"
    "v_mul_f32 v172, v172, v84\n\t"
    "v_mfma_f32_16x16x32_bf16 a[96:99], a[160:163], a[208:211], a[96:99]\n\t"
    "v_mul_f32 v173, v173, v85\n\t"
    "v_mul_f32 v174, v174, v86\n\t"
    "v_mfma_f32_16x16x32_bf16 a[96:99], a[164:167], a[224:227], a[96:99]\n\t"
    "v_mul_f32 v175, v175, v87\n\t"
    "v_cvt_pk_bf16_f32 v172, v172, v173\n\t"
    "v_mfma_f32_16x16x32_bf16 a[96:99], a[168:171], a[240:243], a[96:99]\n\t"
    "v_cvt_pk_bf16_f32 v173, v174, v175\n\t"
    "v_accvgpr_read_b32 v80, a48\n\t"
    "v_mfma_f32_16x16x32_bf16 a[100:103], a[156:159], a[196:199], a[100:103]\n\t"
    "v_accvgpr_read_b32 v81, a49\n\t"
    "v_accvgpr_read_b32 v82, a50\n\t"
    "v_mfma_f32_16x16x32_bf16 a[100:103], a[160:163], a[212:215], a[100:103]\n\t"
    "v_accvgpr_read_b32 v83, a51\n\t"
    "v_max_f32 v176, 0, v176\n\t"
    "v_mfma_f32_16x16x32_bf16 a[100:103], a[164:167], a[228:231], a[100:103]\n\t"
    "v_max_f32 v177, 0, v177\n\t"
    "v_max_f32 v178, 0, v178\n\t"
    "v_mfma_f32_16x16x32_bf16 a[100:103], a[168:171], a[244:247], a[100:103]\n\t"
    "v_max_f32 v179, 0, v179\n\t"
    "v_mul_f32 v176, v176, v80\n\t"
    "v_mfma_f32_16x16x32_bf16 a[104:107], a[156:159], a[200:203], a[104:107]\n\t"
    "v_mul_f32 v177, v177, v81\n\t"
    "v_mul_f32 v178, v178, v82\n\t"
    "v_mfma_f32_16x16x32_bf16 a[104:107], a[160:163], a[216:219], a[104:107]\n\t"
    "v_mul_f32 v179, v179, v83\n\t"
    "v_cvt_pk_bf16_f32 v162, v176, v177\n\t"
    "v_mfma_f32_16x16x32_bf16 a[104:107], a[164:167], a[232:235], a[104:107]\n\t"
    "v_cvt_pk_bf16_f32 v163, v178, v179\n\t"
    "s_add_u32 s35, s68, 1024\n\t"
    "v_mfma_f32_16x16x32_bf16 a[104:107], a[168:171], a[248:251], a[104:107]\n\t"
    "buffer_store_dwordx4 v[160:163], v248, s[84:87], s35 offen sc1\n\t"
    "v_accvgpr_read_b32 v84, a52\n\t"
    "v_mfma_f32_16x16x32_bf16 a[108:111], a[156:159], a[204:207], a[108:111]\n\t"
    "v_accvgpr_read_b32 v85, a53\n\t"
    "v_accvgpr_read_b32 v86, a54\n\t"
    "v_mfma_f32_16x16x32_bf16 a[108:111], a[160:163], a[220:223], a[108:111]\n\t"
    "v_accvgpr_read_b32 v87, a55\n\t"
    "v_max_f32 v180, 0, v180\n\t"
    "v_mfma_f32_16x16x32_bf16 a[108:111], a[164:167], a[236:239], a[108:111]\n\t"
    "v_max_f32 v181, 0, v181\n\t"
    "v_max_f32 v182, 0, v182\n\t"
    "v_mfma_f32_16x16x32_bf16 a[108:111], a[168:171], a[252:255], a[108:111]\n\t"
    "v_max_f32 v183, 0, v183\n\t"
    "v_mul_f32 v180, v180, v84\n\t"
    "s_nop 7\n\t"
    "v_mul_f32 v181, v181, v85\n\t"
    "v_mul_f32 v182, v182, v86\n\t"
    "v_mul_f32 v183, v183, v87\n\t"
    "v_cvt_pk_bf16_f32 v166, v180, v181\n\t"
    "v_cvt_pk_bf16_f32 v167, v182, v183\n\t"
    "s_add_u32 s35, s68, 115712\n\t"
    "buffer_store_dwordx4 v[164:167], v248, s[84:87], s35 offen sc1\n\t"
    "v_accvgpr_read_b32 v80, a56\n\t"
    "v_accvgpr_read_b32 v81, a57\n\t"
    "v_accvgpr_read_b32 v82, a58\n\t"
    "v_accvgpr_read_b32 v83, a59\n\t"
    "v_max_f32 v184, 0, v184\n\t"
    "v_max_f32 v185, 0, v185\n\t"
    "v_max_f32 v186, 0, v186\n\t"
    "v_max_f32 v187, 0, v187\n\t"
    "v_mul_f32 v184, v184, v80\n\t"
    "v_mul_f32 v185, v185, v81\n\t"
    "v_mul_f32 v186, v186, v82\n\t"
    "v_mul_f32 v187, v187, v83\n\t"
    "v_cvt_pk_bf16_f32 v170, v184, v185\n\t"
    "v_cvt_pk_bf16_f32 v171, v186, v187\n\t"
    "s_add_u32 s35, s68, 230400\n\t"
    "buffer_store_dwordx4 v[168:171], v248, s[84:87], s35 offen sc1\n\t"
    "v_accvgpr_read_b32 v84, a60\n\t"
    "v_accvgpr_read_b32 v85, a61\n\t"
    "v_accvgpr_read_b32 v86, a62\n\t"
    "v_accvgpr_read_b32 v87, a63\n\t"
    "v_max_f32 v188, 0, v188\n\t"
    "v_max_f32 v189, 0, v189\n\t"
    "v_max_f32 v190, 0, v190\n\t"
    "v_max_f32 v191, 0, v191\n\t"
    "v_mul_f32 v188, v188, v84\n\t"
    "v_mul_f32 v189, v189, v85\n\t"
    "v_mul_f32 v190, v190, v86\n\t"
    "v_mul_f32 v191, v191, v87\n\t"
    "v_cvt_pk_bf16_f32 v174, v188, v189\n\t"
    "v_cvt_pk_bf16_f32 v175, v190, v191\n\t"
    "s_add_u32 s35, s68, 345088\n\t"
    "buffer_store_dwordx4 v[172:175], v248, s[84:87], s35 offen sc1\n\t"
    "s_mov_b32 s58, 0\n\t"
    "s_mov_b32 s63, 0\n\t"
    "s_mov_b32 s56, 4294868992\n\t"
    "s_mov_b32 s57, 32768\n\t"
    "s_mov_b32 s59, 0\n\t"
    "s_mov_b32 s60, 65536\n\t"
    "s_mov_b32 s61, 0\n\t"
    "s_mov_b32 s64, 4294967266\n\t"
    "v_mov_b32 v96, 0x3f803f80\n\t"
    "v_mov_b32 v97, 0x3f803f80\n\t"
    "v_mov_b32 v98, 0x3f803f80\n\t"
    "v_mov_b32 v99, 0x3f803f80\n\t"
    "v_mov_b32 v160, s25\n\t"
    "v_mov_b32 v161, s25\n\t"
    "v_mov_b32 v162, s25\n\t"
    "v_mov_b32 v163, s25\n\t"
    "v_accvgpr_read_b32 v164, a184\n\t"
    "v_accvgpr_read_b32 v165, a185\n\t"
    "v_accvgpr_read_b32 v166, a186\n\t"
    "v_accvgpr_read_b32 v167, a187\n\t"
    "v_accvgpr_read_b32 v169, a188\n\t"
    "s_waitcnt vmcnt(16) lgkmcnt(0)\n\t"
    "s_barrier\n\t"
    "s_add_u32 m0, s60, s62\n\t"
    "v_accvgpr_read_b32 v80, a64\n\t"
    "v_accvgpr_read_b32 v81, a65\n\t"
    "v_accvgpr_read_b32 v82, a66\n\t"
    "ds_read_b128 a[128:131], v237 offset:0\n\t"
    "v_accvgpr_read_b32 v83, a67\n\t"
    "v_max_f32 v192, 0, v192\n\t"
    "v_max_f32 v193, 0, v193\n\t"
    "v_max_f32 v194, 0, v194\n\t"
    "v_max_f32 v195, 0, v195\n\t"
    "v_mul_f32 v192, v192, v80\n\t"
    "ds_read_b128 a[144:147], v237 offset:4096\n\t"
    "v_mul_f32 v193, v193, v81\n\t"
    "v_mul_f32 v194, v194, v82\n\t"
    "v_mul_f32 v195, v195, v83\n\t"
    "v_cvt_pk_bf16_f32 v192, v192, v193\n\t"
    "v_cvt_pk_bf16_f32 v193, v194, v195\n\t"
    "v_accvgpr_read_b32 v84, a68\n\t"
    "v_accvgpr_read_b32 v85, a69\n\t"
    "v_accvgpr_read_b32 v86, a70\n\t"
    "v_accvgpr_read_b32 v87, a71\n\t"
    "v_max_f32 v196, 0, v196\n\t"
    "v_max_f32 v197, 0, v197\n\t"
    "v_max_f32 v198, 0, v198\n\t"
    "v_max_f32 v199, 0, v199\n\t"
    "v_mul_f32 v196, v196, v84\n\t"
    "v_mul_f32 v197, v197, v85\n\t"
    "v_mul_f32 v198, v198, v86\n\t"
    "v_mul_f32 v199, v199, v87\n\t"
    "v_cvt_pk_bf16_f32 v196, v196, v197\n\t"
    "v_cvt_pk_bf16_f32 v197, v198, v199\n\t"
    "v_accvgpr_read_b32 v80, a72\n\t"
    "v_accvgpr_read_b32 v81, a73\n\t"
    "v_accvgpr_read_b32 v82, a74\n\t"
    "v_accvgpr_read_b32 v83, a75\n\t"
    "v_max_f32 v200, 0, v200\n\t"
    "v_max_f32 v201, 0, v201\n\t"
    "v_max_f32 v202, 0, v202\n\t"
    "v_max_f32 v203, 0, v203\n\t"
    "v_mul_f32 v200, v200, v80\n\t"
    "v_mul_f32 v201, v201, v81\n\t"
    "v_mul_f32 v202, v202, v82\n\t"
    "v_mul_f32 v203, v203, v83\n\t"
    "v_cvt_pk_bf16_f32 v200, v200, v201\n\t"
    "v_cvt_pk_bf16_f32 v201, v202, v203\n\t"
    "v_accvgpr_read_b32 v84, a76\n\t"
    "v_accvgpr_read_b32 v85, a77\n\t"
    "v_accvgpr_read_b32 v86, a78\n\t"
    "buffer_load_dwordx4 v242, s[48:51], s66 offen offset:0 lds\n\t"
    "v_accvgpr_read_b32 v87, a79\n\t"
    "v_max_f32 v204, 0, v204\n\t"
    "v_max_f32 v205, 0, v205\n\t"
    "v_max_f32 v206, 0, v206\n\t"
    "v_max_f32 v207, 0, v207\n\t"
    "v_mul_f32 v204, v204, v84\n\t"
    "v_mul_f32 v205, v205, v85\n\t"
    "v_mul_f32 v206, v206, v86\n\t"
    "v_mul_f32 v207, v207, v87\n\t"
    "ds_read_b128 a[132:135], v238 offset:0\n\t"
    "v_cvt_pk_bf16_f32 v204, v204, v205\n\t"
    "v_cvt_pk_bf16_f32 v205, v206, v207\n\t"
    "v_accvgpr_read_b32 v80, a80\n\t"
    "v_accvgpr_read_b32 v81, a81\n\t"
    "v_accvgpr_read_b32 v82, a82\n\t"
    "v_accvgpr_read_b32 v83, a83\n\t"
    "ds_read_b128 a[148:151], v238 offset:4096\n\t"
    "v_max_f32 v208, 0, v208\n\t"
    "v_max_f32 v209, 0, v209\n\t"
    "v_max_f32 v210, 0, v210\n\t"
    "v_max_f32 v211, 0, v211\n\t"
    "v_mul_f32 v208, v208, v80\n\t"
    "v_mul_f32 v209, v209, v81\n\t"
    "v_mul_f32 v210, v210, v82\n\t"
    "v_mul_f32 v211, v211, v83\n\t"
    "v_cvt_pk_bf16_f32 v194, v208, v209\n\t"
    "v_cvt_pk_bf16_f32 v195, v210, v211\n\t"
    "s_add_u32 s35, s68, 2048\n\t"
    "buffer_store_dwordx4 v[192:195], v248, s[84:87], s35 offen sc1\n\t"
    "v_accvgpr_read_b32 v84, a84\n\t"
    "v_accvgpr_read_b32 v85, a85\n\t"
    "v_accvgpr_read_b32 v86, a86\n\t"
    "v_accvgpr_read_b32 v87, a87\n\t"
    "v_max_f32 v212, 0, v212\n\t"
    "v_max_f32 v213, 0, v213\n\t"
    "v_max_f32 v214, 0, v214\n\t"
    "v_max_f32 v215, 0, v215\n\t"
    "v_mul_f32 v212, v212, v84\n\t"
    "v_mul_f32 v213, v213, v85\n\t"
    "v_mul_f32 v214, v214, v86\n\t"
    "v_mul_f32 v215, v215, v87\n\t"
    "v_cvt_pk_bf16_f32 v198, v212, v213\n\t"
    "v_cvt_pk_bf16_f32 v199, v214, v215\n\t"
    "s_add_u32 s35, s68, 116736\n\t"
    "buffer_store_dwordx4 v[196:199], v248, s[84:87], s35 offen sc1\n\t"
    "v_accvgpr_read_b32 v80, a88\n\t"
    "v_accvgpr_read_b32 v81, a89\n\t"
    "s_add_u32 s59, s59, 1\n\t"
    "v_accvgpr_read_b32 v82, a90\n\t"
    "v_accvgpr_read_b32 v83, a91\n\t"
    "v_max_f32 v216, 0, v216\n\t"
    "v_max_f32 v217, 0, v217\n\t"
    "v_max_f32 v218, 0, v218\n\t"
    "v_max_f32 v219, 0, v219\n\t"
    "buffer_load_dwordx4 v243, s[48:51], s66 offen offset:1024 lds\n\t"
    "v_mul_f32 v216, v216, v80\n\t"
    "v_mul_f32 v217, v217, v81\n\t"
    "v_mul_f32 v218, v218, v82\n\t"
    "v_mul_f32 v219, v219, v83\n\t"
    "v_cvt_pk_bf16_f32 v202, v216, v217\n\t"
    "v_cvt_pk_bf16_f32 v203, v218, v219\n\t"
    "buffer_load_dword v156, v253, s[92:95], s63 offen\n\t"
    "s_add_u32 s35, s68, 231424\n\t"
    "buffer_store_dwordx4 v[200:203], v248, s[84:87], s35 offen sc1\n\t"
    "v_accvgpr_read_b32 v84, a92\n\t"
    "ds_read_b128 a[136:139], v239 offset:0\n\t"
    "v_accvgpr_read_b32 v85, a93\n\t"
    "v_accvgpr_read_b32 v86, a94\n\t"
    "v_accvgpr_read_b32 v87, a95\n\t"
    "v_max_f32 v220, 0, v220\n\t"
    "v_max_f32 v221, 0, v221\n\t"
    "v_max_f32 v222, 0, v222\n\t"
    "ds_read_b128 a[152:155], v239 offset:4096\n\t"
    "v_max_f32 v223, 0, v223\n\t"
    "v_mul_f32 v220, v220, v84\n\t"
    "v_mul_f32 v221, v221, v85\n\t"
    "v_mul_f32 v222, v222, v86\n\t"
    "v_mul_f32 v223, v223, v87\n\t"
    "v_cvt_pk_bf16_f32 v206, v220, v221\n\t"
    "ds_read_b64_tr_b16 a[192:193], v228 offset:0\n\t"
    "v_cvt_pk_bf16_f32 v207, v222, v223\n\t"
    "s_add_u32 s35, s68, 346112\n\t"
    "buffer_store_dwordx4 v[204:207], v248, s[84:87], s35 offen sc1\n\t"
    "v_accvgpr_read_b32 v80, a96\n\t"
    "v_accvgpr_read_b32 v81, a97\n\t"
    "v_accvgpr_read_b32 v82, a98\n\t"
    "ds_read_b64_tr_b16 a[194:195], v228 offset:4096\n\t"
    "v_accvgpr_read_b32 v83, a99\n\t"
    "v_max_f32 v224, 0, v224\n\t"
    "v_max_f32 v225, 0, v225\n\t"
    "v_max_f32 v226, 0, v226\n\t"
    "v_max_f32 v227, 0, v227\n\t"
    "v_mul_f32 v224, v224, v80\n\t"
    "ds_read_b64_tr_b16 a[196:197], v229 offset:0\n\t"
    "v_mul_f32 v225, v225, v81\n\t"
    "v_mul_f32 v226, v226, v82\n\t"
    "v_mul_f32 v227, v227, v83\n\t"
    "v_cvt_pk_bf16_f32 v224, v224, v225\n\t"
    "v_cvt_pk_bf16_f32 v225, v226, v227\n\t"
    "s_add_u32 s35, s69, 0\n\t"
    "ds_read_b64_tr_b16 a[198:199], v229 offset:4096\n\t"
    "buffer_store_dwordx2 v[224:225], v248, s[84:87], s35 offen sc1\n\t"
    "v_accvgpr_read_b32 v92, a112\n\t"
    "v_accvgpr_read_b32 v93, a113\n\t"
    "v_accvgpr_read_b32 v94, a114\n\t"
    "v_accvgpr_read_b32 v95, a115\n\t"
    "v_accvgpr_read_b32 v84, a100\n\t"
    "v_accvgpr_read_b32 v85, a101\n\t"
    "v_accvgpr_read_b32 v86, a102\n\t"
    "v_accvgpr_read_b32 v87, a103\n\t"
    "v_max_f32 v92, 0, v92\n\t"
    "v_max_f32 v93, 0, v93\n\t"
    "v_max_f32 v94, 0, v94\n\t"
    "buffer_load_dwordx4 v244, s[48:51], s66 offen offset:2048 lds\n\t"
    "s_and_b32 s65, s59, 3\n\t"
    "s_cselect_b32 s61, s57, s56\n\t"
    "v_max_f32 v95, 0, v95\n\t"
    "v_mul_f32 v92, v92, v84\n\t"
    "v_mul_f32 v93, v93, v85\n\t"
    "v_mul_f32 v94, v94, v86\n\t"
    "v_mul_f32 v95, v95, v87\n\t"
    "v_cvt_pk_bf16_f32 v92, v92, v93\n\t"
    "v_cvt_pk_bf16_f32 v93, v94, v95\n\t"
    "s_add_u32 s35, s69, 114688\n\t"
    "buffer_store_dwordx2 v[92:93], v248, s[84:87], s35 offen sc1\n\t"
    "ds_read_b128 a[140:143], v240 offset:0\n\t"
    "v_accvgpr_read_b32 v88, a116\n\t"
    "v_accvgpr_read_b32 v89, a117\n\t"
    "v_accvgpr_read_b32 v90, a118\n\t"
    "v_accvgpr_read_b32 v91, a119\n\t"
    "v_accvgpr_read_b32 v80, a104\n\t"
    "v_accvgpr_read_b32 v81, a105\n\t"
    "ds_read_b128 a[156:159], v240 offset:4096\n\t"
    "v_accvgpr_read_b32 v82, a106\n\t"
    "v_accvgpr_read_b32 v83, a107\n\t"
    "v_max_f32 v88, 0, v88\n\t"
    "v_max_f32 v89, 0, v89\n\t"
    "v_max_f32 v90, 0, v90\n\t"
    "v_max_f32 v91, 0, v91\n\t"
    "ds_read_b64_tr_b16 a[200:201], v230 offset:0\n\t"
    "v_mul_f32 v88, v88, v80\n\t"
    "v_mul_f32 v89, v89, v81\n\t"
    "v_mul_f32 v90, v90, v82\n\t"
    "v_mul_f32 v91, v91, v83\n\t"
    "v_cvt_pk_bf16_f32 v88, v88, v89\n\t"
    "v_cvt_pk_bf16_f32 v89, v90, v91\n\t"
    "ds_read_b64_tr_b16 a[202:203], v230 offset:4096\n\t"
    "s_add_u32 s35, s69, 229376\n\t"
    "buffer_store_dwordx2 v[88:89], v248, s[84:87], s35 offen sc1\n\t"
    "v_accvgpr_read_b32 v92, a120\n\t"
    "v_accvgpr_read_b32 v93, a121\n\t"
    "v_accvgpr_read_b32 v94, a122\n\t"
    "v_accvgpr_read_b32 v95, a123\n\t"
    "ds_read_b64_tr_b16 a[204:205], v231 offset:0\n\t"
    "v_accvgpr_read_b32 v84, a108\n\t"
    "v_accvgpr_read_b32 v85, a109\n\t"
    "v_accvgpr_read_b32 v86, a110\n\t"
    "v_accvgpr_read_b32 v87, a111\n\t"
    "v_max_f32 v92, 0, v92\n\t"
    "v_max_f32 v93, 0, v93\n\t"
    "ds_read_b64_tr_b16 a[206:207], v231 offset:4096\n\t"
    "v_max_f32 v94, 0, v94\n\t"
    "v_max_f32 v95, 0, v95\n\t"
    "v_mul_f32 v92, v92, v84\n\t"
    "v_mul_f32 v93, v93, v85\n\t"
    "v_mul_f32 v94, v94, v86\n\t"
    "v_mul_f32 v95, v95, v87\n\t"
    "v_cvt_pk_bf16_f32 v92, v92, v93\n\t"
    "v_cvt_pk_bf16_f32 v93, v94, v95\n\t"
    "s_add_u32 s35, s69, 344064\n\t"
    "buffer_store_dwordx2 v[92:93], v248, s[84:87], s35 offen sc1\n\t"
    "buffer_load_dwordx4 v245, s[48:51], s66 offen offset:3072 lds\n\t"
    "s_add_u32 m0, m0, 16384\n\t"
    "s_waitcnt lgkmcnt(4)\n\t"
    "v_mfma_f32_16x16x32_bf16 v[100:103], a[128:131], v[0:3], 0\n\t"
    "v_mfma_f32_16x16x32_bf16 v[100:103], a[132:135], v[4:7], v[100:103]\n\t"
    "v_mfma_f32_16x16x32_bf16 v[100:103], a[136:139], v[8:11], v[100:103]\n\t"
    "v_mfma_f32_16x16x32_bf16 v[100:103], a[140:143], v[12:15], v[100:103]\n\t"
    "v_mfma_f32_16x16x32_bf16 v[104:107], a[144:147], v[0:3], 0\n\t"
    "v_mfma_f32_16x16x32_bf16 v[104:107], a[148:151], v[4:7], v[104:107]\n\t"
    "v_mfma_f32_16x16x32_bf16 v[104:107], a[152:155], v[8:11], v[104:107]\n\t"
    "v_mfma_f32_16x16x32_bf16 v[104:107], a[156:159], v[12:15], v[104:107]\n\t"
    "v_mfma_f32_16x16x32_bf16 v[108:111], a[128:131], v[16:19], 0\n\t"
    "v_mfma_f32_16x16x32_bf16 v[108:111], a[132:135], v[20:23], v[108:111]\n\t"
    "v_mfma_f32_16x16x32_bf16 v[108:111], a[136:139], v[24:27], v[108:111]\n\t"
    "v_mfma_f32_16x16x32_bf16 v[108:111], a[140:143], v[28:31], v[108:111]\n\t"
    "v_mfma_f32_16x16x32_bf16 v[112:115], a[144:147], v[16:19], 0\n\t"
    "v_mfma_f32_16x16x32_bf16 v[112:115], a[148:151], v[20:23], v[112:115]\n\t"
    "v_mfma_f32_16x16x32_bf16 v[112:115], a[152:155], v[24:27], v[112:115]\n\t"
    "v_mfma_f32_16x16x32_bf16 v[112:115], a[156:159], v[28:31], v[112:115]\n\t"
    "v_mfma_f32_16x16x32_bf16 v[116:119], a[128:131], v[32:35], 0\n\t"
    "v_mfma_f32_16x16x32_bf16 v[116:119], a[132:135], v[36:39], v[116:119]\n\t"
    "v_mfma_f32_16x16x32_bf16 v[116:119], a[136:139], v[40:43], v[116:119]\n\t"
    "v_mfma_f32_16x16x32_bf16 v[116:119], a[140:143], v[44:47], v[116:119]\n\t"
    "v_mfma_f32_16x16x32_bf16 v[120:123], a[144:147], v[32:35], 0\n\t"
    "v_mfma_f32_16x16x32_bf16 v[120:123], a[148:151], v[36:39], v[120:123]\n\t"
    "v_mfma_f32_16x16x32_bf16 v[120:123], a[152:155], v[40:43], v[120:123]\n\t"
    "v_mfma_f32_16x16x32_bf16 v[120:123], a[156:159], v[44:47], v[120:123]\n\t"
    "v_mfma_f32_16x16x32_bf16 v[124:127], a[128:131], v[48:51], 0\n\t"
    "v_mfma_f32_16x16x32_bf16 v[124:127], a[132:135], v[52:55], v[124:127]\n\t"
    "v_mfma_f32_16x16x32_bf16 v[124:127], a[136:139], v[56:59], v[124:127]\n\t"
    "v_mfma_f32_16x16x32_bf16 v[124:127], a[140:143], v[60:63], v[124:127]\n\t"
    "v_mfma_f32_16x16x32_bf16 v[128:131], a[144:147], v[48:51], 0\n\t"
    "v_mfma_f32_16x16x32_bf16 v[128:131], a[148:151], v[52:55], v[128:131]\n\t"
    "v_mfma_f32_16x16x32_bf16 v[128:131], a[152:155], v[56:59], v[128:131]\n\t"
    "v_mfma_f32_16x16x32_bf16 v[128:131], a[156:159], v[60:63], v[128:131]\n\t"
    "s_nop 9\n\t"
    "v_max3_f32 v148, v100, v101, v102\n\t"
    "v_max3_f32 v148, v148, v103, v104\n\t"
    "v_max3_f32 v148, v148, v105, v106\n\t"
    "v_max_f32 v148, v148, v107\n\t"
    "v_mov_b32 v149, v148\n\t"
    "v_max3_f32 v150, v108, v109, v110\n\t"
    "v_max3_f32 v150, v150, v111, v112\n\t"
    "v_max3_f32 v150, v150, v113, v114\n\t"
    "v_max_f32 v150, v150, v115\n\t"
    "v_mov_b32 v151, v150\n\t"
    "v_max3_f32 v152, v116, v117, v118\n\t"
    "v_max3_f32 v152, v152, v119, v120\n\t"
    "v_max3_f32 v152, v152, v121, v122\n\t"
    "v_max_f32 v152, v152, v123\n\t"
    "v_mov_b32 v153, v152\n\t"
    "v_max3_f32 v154, v124, v125, v126\n\t"
    "v_max3_f32 v154, v154, v127, v128\n\t"
    "v_max3_f32 v154, v154, v129, v130\n\t"
    "v_max_f32 v154, v154, v131\n\t"
    "v_mov_b32 v155, v154\n\t"
    "s_nop 1\n\t"
    "v_permlane16_swap_b32 v148, v149\n\t"
    "v_permlane16_swap_b32 v150, v151\n\t"
    "v_permlane16_swap_b32 v152, v153\n\t"
    "v_permlane16_swap_b32 v154, v155\n\t"
    "s_nop 1\n\t"
    "v_max_f32 v148, v148, v149\n\t"
    "v_mov_b32 v149, v148\n\t"
    "v_max_f32 v150, v150, v151\n\t"
    "v_mov_b32 v151, v150\n\t"
    "v_max_f32 v152, v152, v153\n\t"
    "v_mov_b32 v153, v152\n\t"
    "v_max_f32 v154, v154, v155\n\t"
    "v_mov_b32 v155, v154\n\t"
    "s_nop 1\n\t"
    "v_permlane32_swap_b32 v148, v149\n\t"
    "v_permlane32_swap_b32 v150, v151\n\t"
    "v_permlane32_swap_b32 v152, v153\n\t"
    "v_permlane32_swap_b32 v154, v155\n\t"
    "s_nop 1\n\t"
    "v_max_f32 v148, v148, v149\n\t"
    "v_xor_b32 v64, 0x80000000, v148\n\t"
    "v_xor_b32 v65, 0x80000000, v148\n\t"
    "v_xor_b32 v66, 0x80000000, v148\n\t"
    "v_xor_b32 v67, 0x80000000, v148\n\t"
    "v_max_f32 v150, v150, v151\n\t"
    "v_xor_b32 v68, 0x80000000, v150\n\t"
    "v_xor_b32 v69, 0x80000000, v150\n\t"
    "v_xor_b32 v70, 0x80000000, v150\n\t"
    "v_xor_b32 v71, 0x80000000, v150\n\t"
    "v_max_f32 v152, v152, v153\n\t"
    "v_xor_b32 v72, 0x80000000, v152\n\t"
    "v_xor_b32 v73, 0x80000000, v152\n\t"
    "v_xor_b32 v74, 0x80000000, v152\n\t"
    "v_xor_b32 v75, 0x80000000, v152\n\t"
    "v_max_f32 v154, v154, v155\n\t"
    "v_xor_b32 v76, 0x80000000, v154\n\t"
    "v_xor_b32 v77, 0x80000000, v154\n\t"
    "v_xor_b32 v78, 0x80000000, v154\n\t"
    "v_xor_b32 v79, 0x80000000, v154\n\t"
    "s_nop 1\n\t"
    "v_mfma_f32_16x16x32_bf16 v[100:103], a[128:131], v[0:3], v[64:67]\n\t"
    "ds_read_b128 a[160:163], v237 offset:8192\n\t"
    "v_mfma_f32_16x16x32_bf16 v[100:103], a[132:135], v[4:7], v[100:103]\n\t"
    "v_mfma_f32_16x16x32_bf16 v[100:103], a[136:139], v[8:11], v[100:103]\n\t"
    "ds_read_b128 a[176:179], v237 offset:12288\n\t"
    "v_add_u32 v237, s61, v237\n\t"
    "v_mfma_f32_16x16x32_bf16 v[100:103], a[140:143], v[12:15], v[100:103]\n\t"
    "v_mfma_f32_16x16x32_bf16 v[104:107], a[144:147], v[0:3], v[64:67]\n\t"
    "ds_read_b64_tr_b16 a[208:209], v232 offset:0\n\t"
    "v_mfma_f32_16x16x32_bf16 v[104:107], a[148:151], v[4:7], v[104:107]\n\t"
    "v_mfma_f32_16x16x32_bf16 v[104:107], a[152:155], v[8:11], v[104:107]\n\t"
    "ds_read_b64_tr_b16 a[210:211], v232 offset:4096\n\t"
    "v_mfma_f32_16x16x32_bf16 v[104:107], a[156:159], v[12:15], v[104:107]\n\t"
    "s_nop 1\n\t"
    "ds_read_b64_tr_b16 a[212:213], v233 offset:0\n\t"
    "s_nop 1\n\t"
    "s_nop 1\n\t"
    "ds_read_b64_tr_b16 a[214:215], v233 offset:4096\n\t"
    "s_nop 1\n\t"
    "s_nop 1\n\t"
    "s_nop 1\n\t"
    "s_nop 1\n\t"
    "buffer_load_dwordx4 v246, s[52:55], s66 offen offset:0 lds\n\t"
    "s_nop 1\n\t"
    "s_nop 1\n\t"
    "v_mfma_f32_16x16x32_bf16 v[108:111], a[128:131], v[16:19], v[68:71]\n\t"
    "ds_read_b128 a[164:167], v238 offset:8192\n\t"
    "v_mfma_f32_16x16x32_bf16 v[108:111], a[132:135], v[20:23], v[108:111]\n\t"
    "v_exp_f32 v100, v100\n\t"
    "v_mfma_f32_16x16x32_bf16 v[108:111], a[136:139], v[24:27], v[108:111]\n\t"
    "ds_read_b128 a[180:183], v238 offset:12288\n\t"
    "v_add_u32 v238, s61, v238\n\t"
    "v_mfma_f32_16x16x32_bf16 v[108:111], a[140:143], v[28:31], v[108:111]\n\t"
    "v_exp_f32 v101, v101\n\t"
    "v_mfma_f32_16x16x32_bf16 v[112:115], a[144:147], v[16:19], v[68:71]\n\t"
    "ds_read_b64_tr_b16 a[216:217], v234 offset:0\n\t"
    "v_cvt_pk_bf16_f32 v132, v100, v101\n\t"
    "v_mfma_f32_16x16x32_bf16 v[112:115], a[148:151], v[20:23], v[112:115]\n\t"
    "v_exp_f32 v102, v102\n\t"
    "v_mfma_f32_16x16x32_bf16 v[112:115], a[152:155], v[24:27], v[112:115]\n\t"
    "ds_read_b64_tr_b16 a[218:219], v234 offset:4096\n\t"
    "v_mfma_f32_16x16x32_bf16 v[112:115], a[156:159], v[28:31], v[112:115]\n\t"
    "v_exp_f32 v103, v103\n\t"
    "s_nop 1\n\t"
    "ds_read_b64_tr_b16 a[220:221], v235 offset:0\n\t"
    "v_cvt_pk_bf16_f32 v133, v102, v103\n\t"
    "s_nop 1\n\t"
    "v_exp_f32 v104, v104\n\t"
    "s_nop 1\n\t"
    "ds_read_b64_tr_b16 a[222:223], v235 offset:4096\n\t"
    "s_nop 1\n\t"
    "v_exp_f32 v105, v105\n\t"
    "s_nop 1\n\t"
    "s_add_u32 s63, s63, 224\n\t"
    "v_cvt_pk_bf16_f32 v134, v104, v105\n\t"
    "s_nop 1\n\t"
    "v_exp_f32 v106, v106\n\t"
    "s_nop 1\n\t"
    "buffer_load_dwordx4 v247, s[52:55], s66 offen offset:1024 lds\n\t"
    "s_nop 1\n\t"
    "v_exp_f32 v107, v107\n\t"
    "s_nop 1\n\t"
    "buffer_load_dword v156, v252, s[88:91], s58 offen\n\t"
    "v_cvt_pk_bf16_f32 v135, v106, v107\n\t"
    "s_nop 3\n\t"
    "v_mfma_f32_16x16x32_bf16 v[116:119], a[128:131], v[32:35], v[72:75]\n\t"
    "ds_read_b128 a[168:171], v239 offset:8192\n\t"
    "v_mfma_f32_16x16x32_bf16 v[116:119], a[132:135], v[36:39], v[116:119]\n\t"
    "v_exp_f32 v108, v108\n\t"
    "v_mfma_f32_16x16x32_bf16 v[116:119], a[136:139], v[40:43], v[116:119]\n\t"
    "ds_read_b128 a[184:187], v239 offset:12288\n\t"
    "v_add_u32 v239, s61, v239\n\t"
    "v_mfma_f32_16x16x32_bf16 v[116:119], a[140:143], v[44:47], v[116:119]\n\t"
    "v_exp_f32 v109, v109\n\t"
    "v_mfma_f32_16x16x32_bf16 v[120:123], a[144:147], v[32:35], v[72:75]\n\t"
    "ds_read_b64_tr_b16 a[224:225], v228 offset:8192\n\t"
    "v_cvt_pk_bf16_f32 v136, v108, v109\n\t"
    "v_mfma_f32_16x16x32_bf16 v[120:123], a[148:151], v[36:39], v[120:123]\n\t"
    "v_exp_f32 v110, v110\n\t"
    "v_mfma_f32_16x16x32_bf16 v[120:123], a[152:155], v[40:43], v[120:123]\n\t"
    "ds_read_b64_tr_b16 a[226:227], v228 offset:12288\n\t"
    "v_add_u32 v228, s61, v228\n\t"
    "v_mfma_f32_16x16x32_bf16 v[120:123], a[156:159], v[44:47], v[120:123]\n\t"
    "s_waitcnt lgkmcnt(4)\n\t"
    "v_exp_f32 v111, v111\n\t"
    "v_mfma_f32_16x16x32_bf16 a[0:3], a[192:195], v[132:135], 0\n\t"
    "ds_read_b64_tr_b16 a[228:229], v229 offset:8192\n\t"
    "v_cvt_pk_bf16_f32 v137, v110, v111\n\t"
    "v_mfma_f32_16x16x32_bf16 a[16:19], a[196:199], v[132:135], 0\n\t"
    "v_exp_f32 v112, v112\n\t"
    "v_mfma_f32_16x16x32_bf16 a[32:35], a[200:203], v[132:135], 0\n\t"
    "ds_read_b64_tr_b16 a[230:231], v229 offset:12288\n\t"
    "v_add_u32 v229, s61, v229\n\t"
    "v_mfma_f32_16x16x32_bf16 a[48:51], a[204:207], v[132:135], 0\n\t"
    "v_exp_f32 v113, v113\n\t"
    "v_mfma_f32_16x16x32_bf16 a[64:67], a[208:211], v[132:135], 0\n\t"
    "s_add_u32 s58, s58, 256\n\t"
    "v_cvt_pk_bf16_f32 v138, v112, v113\n\t"
    "v_mfma_f32_16x16x32_bf16 a[80:83], a[212:215], v[132:135], 0\n\t"
    "v_exp_f32 v114, v114\n\t"
    "v_mfma_f32_16x16x32_bf16 a[96:99], a[216:219], v[132:135], 0\n\t"
    "buffer_load_dwordx4 v246, s[52:55], s66 offen offset:2048 lds\n\t"
    "s_add_u32 s60, s60, 32768\n\t"
    "v_mfma_f32_16x16x32_bf16 a[112:115], a[220:223], v[132:135], 0\n\t"
    "v_exp_f32 v115, v115\n\t"
    "v_mfma_f32_16x16x32_bf16 v[80:83], v[96:99], v[132:135], 0\n\t"
    "s_and_b32 s60, s60, 0x1ffff\n\t"
    "v_cvt_pk_bf16_f32 v139, v114, v115\n\t"
    "v_mfma_f32_16x16x32_bf16 v[124:127], a[128:131], v[48:51], v[76:79]\n\t"
    "ds_read_b128 a[172:175], v240 offset:8192\n\t"
    "v_mfma_f32_16x16x32_bf16 v[124:127], a[132:135], v[52:55], v[124:127]\n\t"
    "v_exp_f32 v116, v116\n\t"
    "v_mfma_f32_16x16x32_bf16 v[124:127], a[136:139], v[56:59], v[124:127]\n\t"
    "ds_read_b128 a[188:191], v240 offset:12288\n\t"
    "v_add_u32 v240, s61, v240\n\t"
    "v_mfma_f32_16x16x32_bf16 v[124:127], a[140:143], v[60:63], v[124:127]\n\t"
    "v_exp_f32 v117, v117\n\t"
    "v_mfma_f32_16x16x32_bf16 v[128:131], a[144:147], v[48:51], v[76:79]\n\t"
    "ds_read_b64_tr_b16 a[232:233], v230 offset:8192\n\t"
    "v_cvt_pk_bf16_f32 v140, v116, v117\n\t"
    "v_mfma_f32_16x16x32_bf16 v[128:131], a[148:151], v[52:55], v[128:131]\n\t"
    "v_exp_f32 v118, v118\n\t"
    "v_mfma_f32_16x16x32_bf16 v[128:131], a[152:155], v[56:59], v[128:131]\n\t"
    "ds_read_b64_tr_b16 a[234:235], v230 offset:12288\n\t"
    "v_add_u32 v230, s61, v230\n\t"
    "v_mfma_f32_16x16x32_bf16 v[128:131], a[156:159], v[60:63], v[128:131]\n\t"
    "v_exp_f32 v119, v119\n\t"
    "v_mfma_f32_16x16x32_bf16 a[4:7], a[192:195], v[136:139], 0\n\t"
    "ds_read_b64_tr_b16 a[236:237], v231 offset:8192\n\t"
    "v_cvt_pk_bf16_f32 v141, v118, v119\n\t"
    "v_mfma_f32_16x16x32_bf16 a[20:23], a[196:199], v[136:139], 0\n\t"
    "v_exp_f32 v120, v120\n\t"
    "v_mfma_f32_16x16x32_bf16 a[36:39], a[200:203], v[136:139], 0\n\t"
    "ds_read_b64_tr_b16 a[238:239], v231 offset:12288\n\t"
    "v_add_u32 v231, s61, v231\n\t"
    "v_mfma_f32_16x16x32_bf16 a[52:55], a[204:207], v[136:139], 0\n\t"
    "v_exp_f32 v121, v121\n\t"
    "v_mfma_f32_16x16x32_bf16 a[68:71], a[208:211], v[136:139], 0\n\t"
    "s_waitcnt vmcnt(25)\n\t"
    "s_barrier\n\t"
    "v_cvt_pk_bf16_f32 v142, v120, v121\n\t"
    "v_mfma_f32_16x16x32_bf16 a[84:87], a[212:215], v[136:139], 0\n\t"
    "v_exp_f32 v122, v122\n\t"
    "v_mfma_f32_16x16x32_bf16 a[100:103], a[216:219], v[136:139], 0\n\t"
    "buffer_load_dwordx4 v247, s[52:55], s66 offen offset:3072 lds\n\t"
    "s_add_u32 s66, s66, 16384\n\t"
    "s_add_u32 m0, s60, s62\n\t"
    "v_mfma_f32_16x16x32_bf16 a[116:119], a[220:223], v[136:139], 0\n\t"
    "v_exp_f32 v123, v123\n\t"
    "v_mfma_f32_16x16x32_bf16 v[84:87], v[96:99], v[136:139], 0\n\t"
    "s_waitcnt lgkmcnt(4)\n\t"
    "v_cvt_pk_bf16_f32 v143, v122, v123\n\t"
    "v_mfma_f32_16x16x32_bf16 v[100:103], a[160:163], v[0:3], v[64:67]\n\t"
    "ds_read_b128 a[128:131], v237 offset:0\n\t"
    "v_mfma_f32_16x16x32_bf16 v[100:103], a[164:167], v[4:7], v[100:103]\n\t"
    "v_exp_f32 v124, v124\n\t"
    "v_mfma_f32_16x16x32_bf16 v[100:103], a[168:171], v[8:11], v[100:103]\n\t"
    "ds_read_b128 a[144:147], v237 offset:4096\n\t"
    "v_mfma_f32_16x16x32_bf16 v[100:103], a[172:175], v[12:15], v[100:103]\n\t"
    "v_exp_f32 v125, v125\n\t"
    "v_mfma_f32_16x16x32_bf16 v[104:107], a[176:179], v[0:3], v[64:67]\n\t"
    "ds_read_b64_tr_b16 a[240:241], v232 offset:8192\n\t"
    "v_cvt_pk_bf16_f32 v144, v124, v125\n\t"
    "v_mfma_f32_16x16x32_bf16 v[104:107], a[180:183], v[4:7], v[104:107]\n\t"
    "v_exp_f32 v126, v126\n\t"
    "v_mfma_f32_16x16x32_bf16 v[104:107], a[184:187], v[8:11], v[104:107]\n\t"
    "ds_read_b64_tr_b16 a[242:243], v232 offset:12288\n\t"
    "v_add_u32 v232, s61, v232\n\t"
    "v_mfma_f32_16x16x32_bf16 v[104:107], a[188:191], v[12:15], v[104:107]\n\t"
    "v_exp_f32 v127, v127\n\t"
    "v_mfma_f32_16x16x32_bf16 a[8:11], a[192:195], v[140:143], 0\n\t"
    "ds_read_b64_tr_b16 a[244:245], v233 offset:8192\n\t"
    "v_cvt_pk_bf16_f32 v145, v126, v127\n\t"
    "v_mfma_f32_16x16x32_bf16 a[24:27], a[196:199], v[140:143], 0\n\t"
    "v_exp_f32 v128, v128\n\t"
    "v_mfma_f32_16x16x32_bf16 a[40:43], a[200:203], v[140:143], 0\n\t"
    "ds_read_b64_tr_b16 a[246:247], v233 offset:12288\n\t"
    "v_add_u32 v233, s61, v233\n\t"
    "v_mfma_f32_16x16x32_bf16 a[56:59], a[204:207], v[140:143], 0\n\t"
    "v_exp_f32 v129, v129\n\t"
    "v_mfma_f32_16x16x32_bf16 a[72:75], a[208:211], v[140:143], 0\n\t"
    "v_cvt_pk_bf16_f32 v146, v128, v129\n\t"
    "v_mfma_f32_16x16x32_bf16 a[88:91], a[212:215], v[140:143], 0\n\t"
    "v_exp_f32 v130, v130\n\t"
    "v_mfma_f32_16x16x32_bf16 a[104:107], a[216:219], v[140:143], 0\n\t"
    "buffer_load_dwordx4 v242, s[48:51], s66 offen offset:0 lds\n\t"
    "v_mfma_f32_16x16x32_bf16 a[120:123], a[220:223], v[140:143], 0\n\t"
    "v_exp_f32 v131, v131\n\t"
    "v_mfma_f32_16x16x32_bf16 v[88:91], v[96:99], v[140:143], 0\n\t"
    "v_cvt_pk_bf16_f32 v147, v130, v131\n\t"
    "v_mfma_f32_16x16x32_bf16 v[108:111], a[160:163], v[16:19], v[68:71]\n\t"
    "ds_read_b128 a[132:135], v238 offset:0\n\t"
    "v_mfma_f32_16x16x32_bf16 v[108:111], a[164:167], v[20:23], v[108:111]\n\t"
    "v_exp_f32 v100, v100\n\t"
    "v_mfma_f32_16x16x32_bf16 v[108:111], a[168:171], v[24:27], v[108:111]\n\t"
    "ds_read_b128 a[148:151], v238 offset:4096\n\t"
    "v_mfma_f32_16x16x32_bf16 v[108:111], a[172:175], v[28:31], v[108:111]\n\t"
    "v_exp_f32 v101, v101\n\t"
    "v_mfma_f32_16x16x32_bf16 v[112:115], a[176:179], v[16:19], v[68:71]\n\t"
    "ds_read_b64_tr_b16 a[248:249], v234 offset:8192\n\t"
    "v_cvt_pk_bf16_f32 v132, v100, v101\n\t"
    "v_mfma_f32_16x16x32_bf16 v[112:115], a[180:183], v[20:23], v[112:115]\n\t"
    "v_exp_f32 v102, v102\n\t"
    "v_mfma_f32_16x16x32_bf16 v[112:115], a[184:187], v[24:27], v[112:115]\n\t"
    "ds_read_b64_tr_b16 a[250:251], v234 offset:12288\n\t"
    "v_add_u32 v234, s61, v234\n\t"
    "v_mfma_f32_16x16x32_bf16 v[112:115], a[188:191], v[28:31], v[112:115]\n\t"
    "v_exp_f32 v103, v103\n\t"
    "v_mfma_f32_16x16x32_bf16 a[12:15], a[192:195], v[144:147], 0\n\t"
    "ds_read_b64_tr_b16 a[252:253], v235 offset:8192\n\t"
    "v_cvt_pk_bf16_f32 v133, v102, v103\n\t"
    "v_mfma_f32_16x16x32_bf16 a[28:31], a[196:199], v[144:147], 0\n\t"
    "v_exp_f32 v104, v104\n\t"
    "v_mfma_f32_16x16x32_bf16 a[44:47], a[200:203], v[144:147], 0\n\t"
    "ds_read_b64_tr_b16 a[254:255], v235 offset:12288\n\t"
    "v_add_u32 v235, s61, v235\n\t"
    "v_mfma_f32_16x16x32_bf16 a[60:63], a[204:207], v[144:147], 0\n\t"
    "v_exp_f32 v105, v105\n\t"
    "v_mfma_f32_16x16x32_bf16 a[76:79], a[208:211], v[144:147], 0\n\t"
    "s_add_u32 s59, s59, 1\n\t"
    "v_cvt_pk_bf16_f32 v134, v104, v105\n\t"
    "v_mfma_f32_16x16x32_bf16 a[92:95], a[212:215], v[144:147], 0\n\t"
    "v_exp_f32 v106, v106\n\t"
    "v_mfma_f32_16x16x32_bf16 a[108:111], a[216:219], v[144:147], 0\n\t"
    "buffer_load_dwordx4 v243, s[48:51], s66 offen offset:1024 lds\n\t"
    "v_mfma_f32_16x16x32_bf16 a[124:127], a[220:223], v[144:147], 0\n\t"
    "v_exp_f32 v107, v107\n\t"
    "v_mfma_f32_16x16x32_bf16 v[92:95], v[96:99], v[144:147], 0\n\t"
    "buffer_load_dword v156, v253, s[92:95], s63 offen\n\t"
    "v_cvt_pk_bf16_f32 v135, v106, v107\n\t"
    "v_mfma_f32_16x16x32_bf16 v[116:119], a[160:163], v[32:35], v[72:75]\n\t"
    "ds_read_b128 a[136:139], v239 offset:0\n\t"
    "v_mfma_f32_16x16x32_bf16 v[116:119], a[164:167], v[36:39], v[116:119]\n\t"
    "v_exp_f32 v108, v108\n\t"
    "v_mfma_f32_16x16x32_bf16 v[116:119], a[168:171], v[40:43], v[116:119]\n\t"
    "ds_read_b128 a[152:155], v239 offset:4096\n\t"
    "v_mfma_f32_16x16x32_bf16 v[116:119], a[172:175], v[44:47], v[116:119]\n\t"
    "v_exp_f32 v109, v109\n\t"
    "v_mfma_f32_16x16x32_bf16 v[120:123], a[176:179], v[32:35], v[72:75]\n\t"
    "ds_read_b64_tr_b16 a[192:193], v228 offset:0\n\t"
    "v_cvt_pk_bf16_f32 v136, v108, v109\n\t"
    "v_mfma_f32_16x16x32_bf16 v[120:123], a[180:183], v[36:39], v[120:123]\n\t"
    "v_exp_f32 v110, v110\n\t"
    "v_mfma_f32_16x16x32_bf16 v[120:123], a[184:187], v[40:43], v[120:123]\n\t"
    "ds_read_b64_tr_b16 a[194:195], v228 offset:4096\n\t"
    "v_mfma_f32_16x16x32_bf16 v[120:123], a[188:191], v[44:47], v[120:123]\n\t"
    "s_waitcnt lgkmcnt(4)\n\t"
    "v_exp_f32 v111, v111\n\t"
    "v_mfma_f32_16x16x32_bf16 a[0:3], a[224:227], v[132:135], a[0:3]\n\t"
    "ds_read_b64_tr_b16 a[196:197], v229 offset:0\n\t"
    "v_cvt_pk_bf16_f32 v137, v110, v111\n\t"
    "v_mfma_f32_16x16x32_bf16 a[16:19], a[228:231], v[132:135], a[16:19]\n\t"
    "v_exp_f32 v112, v112\n\t"
    "v_mfma_f32_16x16x32_bf16 a[32:35], a[232:235], v[132:135], a[32:35]\n\t"
    "ds_read_b64_tr_b16 a[198:199], v229 offset:4096\n\t"
    "v_mfma_f32_16x16x32_bf16 a[48:51], a[236:239], v[132:135], a[48:51]\n\t"
    "v_exp_f32 v113, v113\n\t"
    "v_mfma_f32_16x16x32_bf16 a[64:67], a[240:243], v[132:135], a[64:67]\n\t"
    "v_cvt_pk_bf16_f32 v138, v112, v113\n\t"
    "v_mfma_f32_16x16x32_bf16 a[80:83], a[244:247], v[132:135], a[80:83]\n\t"
    "v_exp_f32 v114, v114\n\t"
    "v_mfma_f32_16x16x32_bf16 a[96:99], a[248:251], v[132:135], a[96:99]\n\t"
    "buffer_load_dwordx4 v244, s[48:51], s66 offen offset:2048 lds\n\t"
    "s_and_b32 s65, s59, 3\n\t"
    "s_cselect_b32 s61, s57, s56\n\t"
    "v_mfma_f32_16x16x32_bf16 a[112:115], a[252:255], v[132:135], a[112:115]\n\t"
    "v_exp_f32 v115, v115\n\t"
    "v_mfma_f32_16x16x32_bf16 v[80:83], v[96:99], v[132:135], v[80:83]\n\t"
    "v_cvt_pk_bf16_f32 v139, v114, v115\n\t"
    "v_mfma_f32_16x16x32_bf16 v[124:127], a[160:163], v[48:51], v[76:79]\n\t"
    "ds_read_b128 a[140:143], v240 offset:0\n\t"
    "v_mfma_f32_16x16x32_bf16 v[124:127], a[164:167], v[52:55], v[124:127]\n\t"
    "v_exp_f32 v116, v116\n\t"
    "v_mfma_f32_16x16x32_bf16 v[124:127], a[168:171], v[56:59], v[124:127]\n\t"
    "ds_read_b128 a[156:159], v240 offset:4096\n\t"
    "v_mfma_f32_16x16x32_bf16 v[124:127], a[172:175], v[60:63], v[124:127]\n\t"
    "v_exp_f32 v117, v117\n\t"
    "v_mfma_f32_16x16x32_bf16 v[128:131], a[176:179], v[48:51], v[76:79]\n\t"
    "ds_read_b64_tr_b16 a[200:201], v230 offset:0\n\t"
    "v_cvt_pk_bf16_f32 v140, v116, v117\n\t"
    "v_mfma_f32_16x16x32_bf16 v[128:131], a[180:183], v[52:55], v[128:131]\n\t"
    "v_exp_f32 v118, v118\n\t"
    "v_mfma_f32_16x16x32_bf16 v[128:131], a[184:187], v[56:59], v[128:131]\n\t"
    "ds_read_b64_tr_b16 a[202:203], v230 offset:4096\n\t"
    "v_mfma_f32_16x16x32_bf16 v[128:131], a[188:191], v[60:63], v[128:131]\n\t"
    "v_exp_f32 v119, v119\n\t"
    "v_mfma_f32_16x16x32_bf16 a[4:7], a[224:227], v[136:139], a[4:7]\n\t"
    "ds_read_b64_tr_b16 a[204:205], v231 offset:0\n\t"
    "v_cvt_pk_bf16_f32 v141, v118, v119\n\t"
    "v_mfma_f32_16x16x32_bf16 a[20:23], a[228:231], v[136:139], a[20:23]\n\t"
    "v_exp_f32 v120, v120\n\t"
    "v_mfma_f32_16x16x32_bf16 a[36:39], a[232:235], v[136:139], a[36:39]\n\t"
    "ds_read_b64_tr_b16 a[206:207], v231 offset:4096\n\t"
    "v_mfma_f32_16x16x32_bf16 a[52:55], a[236:239], v[136:139], a[52:55]\n\t"
    "v_exp_f32 v121, v121\n\t"
    "v_mfma_f32_16x16x32_bf16 a[68:71], a[240:243], v[136:139], a[68:71]\n\t"
    "v_cvt_pk_bf16_f32 v142, v120, v121\n\t"
    "v_mfma_f32_16x16x32_bf16 a[84:87], a[244:247], v[136:139], a[84:87]\n\t"
    "v_exp_f32 v122, v122\n\t"
    "v_mfma_f32_16x16x32_bf16 a[100:103], a[248:251], v[136:139], a[100:103]\n\t"
    "buffer_load_dwordx4 v245, s[48:51], s66 offen offset:3072 lds\n\t"
    "s_add_u32 m0, m0, 16384\n\t"
    "v_mfma_f32_16x16x32_bf16 a[116:119], a[252:255], v[136:139], a[116:119]\n\t"
    "v_exp_f32 v123, v123\n\t"
    "v_mfma_f32_16x16x32_bf16 v[84:87], v[96:99], v[136:139], v[84:87]\n\t"
    "s_waitcnt lgkmcnt(4)\n\t"
    "v_cvt_pk_bf16_f32 v143, v122, v123\n\t"
    "v_mfma_f32_16x16x32_bf16 v[100:103], a[128:131], v[0:3], v[64:67]\n\t"
    "ds_read_b128 a[160:163], v237 offset:8192\n\t"
    "v_mfma_f32_16x16x32_bf16 v[100:103], a[132:135], v[4:7], v[100:103]\n\t"
    "v_exp_f32 v124, v124\n\t"
    "v_mfma_f32_16x16x32_bf16 v[100:103], a[136:139], v[8:11], v[100:103]\n\t"
    "ds_read_b128 a[176:179], v237 offset:12288\n\t"
    "v_add_u32 v237, s61, v237\n\t"
    "v_mfma_f32_16x16x32_bf16 v[100:103], a[140:143], v[12:15], v[100:103]\n\t"
    "v_exp_f32 v125, v125\n\t"
    "v_mfma_f32_16x16x32_bf16 v[104:107], a[144:147], v[0:3], v[64:67]\n\t"
    "ds_read_b64_tr_b16 a[208:209], v232 offset:0\n\t"
    "v_cvt_pk_bf16_f32 v144, v124, v125\n\t"
    "v_mfma_f32_16x16x32_bf16 v[104:107], a[148:151], v[4:7], v[104:107]\n\t"
    "v_exp_f32 v126, v126\n\t"
    "v_mfma_f32_16x16x32_bf16 v[104:107], a[152:155], v[8:11], v[104:107]\n\t"
    "ds_read_b64_tr_b16 a[210:211], v232 offset:4096\n\t"
    "v_mfma_f32_16x16x32_bf16 v[104:107], a[156:159], v[12:15], v[104:107]\n\t"
    "v_exp_f32 v127, v127\n\t"
    "v_mfma_f32_16x16x32_bf16 a[8:11], a[224:227], v[140:143], a[8:11]\n\t"
    "ds_read_b64_tr_b16 a[212:213], v233 offset:0\n\t"
    "v_cvt_pk_bf16_f32 v145, v126, v127\n\t"
    "v_mfma_f32_16x16x32_bf16 a[24:27], a[228:231], v[140:143], a[24:27]\n\t"
    "v_exp_f32 v128, v128\n\t"
    "v_mfma_f32_16x16x32_bf16 a[40:43], a[232:235], v[140:143], a[40:43]\n\t"
    "ds_read_b64_tr_b16 a[214:215], v233 offset:4096\n\t"
    "v_mfma_f32_16x16x32_bf16 a[56:59], a[236:239], v[140:143], a[56:59]\n\t"
    "v_exp_f32 v129, v129\n\t"
    "v_mfma_f32_16x16x32_bf16 a[72:75], a[240:243], v[140:143], a[72:75]\n\t"
    "v_cvt_pk_bf16_f32 v146, v128, v129\n\t"
    "v_mfma_f32_16x16x32_bf16 a[88:91], a[244:247], v[140:143], a[88:91]\n\t"
    "v_exp_f32 v130, v130\n\t"
    "v_mfma_f32_16x16x32_bf16 a[104:107], a[248:251], v[140:143], a[104:107]\n\t"
    "buffer_load_dwordx4 v246, s[52:55], s66 offen offset:0 lds\n\t"
    "v_mfma_f32_16x16x32_bf16 a[120:123], a[252:255], v[140:143], a[120:123]\n\t"
    "v_exp_f32 v131, v131\n\t"
    "v_mfma_f32_16x16x32_bf16 v[88:91], v[96:99], v[140:143], v[88:91]\n\t"
    "v_cvt_pk_bf16_f32 v147, v130, v131\n\t"
    "v_mfma_f32_16x16x32_bf16 v[108:111], a[128:131], v[16:19], v[68:71]\n\t"
    "ds_read_b128 a[164:167], v238 offset:8192\n\t"
    "v_mfma_f32_16x16x32_bf16 v[108:111], a[132:135], v[20:23], v[108:111]\n\t"
    "v_exp_f32 v100, v100\n\t"
    "v_mfma_f32_16x16x32_bf16 v[108:111], a[136:139], v[24:27], v[108:111]\n\t"
    "ds_read_b128 a[180:183], v238 offset:12288\n\t"
    "v_add_u32 v238, s61, v238\n\t"
    "v_mfma_f32_16x16x32_bf16 v[108:111], a[140:143], v[28:31], v[108:111]\n\t"
    "v_exp_f32 v101, v101\n\t"
    "v_mfma_f32_16x16x32_bf16 v[112:115], a[144:147], v[16:19], v[68:71]\n\t"
    "ds_read_b64_tr_b16 a[216:217], v234 offset:0\n\t"
    "v_cvt_pk_bf16_f32 v132, v100, v101\n\t"
    "v_mfma_f32_16x16x32_bf16 v[112:115], a[148:151], v[20:23], v[112:115]\n\t"
    "v_exp_f32 v102, v102\n\t"
    "v_mfma_f32_16x16x32_bf16 v[112:115], a[152:155], v[24:27], v[112:115]\n\t"
    "ds_read_b64_tr_b16 a[218:219], v234 offset:4096\n\t"
    "v_mfma_f32_16x16x32_bf16 v[112:115], a[156:159], v[28:31], v[112:115]\n\t"
    "v_exp_f32 v103, v103\n\t"
    "v_mfma_f32_16x16x32_bf16 a[12:15], a[224:227], v[144:147], a[12:15]\n\t"
    "ds_read_b64_tr_b16 a[220:221], v235 offset:0\n\t"
    "v_cvt_pk_bf16_f32 v133, v102, v103\n\t"
    "v_mfma_f32_16x16x32_bf16 a[28:31], a[228:231], v[144:147], a[28:31]\n\t"
    "v_exp_f32 v104, v104\n\t"
    "v_mfma_f32_16x16x32_bf16 a[44:47], a[232:235], v[144:147], a[44:47]\n\t"
    "ds_read_b64_tr_b16 a[222:223], v235 offset:4096\n\t"
    "v_mfma_f32_16x16x32_bf16 a[60:63], a[236:239], v[144:147], a[60:63]\n\t"
    "v_exp_f32 v105, v105\n\t"
    "v_mfma_f32_16x16x32_bf16 a[76:79], a[240:243], v[144:147], a[76:79]\n\t"
    "s_add_u32 s63, s63, 224\n\t"
    "v_cvt_pk_bf16_f32 v134, v104, v105\n\t"
    "v_mfma_f32_16x16x32_bf16 a[92:95], a[244:247], v[144:147], a[92:95]\n\t"
    "v_exp_f32 v106, v106\n\t"
    "v_mfma_f32_16x16x32_bf16 a[108:111], a[248:251], v[144:147], a[108:111]\n\t"
    "buffer_load_dwordx4 v247, s[52:55], s66 offen offset:1024 lds\n\t"
    "v_mfma_f32_16x16x32_bf16 a[124:127], a[252:255], v[144:147], a[124:127]\n\t"
    "v_exp_f32 v107, v107\n\t"
    "v_mfma_f32_16x16x32_bf16 v[92:95], v[96:99], v[144:147], v[92:95]\n\t"
    "buffer_load_dword v156, v252, s[88:91], s58 offen\n\t"
    "v_cvt_pk_bf16_f32 v135, v106, v107\n\t"
    "v_mfma_f32_16x16x32_bf16 v[116:119], a[128:131], v[32:35], v[72:75]\n\t"
    "ds_read_b128 a[168:171], v239 offset:8192\n\t"
    "v_mfma_f32_16x16x32_bf16 v[116:119], a[132:135], v[36:39], v[116:119]\n\t"
    "v_exp_f32 v108, v108\n\t"
    "v_mfma_f32_16x16x32_bf16 v[116:119], a[136:139], v[40:43], v[116:119]\n\t"
    "ds_read_b128 a[184:187], v239 offset:12288\n\t"
    "v_add_u32 v239, s61, v239\n\t"
    "v_mfma_f32_16x16x32_bf16 v[116:119], a[140:143], v[44:47], v[116:119]\n\t"
    "v_exp_f32 v109, v109\n\t"
    "v_mfma_f32_16x16x32_bf16 v[120:123], a[144:147], v[32:35], v[72:75]\n\t"
    "ds_read_b64_tr_b16 a[224:225], v228 offset:8192\n\t"
    "v_cvt_pk_bf16_f32 v136, v108, v109\n\t"
    "v_mfma_f32_16x16x32_bf16 v[120:123], a[148:151], v[36:39], v[120:123]\n\t"
    "v_exp_f32 v110, v110\n\t"
    "v_mfma_f32_16x16x32_bf16 v[120:123], a[152:155], v[40:43], v[120:123]\n\t"
    "ds_read_b64_tr_b16 a[226:227], v228 offset:12288\n\t"
    "v_add_u32 v228, s61, v228\n\t"
    "v_mfma_f32_16x16x32_bf16 v[120:123], a[156:159], v[44:47], v[120:123]\n\t"
    "s_waitcnt lgkmcnt(4)\n\t"
    "v_exp_f32 v111, v111\n\t"
    "v_mfma_f32_16x16x32_bf16 a[0:3], a[192:195], v[132:135], a[0:3]\n\t"
    "ds_read_b64_tr_b16 a[228:229], v229 offset:8192\n\t"
    "v_cvt_pk_bf16_f32 v137, v110, v111\n\t"
    "v_mfma_f32_16x16x32_bf16 a[16:19], a[196:199], v[132:135], a[16:19]\n\t"
    "v_exp_f32 v112, v112\n\t"
    "v_mfma_f32_16x16x32_bf16 a[32:35], a[200:203], v[132:135], a[32:35]\n\t"
    "ds_read_b64_tr_b16 a[230:231], v229 offset:12288\n\t"
    "v_add_u32 v229, s61, v229\n\t"
    "v_mfma_f32_16x16x32_bf16 a[48:51], a[204:207], v[132:135], a[48:51]\n\t"
    "v_exp_f32 v113, v113\n\t"
    "v_mfma_f32_16x16x32_bf16 a[64:67], a[208:211], v[132:135], a[64:67]\n\t"
    "s_add_u32 s58, s58, 256\n\t"
    "v_cvt_pk_bf16_f32 v138, v112, v113\n\t"
    "v_mfma_f32_16x16x32_bf16 a[80:83], a[212:215], v[132:135], a[80:83]\n\t"
    "v_exp_f32 v114, v114\n\t"
    "v_mfma_f32_16x16x32_bf16 a[96:99], a[216:219], v[132:135], a[96:99]\n\t"
    "buffer_load_dwordx4 v246, s[52:55], s66 offen offset:2048 lds\n\t"
    "s_add_u32 s60, s60, 32768\n\t"
    "v_mfma_f32_16x16x32_bf16 a[112:115], a[220:223], v[132:135], a[112:115]\n\t"
    "v_exp_f32 v115, v115\n\t"
    "v_mfma_f32_16x16x32_bf16 v[80:83], v[96:99], v[132:135], v[80:83]\n\t"
    "s_and_b32 s60, s60, 0x1ffff\n\t"
    "v_cvt_pk_bf16_f32 v139, v114, v115\n\t"
    "v_mfma_f32_16x16x32_bf16 v[124:127], a[128:131], v[48:51], v[76:79]\n\t"
    "ds_read_b128 a[172:175], v240 offset:8192\n\t"
    "v_mfma_f32_16x16x32_bf16 v[124:127], a[132:135], v[52:55], v[124:127]\n\t"
    "v_exp_f32 v116, v116\n\t"
    "v_mfma_f32_16x16x32_bf16 v[124:127], a[136:139], v[56:59], v[124:127]\n\t"
    "ds_read_b128 a[188:191], v240 offset:12288\n\t"
    "v_add_u32 v240, s61, v240\n\t"
    "v_mfma_f32_16x16x32_bf16 v[124:127], a[140:143], v[60:63], v[124:127]\n\t"
    "v_exp_f32 v117, v117\n\t"
    "v_mfma_f32_16x16x32_bf16 v[128:131], a[144:147], v[48:51], v[76:79]\n\t"
    "ds_read_b64_tr_b16 a[232:233], v230 offset:8192\n\t"
    "v_cvt_pk_bf16_f32 v140, v116, v117\n\t"
    "v_mfma_f32_16x16x32_bf16 v[128:131], a[148:151], v[52:55], v[128:131]\n\t"
    "v_exp_f32 v118, v118\n\t"
    "v_mfma_f32_16x16x32_bf16 v[128:131], a[152:155], v[56:59], v[128:131]\n\t"
    "ds_read_b64_tr_b16 a[234:235], v230 offset:12288\n\t"
    "v_add_u32 v230, s61, v230\n\t"
    "v_mfma_f32_16x16x32_bf16 v[128:131], a[156:159], v[60:63], v[128:131]\n\t"
    "v_exp_f32 v119, v119\n\t"
    "v_mfma_f32_16x16x32_bf16 a[4:7], a[192:195], v[136:139], a[4:7]\n\t"
    "ds_read_b64_tr_b16 a[236:237], v231 offset:8192\n\t"
    "v_cvt_pk_bf16_f32 v141, v118, v119\n\t"
    "v_mfma_f32_16x16x32_bf16 a[20:23], a[196:199], v[136:139], a[20:23]\n\t"
    "v_exp_f32 v120, v120\n\t"
    "v_mfma_f32_16x16x32_bf16 a[36:39], a[200:203], v[136:139], a[36:39]\n\t"
    "ds_read_b64_tr_b16 a[238:239], v231 offset:12288\n\t"
    "v_add_u32 v231, s61, v231\n\t"
    "v_mfma_f32_16x16x32_bf16 a[52:55], a[204:207], v[136:139], a[52:55]\n\t"
    "v_exp_f32 v121, v121\n\t"
    "v_mfma_f32_16x16x32_bf16 a[68:71], a[208:211], v[136:139], a[68:71]\n\t"
    "s_waitcnt vmcnt(9)\n\t"
    "s_barrier\n\t"
    "v_cvt_pk_bf16_f32 v142, v120, v121\n\t"
    "v_mfma_f32_16x16x32_bf16 a[84:87], a[212:215], v[136:139], a[84:87]\n\t"
    "v_exp_f32 v122, v122\n\t"
    "v_mfma_f32_16x16x32_bf16 a[100:103], a[216:219], v[136:139], a[100:103]\n\t"
    "buffer_load_dwordx4 v247, s[52:55], s66 offen offset:3072 lds\n\t"
    "s_add_u32 s66, s66, 16384\n\t"
    "s_add_u32 m0, s60, s62\n\t"
    "v_mfma_f32_16x16x32_bf16 a[116:119], a[220:223], v[136:139], a[116:119]\n\t"
    "v_exp_f32 v123, v123\n\t"
    "v_mfma_f32_16x16x32_bf16 v[84:87], v[96:99], v[136:139], v[84:87]\n\t"
    "buffer_store_dword v160, off, s[28:31], s34 sc1\n\t"
    "s_waitcnt lgkmcnt(4)\n\t"
    "v_cvt_pk_bf16_f32 v143, v122, v123\n\t"
    "L_at_loop%=:\n\t"
    "v_mfma_f32_16x16x32_bf16 v[100:103], a[160:163], v[0:3], v[64:67]\n\t"
    "ds_read_b128 a[128:131], v237 offset:0\n\t"
    "v_mfma_f32_16x16x32_bf16 v[100:103], a[164:167], v[4:7], v[100:103]\n\t"
    "v_exp_f32 v124, v124\n\t"
    "v_mfma_f32_16x16x32_bf16 v[100:103], a[168:171], v[8:11], v[100:103]\n\t"
    "ds_read_b128 a[144:147], v237 offset:4096\n\t"
    "v_mfma_f32_16x16x32_bf16 v[100:103], a[172:175], v[12:15], v[100:103]\n\t"
    "v_exp_f32 v125, v125\n\t"
    "v_mfma_f32_16x16x32_bf16 v[104:107], a[176:179], v[0:3], v[64:67]\n\t"
    "ds_read_b64_tr_b16 a[240:241], v232 offset:8192\n\t"
    "v_cvt_pk_bf16_f32 v144, v124, v125\n\t"
    "v_mfma_f32_16x16x32_bf16 v[104:107], a[180:183], v[4:7], v[104:107]\n\t"
    "v_exp_f32 v126, v126\n\t"
    "v_mfma_f32_16x16x32_bf16 v[104:107], a[184:187], v[8:11], v[104:107]\n\t"
    "ds_read_b64_tr_b16 a[242:243], v232 offset:12288\n\t"
    "v_add_u32 v232, s61, v232\n\t"
    "v_mfma_f32_16x16x32_bf16 v[104:107], a[188:191], v[12:15], v[104:107]\n\t"
    "v_exp_f32 v127, v127\n\t"
    "v_mfma_f32_16x16x32_bf16 a[8:11], a[192:195], v[140:143], a[8:11]\n\t"
    "ds_read_b64_tr_b16 a[244:245], v233 offset:8192\n\t"
    "v_cvt_pk_bf16_f32 v145, v126, v127\n\t"
    "v_mfma_f32_16x16x32_bf16 a[24:27], a[196:199], v[140:143], a[24:27]\n\t"
    "v_exp_f32 v128, v128\n\t"
    "v_mfma_f32_16x16x32_bf16 a[40:43], a[200:203], v[140:143], a[40:43]\n\t"
    "ds_read_b64_tr_b16 a[246:247], v233 offset:12288\n\t"
    "v_add_u32 v233, s61, v233\n\t"
    "v_mfma_f32_16x16x32_bf16 a[56:59], a[204:207], v[140:143], a[56:59]\n\t"
    "v_exp_f32 v129, v129\n\t"
    "v_mfma_f32_16x16x32_bf16 a[72:75], a[208:211], v[140:143], a[72:75]\n\t"
    "v_cvt_pk_bf16_f32 v146, v128, v129\n\t"
    "v_mfma_f32_16x16x32_bf16 a[88:91], a[212:215], v[140:143], a[88:91]\n\t"
    "v_exp_f32 v130, v130\n\t"
    "v_mfma_f32_16x16x32_bf16 a[104:107], a[216:219], v[140:143], a[104:107]\n\t"
    "buffer_load_dwordx4 v242, s[48:51], s66 offen offset:0 lds\n\t"
    "v_mfma_f32_16x16x32_bf16 a[120:123], a[220:223], v[140:143], a[120:123]\n\t"
    "v_exp_f32 v131, v131\n\t"
    "v_mfma_f32_16x16x32_bf16 v[88:91], v[96:99], v[140:143], v[88:91]\n\t"
    "v_cvt_pk_bf16_f32 v147, v130, v131\n\t"
    "v_mfma_f32_16x16x32_bf16 v[108:111], a[160:163], v[16:19], v[68:71]\n\t"
    "ds_read_b128 a[132:135], v238 offset:0\n\t"
    "v_mfma_f32_16x16x32_bf16 v[108:111], a[164:167], v[20:23], v[108:111]\n\t"
    "v_exp_f32 v100, v100\n\t"
    "v_mfma_f32_16x16x32_bf16 v[108:111], a[168:171], v[24:27], v[108:111]\n\t"
    "ds_read_b128 a[148:151], v238 offset:4096\n\t"
    "v_mfma_f32_16x16x32_bf16 v[108:111], a[172:175], v[28:31], v[108:111]\n\t"
    "v_exp_f32 v101, v101\n\t"
    "v_mfma_f32_16x16x32_bf16 v[112:115], a[176:179], v[16:19], v[68:71]\n\t"
    "ds_read_b64_tr_b16 a[248:249], v234 offset:8192\n\t"
    "v_cvt_pk_bf16_f32 v132, v100, v101\n\t"
    "v_mfma_f32_16x16x32_bf16 v[112:115], a[180:183], v[20:23], v[112:115]\n\t"
    "v_exp_f32 v102, v102\n\t"
    "v_mfma_f32_16x16x32_bf16 v[112:115], a[184:187], v[24:27], v[112:115]\n\t"
    "ds_read_b64_tr_b16 a[250:251], v234 offset:12288\n\t"
    "v_add_u32 v234, s61, v234\n\t"
    "v_mfma_f32_16x16x32_bf16 v[112:115], a[188:191], v[28:31], v[112:115]\n\t"
    "v_exp_f32 v103, v103\n\t"
    "v_mfma_f32_16x16x32_bf16 a[12:15], a[192:195], v[144:147], a[12:15]\n\t"
    "ds_read_b64_tr_b16 a[252:253], v235 offset:8192\n\t"
    "v_cvt_pk_bf16_f32 v133, v102, v103\n\t"
    "v_mfma_f32_16x16x32_bf16 a[28:31], a[196:199], v[144:147], a[28:31]\n\t"
    "v_exp_f32 v104, v104\n\t"
    "v_mfma_f32_16x16x32_bf16 a[44:47], a[200:203], v[144:147], a[44:47]\n\t"
    "ds_read_b64_tr_b16 a[254:255], v235 offset:12288\n\t"
    "v_add_u32 v235, s61, v235\n\t"
    "v_mfma_f32_16x16x32_bf16 a[60:63], a[204:207], v[144:147], a[60:63]\n\t"
    "v_exp_f32 v105, v105\n\t"
    "v_mfma_f32_16x16x32_bf16 a[76:79], a[208:211], v[144:147], a[76:79]\n\t"
    "s_add_u32 s59, s59, 1\n\t"
    "v_cvt_pk_bf16_f32 v134, v104, v105\n\t"
    "v_mfma_f32_16x16x32_bf16 a[92:95], a[212:215], v[144:147], a[92:95]\n\t"
    "v_exp_f32 v106, v106\n\t"
    "v_mfma_f32_16x16x32_bf16 a[108:111], a[216:219], v[144:147], a[108:111]\n\t"
    "buffer_load_dwordx4 v243, s[48:51], s66 offen offset:1024 lds\n\t"
    "v_mfma_f32_16x16x32_bf16 a[124:127], a[220:223], v[144:147], a[124:127]\n\t"
    "v_exp_f32 v107, v107\n\t"
    "v_mfma_f32_16x16x32_bf16 v[92:95], v[96:99], v[144:147], v[92:95]\n\t"
    "buffer_load_dword v156, v253, s[92:95], s63 offen\n\t"
    "v_cvt_pk_bf16_f32 v135, v106, v107\n\t"
    "v_mfma_f32_16x16x32_bf16 v[116:119], a[160:163], v[32:35], v[72:75]\n\t"
    "ds_read_b128 a[136:139], v239 offset:0\n\t"
    "v_mfma_f32_16x16x32_bf16 v[116:119], a[164:167], v[36:39], v[116:119]\n\t"
    "v_exp_f32 v108, v108\n\t"
    "v_mfma_f32_16x16x32_bf16 v[116:119], a[168:171], v[40:43], v[116:119]\n\t"
    "ds_read_b128 a[152:155], v239 offset:4096\n\t"
    "v_mfma_f32_16x16x32_bf16 v[116:119], a[172:175], v[44:47], v[116:119]\n\t"
    "v_exp_f32 v109, v109\n\t"
    "v_mfma_f32_16x16x32_bf16 v[120:123], a[176:179], v[32:35], v[72:75]\n\t"
    "ds_read_b64_tr_b16 a[192:193], v228 offset:0\n\t"
    "v_cvt_pk_bf16_f32 v136, v108, v109\n\t"
    "v_mfma_f32_16x16x32_bf16 v[120:123], a[180:183], v[36:39], v[120:123]\n\t"
    "v_exp_f32 v110, v110\n\t"
    "v_mfma_f32_16x16x32_bf16 v[120:123], a[184:187], v[40:43], v[120:123]\n\t"
    "ds_read_b64_tr_b16 a[194:195], v228 offset:4096\n\t"
    "v_mfma_f32_16x16x32_bf16 v[120:123], a[188:191], v[44:47], v[120:123]\n\t"
    "s_waitcnt lgkmcnt(4)\n\t"
    "v_exp_f32 v111, v111\n\t"
    "v_mfma_f32_16x16x32_bf16 a[0:3], a[224:227], v[132:135], a[0:3]\n\t"
    "ds_read_b64_tr_b16 a[196:197], v229 offset:0\n\t"
    "v_cvt_pk_bf16_f32 v137, v110, v111\n\t"
    "v_mfma_f32_16x16x32_bf16 a[16:19], a[228:231], v[132:135], a[16:19]\n\t"
    "v_exp_f32 v112, v112\n\t"
    "v_mfma_f32_16x16x32_bf16 a[32:35], a[232:235], v[132:135], a[32:35]\n\t"
    "ds_read_b64_tr_b16 a[198:199], v229 offset:4096\n\t"
    "v_mfma_f32_16x16x32_bf16 a[48:51], a[236:239], v[132:135], a[48:51]\n\t"
    "v_exp_f32 v113, v113\n\t"
    "v_mfma_f32_16x16x32_bf16 a[64:67], a[240:243], v[132:135], a[64:67]\n\t"
    "v_cvt_pk_bf16_f32 v138, v112, v113\n\t"
    "v_mfma_f32_16x16x32_bf16 a[80:83], a[244:247], v[132:135], a[80:83]\n\t"
    "v_exp_f32 v114, v114\n\t"
    "v_mfma_f32_16x16x32_bf16 a[96:99], a[248:251], v[132:135], a[96:99]\n\t"
    "buffer_load_dwordx4 v244, s[48:51], s66 offen offset:2048 lds\n\t"
    "s_and_b32 s65, s59, 3\n\t"
    "s_cselect_b32 s61, s57, s56\n\t"
    "v_mfma_f32_16x16x32_bf16 a[112:115], a[252:255], v[132:135], a[112:115]\n\t"
    "v_exp_f32 v115, v115\n\t"
    "v_mfma_f32_16x16x32_bf16 v[80:83], v[96:99], v[132:135], v[80:83]\n\t"
    "v_cvt_pk_bf16_f32 v139, v114, v115\n\t"
    "v_mfma_f32_16x16x32_bf16 v[124:127], a[160:163], v[48:51], v[76:79]\n\t"
    "ds_read_b128 a[140:143], v240 offset:0\n\t"
    "v_mfma_f32_16x16x32_bf16 v[124:127], a[164:167], v[52:55], v[124:127]\n\t"
    "v_exp_f32 v116, v116\n\t"
    "v_mfma_f32_16x16x32_bf16 v[124:127], a[168:171], v[56:59], v[124:127]\n\t"
    "ds_read_b128 a[156:159], v240 offset:4096\n\t"
    "v_mfma_f32_16x16x32_bf16 v[124:127], a[172:175], v[60:63], v[124:127]\n\t"
    "v_exp_f32 v117, v117\n\t"
    "v_mfma_f32_16x16x32_bf16 v[128:131], a[176:179], v[48:51], v[76:79]\n\t"
    "ds_read_b64_tr_b16 a[200:201], v230 offset:0\n\t"
    "v_cvt_pk_bf16_f32 v140, v116, v117\n\t"
    "v_mfma_f32_16x16x32_bf16 v[128:131], a[180:183], v[52:55], v[128:131]\n\t"
    "v_exp_f32 v118, v118\n\t"
    "v_mfma_f32_16x16x32_bf16 v[128:131], a[184:187], v[56:59], v[128:131]\n\t"
    "ds_read_b64_tr_b16 a[202:203], v230 offset:4096\n\t"
    "v_mfma_f32_16x16x32_bf16 v[128:131], a[188:191], v[60:63], v[128:131]\n\t"
    "v_exp_f32 v119, v119\n\t"
    "v_mfma_f32_16x16x32_bf16 a[4:7], a[224:227], v[136:139], a[4:7]\n\t"
    "ds_read_b64_tr_b16 a[204:205], v231 offset:0\n\t"
    "v_cvt_pk_bf16_f32 v141, v118, v119\n\t"
    "v_mfma_f32_16x16x32_bf16 a[20:23], a[228:231], v[136:139], a[20:23]\n\t"
    "v_exp_f32 v120, v120\n\t"
    "v_mfma_f32_16x16x32_bf16 a[36:39], a[232:235], v[136:139], a[36:39]\n\t"
    "ds_read_b64_tr_b16 a[206:207], v231 offset:4096\n\t"
    "v_mfma_f32_16x16x32_bf16 a[52:55], a[236:239], v[136:139], a[52:55]\n\t"
    "v_exp_f32 v121, v121\n\t"
    "v_mfma_f32_16x16x32_bf16 a[68:71], a[240:243], v[136:139], a[68:71]\n\t"
    "v_cvt_pk_bf16_f32 v142, v120, v121\n\t"
    "v_mfma_f32_16x16x32_bf16 a[84:87], a[244:247], v[136:139], a[84:87]\n\t"
    "v_exp_f32 v122, v122\n\t"
    "v_mfma_f32_16x16x32_bf16 a[100:103], a[248:251], v[136:139], a[100:103]\n\t"
    "buffer_load_dwordx4 v245, s[48:51], s66 offen offset:3072 lds\n\t"
    "s_add_u32 m0, m0, 16384\n\t"
    "v_mfma_f32_16x16x32_bf16 a[116:119], a[252:255], v[136:139], a[116:119]\n\t"
    "v_exp_f32 v123, v123\n\t"
    "v_mfma_f32_16x16x32_bf16 v[84:87], v[96:99], v[136:139], v[84:87]\n\t"
    "s_waitcnt lgkmcnt(4)\n\t"
    "v_cvt_pk_bf16_f32 v143, v122, v123\n\t"
    "v_mfma_f32_16x16x32_bf16 v[100:103], a[128:131], v[0:3], v[64:67]\n\t"
    "ds_read_b128 a[160:163], v237 offset:8192\n\t"
    "v_mfma_f32_16x16x32_bf16 v[100:103], a[132:135], v[4:7], v[100:103]\n\t"
    "v_exp_f32 v124, v124\n\t"
    "v_mfma_f32_16x16x32_bf16 v[100:103], a[136:139], v[8:11], v[100:103]\n\t"
    "ds_read_b128 a[176:179], v237 offset:12288\n\t"
    "v_add_u32 v237, s61, v237\n\t"
    "v_mfma_f32_16x16x32_bf16 v[100:103], a[140:143], v[12:15], v[100:103]\n\t"
    "v_exp_f32 v125, v125\n\t"
    "v_mfma_f32_16x16x32_bf16 v[104:107], a[144:147], v[0:3], v[64:67]\n\t"
    "ds_read_b64_tr_b16 a[208:209], v232 offset:0\n\t"
    "v_cvt_pk_bf16_f32 v144, v124, v125\n\t"
    "v_mfma_f32_16x16x32_bf16 v[104:107], a[148:151], v[4:7], v[104:107]\n\t"
    "v_exp_f32 v126, v126\n\t"
    "v_mfma_f32_16x16x32_bf16 v[104:107], a[152:155], v[8:11], v[104:107]\n\t"
    "ds_read_b64_tr_b16 a[210:211], v232 offset:4096\n\t"
    "v_mfma_f32_16x16x32_bf16 v[104:107], a[156:159], v[12:15], v[104:107]\n\t"
    "v_exp_f32 v127, v127\n\t"
    "v_mfma_f32_16x16x32_bf16 a[8:11], a[224:227], v[140:143], a[8:11]\n\t"
    "ds_read_b64_tr_b16 a[212:213], v233 offset:0\n\t"
    "v_cvt_pk_bf16_f32 v145, v126, v127\n\t"
    "v_mfma_f32_16x16x32_bf16 a[24:27], a[228:231], v[140:143], a[24:27]\n\t"
    "v_exp_f32 v128, v128\n\t"
    "v_mfma_f32_16x16x32_bf16 a[40:43], a[232:235], v[140:143], a[40:43]\n\t"
    "ds_read_b64_tr_b16 a[214:215], v233 offset:4096\n\t"
    "v_mfma_f32_16x16x32_bf16 a[56:59], a[236:239], v[140:143], a[56:59]\n\t"
    "v_exp_f32 v129, v129\n\t"
    "v_mfma_f32_16x16x32_bf16 a[72:75], a[240:243], v[140:143], a[72:75]\n\t"
    "v_cvt_pk_bf16_f32 v146, v128, v129\n\t"
    "v_mfma_f32_16x16x32_bf16 a[88:91], a[244:247], v[140:143], a[88:91]\n\t"
    "v_exp_f32 v130, v130\n\t"
    "v_mfma_f32_16x16x32_bf16 a[104:107], a[248:251], v[140:143], a[104:107]\n\t"
    "buffer_load_dwordx4 v246, s[52:55], s66 offen offset:0 lds\n\t"
    "v_mfma_f32_16x16x32_bf16 a[120:123], a[252:255], v[140:143], a[120:123]\n\t"
    "v_exp_f32 v131, v131\n\t"
    "v_mfma_f32_16x16x32_bf16 v[88:91], v[96:99], v[140:143], v[88:91]\n\t"
    "v_cvt_pk_bf16_f32 v147, v130, v131\n\t"
    "v_mfma_f32_16x16x32_bf16 v[108:111], a[128:131], v[16:19], v[68:71]\n\t"
    "ds_read_b128 a[164:167], v238 offset:8192\n\t"
    "v_mfma_f32_16x16x32_bf16 v[108:111], a[132:135], v[20:23], v[108:111]\n\t"
    "v_exp_f32 v100, v100\n\t"
    "v_mfma_f32_16x16x32_bf16 v[108:111], a[136:139], v[24:27], v[108:111]\n\t"
    "ds_read_b128 a[180:183], v238 offset:12288\n\t"
    "v_add_u32 v238, s61, v238\n\t"
    "v_mfma_f32_16x16x32_bf16 v[108:111], a[140:143], v[28:31], v[108:111]\n\t"
    "v_exp_f32 v101, v101\n\t"
    "v_mfma_f32_16x16x32_bf16 v[112:115], a[144:147], v[16:19], v[68:71]\n\t"
    "ds_read_b64_tr_b16 a[216:217], v234 offset:0\n\t"
    "v_cvt_pk_bf16_f32 v132, v100, v101\n\t"
    "v_mfma_f32_16x16x32_bf16 v[112:115], a[148:151], v[20:23], v[112:115]\n\t"
    "v_exp_f32 v102, v102\n\t"
    "v_mfma_f32_16x16x32_bf16 v[112:115], a[152:155], v[24:27], v[112:115]\n\t"
    "ds_read_b64_tr_b16 a[218:219], v234 offset:4096\n\t"
    "v_mfma_f32_16x16x32_bf16 v[112:115], a[156:159], v[28:31], v[112:115]\n\t"
    "v_exp_f32 v103, v103\n\t"
    "v_mfma_f32_16x16x32_bf16 a[12:15], a[224:227], v[144:147], a[12:15]\n\t"
    "ds_read_b64_tr_b16 a[220:221], v235 offset:0\n\t"
    "v_cvt_pk_bf16_f32 v133, v102, v103\n\t"
    "v_mfma_f32_16x16x32_bf16 a[28:31], a[228:231], v[144:147], a[28:31]\n\t"
    "v_exp_f32 v104, v104\n\t"
    "v_mfma_f32_16x16x32_bf16 a[44:47], a[232:235], v[144:147], a[44:47]\n\t"
    "ds_read_b64_tr_b16 a[222:223], v235 offset:4096\n\t"
    "v_mfma_f32_16x16x32_bf16 a[60:63], a[236:239], v[144:147], a[60:63]\n\t"
    "v_exp_f32 v105, v105\n\t"
    "v_mfma_f32_16x16x32_bf16 a[76:79], a[240:243], v[144:147], a[76:79]\n\t"
    "s_add_u32 s63, s63, 224\n\t"
    "v_cvt_pk_bf16_f32 v134, v104, v105\n\t"
    "v_mfma_f32_16x16x32_bf16 a[92:95], a[244:247], v[144:147], a[92:95]\n\t"
    "v_exp_f32 v106, v106\n\t"
    "v_mfma_f32_16x16x32_bf16 a[108:111], a[248:251], v[144:147], a[108:111]\n\t"
    "buffer_load_dwordx4 v247, s[52:55], s66 offen offset:1024 lds\n\t"
    "v_mfma_f32_16x16x32_bf16 a[124:127], a[252:255], v[144:147], a[124:127]\n\t"
    "v_exp_f32 v107, v107\n\t"
    "v_mfma_f32_16x16x32_bf16 v[92:95], v[96:99], v[144:147], v[92:95]\n\t"
    "buffer_load_dword v156, v252, s[88:91], s58 offen\n\t"
    "v_cvt_pk_bf16_f32 v135, v106, v107\n\t"
    "v_mfma_f32_16x16x32_bf16 v[116:119], a[128:131], v[32:35], v[72:75]\n\t"
    "ds_read_b128 a[168:171], v239 offset:8192\n\t"
    "v_mfma_f32_16x16x32_bf16 v[116:119], a[132:135], v[36:39], v[116:119]\n\t"
    "v_exp_f32 v108, v108\n\t"
    "v_mfma_f32_16x16x32_bf16 v[116:119], a[136:139], v[40:43], v[116:119]\n\t"
    "ds_read_b128 a[184:187], v239 offset:12288\n\t"
    "v_add_u32 v239, s61, v239\n\t"
    "v_mfma_f32_16x16x32_bf16 v[116:119], a[140:143], v[44:47], v[116:119]\n\t"
    "v_exp_f32 v109, v109\n\t"
    "v_mfma_f32_16x16x32_bf16 v[120:123], a[144:147], v[32:35], v[72:75]\n\t"
    "ds_read_b64_tr_b16 a[224:225], v228 offset:8192\n\t"
    "v_cvt_pk_bf16_f32 v136, v108, v109\n\t"
    "v_mfma_f32_16x16x32_bf16 v[120:123], a[148:151], v[36:39], v[120:123]\n\t"
    "v_exp_f32 v110, v110\n\t"
    "v_mfma_f32_16x16x32_bf16 v[120:123], a[152:155], v[40:43], v[120:123]\n\t"
    "ds_read_b64_tr_b16 a[226:227], v228 offset:12288\n\t"
    "v_add_u32 v228, s61, v228\n\t"
    "v_mfma_f32_16x16x32_bf16 v[120:123], a[156:159], v[44:47], v[120:123]\n\t"
    "s_waitcnt lgkmcnt(4)\n\t"
    "v_exp_f32 v111, v111\n\t"
    "v_mfma_f32_16x16x32_bf16 a[0:3], a[192:195], v[132:135], a[0:3]\n\t"
    "ds_read_b64_tr_b16 a[228:229], v229 offset:8192\n\t"
    "v_cvt_pk_bf16_f32 v137, v110, v111\n\t"
    "v_mfma_f32_16x16x32_bf16 a[16:19], a[196:199], v[132:135], a[16:19]\n\t"
    "v_exp_f32 v112, v112\n\t"
    "v_mfma_f32_16x16x32_bf16 a[32:35], a[200:203], v[132:135], a[32:35]\n\t"
    "ds_read_b64_tr_b16 a[230:231], v229 offset:12288\n\t"
    "v_add_u32 v229, s61, v229\n\t"
    "v_mfma_f32_16x16x32_bf16 a[48:51], a[204:207], v[132:135], a[48:51]\n\t"
    "v_exp_f32 v113, v113\n\t"
    "v_mfma_f32_16x16x32_bf16 a[64:67], a[208:211], v[132:135], a[64:67]\n\t"
    "s_add_u32 s58, s58, 256\n\t"
    "v_cvt_pk_bf16_f32 v138, v112, v113\n\t"
    "v_mfma_f32_16x16x32_bf16 a[80:83], a[212:215], v[132:135], a[80:83]\n\t"
    "v_exp_f32 v114, v114\n\t"
    "v_mfma_f32_16x16x32_bf16 a[96:99], a[216:219], v[132:135], a[96:99]\n\t"
    "buffer_load_dwordx4 v246, s[52:55], s66 offen offset:2048 lds\n\t"
    "s_add_u32 s60, s60, 32768\n\t"
    "v_mfma_f32_16x16x32_bf16 a[112:115], a[220:223], v[132:135], a[112:115]\n\t"
    "v_exp_f32 v115, v115\n\t"
    "v_mfma_f32_16x16x32_bf16 v[80:83], v[96:99], v[132:135], v[80:83]\n\t"
    "s_and_b32 s60, s60, 0x1ffff\n\t"
    "v_cvt_pk_bf16_f32 v139, v114, v115\n\t"
    "v_mfma_f32_16x16x32_bf16 v[124:127], a[128:131], v[48:51], v[76:79]\n\t"
    "ds_read_b128 a[172:175], v240 offset:8192\n\t"
    "v_mfma_f32_16x16x32_bf16 v[124:127], a[132:135], v[52:55], v[124:127]\n\t"
    "v_exp_f32 v116, v116\n\t"
    "v_mfma_f32_16x16x32_bf16 v[124:127], a[136:139], v[56:59], v[124:127]\n\t"
    "ds_read_b128 a[188:191], v240 offset:12288\n\t"
    "v_add_u32 v240, s61, v240\n\t"
    "v_mfma_f32_16x16x32_bf16 v[124:127], a[140:143], v[60:63], v[124:127]\n\t"
    "v_exp_f32 v117, v117\n\t"
    "v_mfma_f32_16x16x32_bf16 v[128:131], a[144:147], v[48:51], v[76:79]\n\t"
    "ds_read_b64_tr_b16 a[232:233], v230 offset:8192\n\t"
    "v_cvt_pk_bf16_f32 v140, v116, v117\n\t"
    "v_mfma_f32_16x16x32_bf16 v[128:131], a[148:151], v[52:55], v[128:131]\n\t"
    "v_exp_f32 v118, v118\n\t"
    "v_mfma_f32_16x16x32_bf16 v[128:131], a[152:155], v[56:59], v[128:131]\n\t"
    "ds_read_b64_tr_b16 a[234:235], v230 offset:12288\n\t"
    "v_add_u32 v230, s61, v230\n\t"
    "v_mfma_f32_16x16x32_bf16 v[128:131], a[156:159], v[60:63], v[128:131]\n\t"
    "v_exp_f32 v119, v119\n\t"
    "v_mfma_f32_16x16x32_bf16 a[4:7], a[192:195], v[136:139], a[4:7]\n\t"
    "ds_read_b64_tr_b16 a[236:237], v231 offset:8192\n\t"
    "v_cvt_pk_bf16_f32 v141, v118, v119\n\t"
    "v_mfma_f32_16x16x32_bf16 a[20:23], a[196:199], v[136:139], a[20:23]\n\t"
    "v_exp_f32 v120, v120\n\t"
    "v_mfma_f32_16x16x32_bf16 a[36:39], a[200:203], v[136:139], a[36:39]\n\t"
    "ds_read_b64_tr_b16 a[238:239], v231 offset:12288\n\t"
    "v_add_u32 v231, s61, v231\n\t"
    "v_mfma_f32_16x16x32_bf16 a[52:55], a[204:207], v[136:139], a[52:55]\n\t"
    "v_exp_f32 v121, v121\n\t"
    "v_mfma_f32_16x16x32_bf16 a[68:71], a[208:211], v[136:139], a[68:71]\n\t"
    "s_waitcnt vmcnt(9)\n\t"
    "s_barrier\n\t"
    "v_cvt_pk_bf16_f32 v142, v120, v121\n\t"
    "v_mfma_f32_16x16x32_bf16 a[84:87], a[212:215], v[136:139], a[84:87]\n\t"
    "v_exp_f32 v122, v122\n\t"
    "v_mfma_f32_16x16x32_bf16 a[100:103], a[216:219], v[136:139], a[100:103]\n\t"
    "buffer_load_dwordx4 v247, s[52:55], s66 offen offset:3072 lds\n\t"
    "s_add_u32 s66, s66, 16384\n\t"
    "s_add_u32 m0, s60, s62\n\t"
    "v_mfma_f32_16x16x32_bf16 a[116:119], a[220:223], v[136:139], a[116:119]\n\t"
    "v_exp_f32 v123, v123\n\t"
    "v_mfma_f32_16x16x32_bf16 v[84:87], v[96:99], v[136:139], v[84:87]\n\t"
    "s_waitcnt lgkmcnt(4)\n\t"
    "v_cvt_pk_bf16_f32 v143, v122, v123\n\t"
    "s_add_u32 s64, s64, 1\n\t"
    "s_cbranch_scc0 L_at_loop%=\n\t"
    "s_mov_b64 s[26:27], 0\n\t"
    "v_mov_b32 v172, 0\n\t"
    "buffer_load_dword v168, v169, s[28:31], 0 offen sc1\n\t"
    "v_lshlrev_b32 v173, 3, v248\n\t"
    "v_and_b32 v173, 0x1f80, v173\n\t"
    "v_lshrrev_b32 v224, 4, v248\n\t"
    "v_and_b32 v224, 0x6000, v224\n\t"
    "v_min_u32 v224, 0x4000, v224\n\t"
    "v_or_b32 v224, v224, v173\n\t"
    "s_getpc_b64 s[36:37]\n\t"
    "global_load_dword v156, v224, s[36:37] offset:3584\n\t"
    "s_nop 1\n\t"
    "s_mov_b32 s35, 0\n\t"
    "buffer_load_dword v224, v173, s[20:23], s35 offen sc1\n\t"
    "s_mov_b32 s35, 8192\n\t"
    "buffer_load_dword v225, v173, s[20:23], s35 offen sc1\n\t"
    "s_mov_b32 s35, 16384\n\t"
    "buffer_load_dword v226, v173, s[20:23], s35 offen sc1\n\t"
    "s_mov_b32 s35, 24576\n\t"
    "buffer_load_dword v227, v173, s[20:23], s35 offen sc1\n\t"
    "v_mfma_f32_16x16x32_bf16 v[100:103], a[160:163], v[0:3], v[64:67]\n\t"
    "v_mfma_f32_16x16x32_bf16 v[100:103], a[164:167], v[4:7], v[100:103]\n\t"
    "v_exp_f32 v124, v124\n\t"
    "v_mfma_f32_16x16x32_bf16 v[100:103], a[168:171], v[8:11], v[100:103]\n\t"
    "v_mfma_f32_16x16x32_bf16 v[100:103], a[172:175], v[12:15], v[100:103]\n\t"
    "v_exp_f32 v125, v125\n\t"
    "v_mfma_f32_16x16x32_bf16 v[104:107], a[176:179], v[0:3], v[64:67]\n\t"
    "ds_read_b64_tr_b16 a[240:241], v232 offset:8192\n\t"
    "v_cvt_pk_bf16_f32 v144, v124, v125\n\t"
    "v_mfma_f32_16x16x32_bf16 v[104:107], a[180:183], v[4:7], v[104:107]\n\t"
    "v_exp_f32 v126, v126\n\t"
    "v_mfma_f32_16x16x32_bf16 v[104:107], a[184:187], v[8:11], v[104:107]\n\t"
    "ds_read_b64_tr_b16 a[242:243], v232 offset:12288\n\t"
    "v_add_u32 v232, s61, v232\n\t"
    "v_mfma_f32_16x16x32_bf16 v[104:107], a[188:191], v[12:15], v[104:107]\n\t"
    "v_exp_f32 v127, v127\n\t"
    "v_mfma_f32_16x16x32_bf16 a[8:11], a[192:195], v[140:143], a[8:11]\n\t"
    "ds_read_b64_tr_b16 a[244:245], v233 offset:8192\n\t"
    "v_cvt_pk_bf16_f32 v145, v126, v127\n\t"
    "v_mfma_f32_16x16x32_bf16 a[24:27], a[196:199], v[140:143], a[24:27]\n\t"
    "v_exp_f32 v128, v128\n\t"
    "v_mfma_f32_16x16x32_bf16 a[40:43], a[200:203], v[140:143], a[40:43]\n\t"
    "ds_read_b64_tr_b16 a[246:247], v233 offset:12288\n\t"
    "v_add_u32 v233, s61, v233\n\t"
    "v_mfma_f32_16x16x32_bf16 a[56:59], a[204:207], v[140:143], a[56:59]\n\t"
    "v_exp_f32 v129, v129\n\t"
    "v_mfma_f32_16x16x32_bf16 a[72:75], a[208:211], v[140:143], a[72:75]\n\t"
    "v_cvt_pk_bf16_f32 v146, v128, v129\n\t"
    "v_mfma_f32_16x16x32_bf16 a[88:91], a[212:215], v[140:143], a[88:91]\n\t"
    "v_exp_f32 v130, v130\n\t"
    "v_mfma_f32_16x16x32_bf16 a[104:107], a[216:219], v[140:143], a[104:107]\n\t"
    "v_mfma_f32_16x16x32_bf16 a[120:123], a[220:223], v[140:143], a[120:123]\n\t"
    "v_exp_f32 v131, v131\n\t"
    "v_mfma_f32_16x16x32_bf16 v[88:91], v[96:99], v[140:143], v[88:91]\n\t"
    "v_cvt_pk_bf16_f32 v147, v130, v131\n\t"
    "v_mfma_f32_16x16x32_bf16 v[108:111], a[160:163], v[16:19], v[68:71]\n\t"
    "v_mfma_f32_16x16x32_bf16 v[108:111], a[164:167], v[20:23], v[108:111]\n\t"
    "v_exp_f32 v100, v100\n\t"
    "v_mfma_f32_16x16x32_bf16 v[108:111], a[168:171], v[24:27], v[108:111]\n\t"
    "v_mfma_f32_16x16x32_bf16 v[108:111], a[172:175], v[28:31], v[108:111]\n\t"
    "v_exp_f32 v101, v101\n\t"
    "v_mfma_f32_16x16x32_bf16 v[112:115], a[176:179], v[16:19], v[68:71]\n\t"
    "ds_read_b64_tr_b16 a[248:249], v234 offset:8192\n\t"
    "v_cvt_pk_bf16_f32 v132, v100, v101\n\t"
    "v_mfma_f32_16x16x32_bf16 v[112:115], a[180:183], v[20:23], v[112:115]\n\t"
    "v_exp_f32 v102, v102\n\t"
    "v_mfma_f32_16x16x32_bf16 v[112:115], a[184:187], v[24:27], v[112:115]\n\t"
    "ds_read_b64_tr_b16 a[250:251], v234 offset:12288\n\t"
    "v_add_u32 v234, s61, v234\n\t"
    "v_mfma_f32_16x16x32_bf16 v[112:115], a[188:191], v[28:31], v[112:115]\n\t"
    "v_exp_f32 v103, v103\n\t"
    "v_mfma_f32_16x16x32_bf16 a[12:15], a[192:195], v[144:147], a[12:15]\n\t"
    "ds_read_b64_tr_b16 a[252:253], v235 offset:8192\n\t"
    "v_cvt_pk_bf16_f32 v133, v102, v103\n\t"
    "v_mfma_f32_16x16x32_bf16 a[28:31], a[196:199], v[144:147], a[28:31]\n\t"
    "v_exp_f32 v104, v104\n\t"
    "v_mfma_f32_16x16x32_bf16 a[44:47], a[200:203], v[144:147], a[44:47]\n\t"
    "ds_read_b64_tr_b16 a[254:255], v235 offset:12288\n\t"
    "v_add_u32 v235, s61, v235\n\t"
    "v_mfma_f32_16x16x32_bf16 a[60:63], a[204:207], v[144:147], a[60:63]\n\t"
    "v_exp_f32 v105, v105\n\t"
    "v_mfma_f32_16x16x32_bf16 a[76:79], a[208:211], v[144:147], a[76:79]\n\t"
    "v_cvt_pk_bf16_f32 v134, v104, v105\n\t"
    "v_mfma_f32_16x16x32_bf16 a[92:95], a[212:215], v[144:147], a[92:95]\n\t"
    "v_exp_f32 v106, v106\n\t"
    "v_mfma_f32_16x16x32_bf16 a[108:111], a[216:219], v[144:147], a[108:111]\n\t"
    "v_mfma_f32_16x16x32_bf16 a[124:127], a[220:223], v[144:147], a[124:127]\n\t"
    "v_exp_f32 v107, v107\n\t"
    "v_mfma_f32_16x16x32_bf16 v[92:95], v[96:99], v[144:147], v[92:95]\n\t"
    "v_cvt_pk_bf16_f32 v135, v106, v107\n\t"
    "v_mfma_f32_16x16x32_bf16 v[116:119], a[160:163], v[32:35], v[72:75]\n\t"
    "v_mfma_f32_16x16x32_bf16 v[116:119], a[164:167], v[36:39], v[116:119]\n\t"
    "v_exp_f32 v108, v108\n\t"
    "v_mfma_f32_16x16x32_bf16 v[116:119], a[168:171], v[40:43], v[116:119]\n\t"
    "v_mfma_f32_16x16x32_bf16 v[116:119], a[172:175], v[44:47], v[116:119]\n\t"
    "v_exp_f32 v109, v109\n\t"
    "v_mfma_f32_16x16x32_bf16 v[120:123], a[176:179], v[32:35], v[72:75]\n\t"
    "v_cvt_pk_bf16_f32 v136, v108, v109\n\t"
    "v_mfma_f32_16x16x32_bf16 v[120:123], a[180:183], v[36:39], v[120:123]\n\t"
    "v_exp_f32 v110, v110\n\t"
    "v_mfma_f32_16x16x32_bf16 v[120:123], a[184:187], v[40:43], v[120:123]\n\t"
    "v_mfma_f32_16x16x32_bf16 v[120:123], a[188:191], v[44:47], v[120:123]\n\t"
    "s_waitcnt lgkmcnt(0)\n\t"
    "v_exp_f32 v111, v111\n\t"
    "v_mfma_f32_16x16x32_bf16 a[0:3], a[224:227], v[132:135], a[0:3]\n\t"
    "s_waitcnt vmcnt(6) lgkmcnt(0)\n\t"
    "s_barrier\n\t"
    "v_cvt_pk_bf16_f32 v137, v110, v111\n\t"
    "v_mfma_f32_16x16x32_bf16 a[16:19], a[228:231], v[132:135], a[16:19]\n\t"
    "v_exp_f32 v112, v112\n\t"
    "v_mfma_f32_16x16x32_bf16 a[32:35], a[232:235], v[132:135], a[32:35]\n\t"
    "s_add_u32 m0, s62, 32768\n\t"
    "v_mfma_f32_16x16x32_bf16 a[48:51], a[236:239], v[132:135], a[48:51]\n\t"
    "v_exp_f32 v113, v113\n\t"
    "v_mfma_f32_16x16x32_bf16 a[64:67], a[240:243], v[132:135], a[64:67]\n\t"
    "buffer_load_dwordx4 v164, s[92:95], s24 offen offset:0 lds\n\t"
    "v_cvt_pk_bf16_f32 v138, v112, v113\n\t"
    "v_mfma_f32_16x16x32_bf16 a[80:83], a[244:247], v[132:135], a[80:83]\n\t"
    "v_exp_f32 v114, v114\n\t"
    "v_mfma_f32_16x16x32_bf16 a[96:99], a[248:251], v[132:135], a[96:99]\n\t"
    "buffer_load_dwordx4 v165, s[92:95], s24 offen offset:1024 lds\n\t"
    "v_mfma_f32_16x16x32_bf16 a[112:115], a[252:255], v[132:135], a[112:115]\n\t"
    "v_exp_f32 v115, v115\n\t"
    "v_mfma_f32_16x16x32_bf16 v[80:83], v[96:99], v[132:135], v[80:83]\n\t"
    "buffer_load_dwordx4 v166, s[92:95], s24 offen offset:2048 lds\n\t"
    "v_cvt_pk_bf16_f32 v139, v114, v115\n\t"
    "v_mfma_f32_16x16x32_bf16 v[124:127], a[160:163], v[48:51], v[76:79]\n\t"
    "buffer_load_dwordx4 v167, s[92:95], s24 offen offset:3072 lds\n\t"
    "s_add_u32 s24, s24, 131072\n\t"
    "s_add_u32 m0, s62, 49152\n\t"
    "v_mfma_f32_16x16x32_bf16 v[124:127], a[164:167], v[52:55], v[124:127]\n\t"
    "v_exp_f32 v116, v116\n\t"
    "v_mfma_f32_16x16x32_bf16 v[124:127], a[168:171], v[56:59], v[124:127]\n\t"
    "buffer_load_dwordx4 v164, s[92:95], s24 offen offset:0 lds\n\t"
    "v_mfma_f32_16x16x32_bf16 v[124:127], a[172:175], v[60:63], v[124:127]\n\t"
    "v_exp_f32 v117, v117\n\t"
    "v_mfma_f32_16x16x32_bf16 v[128:131], a[176:179], v[48:51], v[76:79]\n\t"
    "buffer_load_dwordx4 v165, s[92:95], s24 offen offset:1024 lds\n\t"
    "v_cvt_pk_bf16_f32 v140, v116, v117\n\t"
    "v_mfma_f32_16x16x32_bf16 v[128:131], a[180:183], v[52:55], v[128:131]\n\t"
    "v_exp_f32 v118, v118\n\t"
    "v_mfma_f32_16x16x32_bf16 v[128:131], a[184:187], v[56:59], v[128:131]\n\t"
    "buffer_load_dwordx4 v166, s[92:95], s24 offen offset:2048 lds\n\t"
    "v_mfma_f32_16x16x32_bf16 v[128:131], a[188:191], v[60:63], v[128:131]\n\t"
    "v_exp_f32 v119, v119\n\t"
    "v_mfma_f32_16x16x32_bf16 a[4:7], a[224:227], v[136:139], a[4:7]\n\t"
    "buffer_load_dwordx4 v167, s[92:95], s24 offen offset:3072 lds\n\t"
    "v_cvt_pk_bf16_f32 v141, v118, v119\n\t"
    "s_add_u32 s24, s24, 131072\n\t"
    "v_mfma_f32_16x16x32_bf16 a[20:23], a[228:231], v[136:139], a[20:23]\n\t"
    "v_exp_f32 v120, v120\n\t"
    "v_mfma_f32_16x16x32_bf16 a[36:39], a[232:235], v[136:139], a[36:39]\n\t"
    "s_nop 7\n\t"
    "v_rcp_f32 v173, v80\n\t"
    "v_mfma_f32_16x16x32_bf16 a[52:55], a[236:239], v[136:139], a[52:55]\n\t"
    "v_exp_f32 v121, v121\n\t"
    "v_mfma_f32_16x16x32_bf16 a[68:71], a[240:243], v[136:139], a[68:71]\n\t"
    "v_cvt_pk_bf16_f32 v142, v120, v121\n\t"
    "v_cmp_ngt_f32 vcc, 0x7149f2ca, v80\n\t"
    "v_mfma_f32_16x16x32_bf16 a[84:87], a[244:247], v[136:139], a[84:87]\n\t"
    "v_exp_f32 v122, v122\n\t"
    "v_mfma_f32_16x16x32_bf16 a[100:103], a[248:251], v[136:139], a[100:103]\n\t"
    "s_or_b64 s[26:27], s[26:27], vcc\n\t"
    "s_mov_b32 s35, 0\n\t"
    "v_accvgpr_read_b32 v176, a0\n\t"
    "v_mfma_f32_16x16x32_bf16 a[116:119], a[252:255], v[136:139], a[116:119]\n\t"
    "v_exp_f32 v123, v123\n\t"
    "v_mfma_f32_16x16x32_bf16 v[84:87], v[96:99], v[136:139], v[84:87]\n\t"
    "v_cvt_pk_bf16_f32 v143, v122, v123\n\t"
    "v_accvgpr_read_b32 v177, a1\n\t"
    "v_accvgpr_read_b32 v178, a2\n\t"
    "v_accvgpr_read_b32 v179, a3\n\t"
    "v_accvgpr_read_b32 v180, a16\n\t"
    "v_accvgpr_read_b32 v181, a17\n\t"
    "v_accvgpr_read_b32 v182, a18\n\t"
    "v_accvgpr_read_b32 v183, a19\n\t"
    "v_exp_f32 v124, v124\n\t"
    "v_mul_f32 v176, v176, v173\n\t"
    "v_mul_f32 v177, v177, v173\n\t"
    "v_mul_f32 v178, v178, v173\n\t"
    "v_mul_f32 v179, v179, v173\n\t"
    "v_mul_f32 v180, v180, v173\n\t"
    "v_mul_f32 v181, v181, v173\n\t"
    "v_exp_f32 v125, v125\n\t"
    "v_mul_f32 v182, v182, v173\n\t"
    "v_mul_f32 v183, v183, v173\n\t"
    "v_cvt_pk_bf16_f32 v192, v176, v177\n\t"
    "v_cvt_pk_bf16_f32 v144, v124, v125\n\t"
    "v_cvt_pk_bf16_f32 v193, v178, v179\n\t"
    "v_cvt_pk_bf16_f32 v194, v180, v181\n\t"
    "v_cvt_pk_bf16_f32 v195, v182, v183\n\t"
    "v_exp_f32 v126, v126\n\t"
    "buffer_store_dwordx4 v[192:195], v248, s[16:19], s35 offen offset:0 sc1\n\t"
    "v_dot2c_f32_bf16 v172, 0, v192\n\t"
    "v_dot2c_f32_bf16 v172, 0, v193\n\t"
    "v_dot2c_f32_bf16 v172, 0, v194\n\t"
    "v_dot2c_f32_bf16 v172, 0, v195\n\t"
    "v_accvgpr_read_b32 v184, a32\n\t"
    "v_exp_f32 v127, v127\n\t"
    "v_mfma_f32_16x16x32_bf16 a[8:11], a[224:227], v[140:143], a[8:11]\n\t"
    "v_cvt_pk_bf16_f32 v145, v126, v127\n\t"
    "v_accvgpr_read_b32 v185, a33\n\t"
    "v_mfma_f32_16x16x32_bf16 a[24:27], a[228:231], v[140:143], a[24:27]\n\t"
    "v_exp_f32 v128, v128\n\t"
    "v_mfma_f32_16x16x32_bf16 a[40:43], a[232:235], v[140:143], a[40:43]\n\t"
    "v_accvgpr_read_b32 v186, a34\n\t"
    "v_accvgpr_read_b32 v187, a35\n\t"
    "v_mfma_f32_16x16x32_bf16 a[56:59], a[236:239], v[140:143], a[56:59]\n\t"
    "v_exp_f32 v129, v129\n\t"
    "v_mfma_f32_16x16x32_bf16 a[72:75], a[240:243], v[140:143], a[72:75]\n\t"
    "v_cvt_pk_bf16_f32 v146, v128, v129\n\t"
    "v_accvgpr_read_b32 v188, a48\n\t"
    "v_mfma_f32_16x16x32_bf16 a[88:91], a[244:247], v[140:143], a[88:91]\n\t"
    "v_exp_f32 v130, v130\n\t"
    "v_mfma_f32_16x16x32_bf16 a[104:107], a[248:251], v[140:143], a[104:107]\n\t"
    "v_accvgpr_read_b32 v189, a49\n\t"
    "v_accvgpr_read_b32 v190, a50\n\t"
    "v_mfma_f32_16x16x32_bf16 a[120:123], a[252:255], v[140:143], a[120:123]\n\t"
    "v_exp_f32 v131, v131\n\t"
    "v_mfma_f32_16x16x32_bf16 v[88:91], v[96:99], v[140:143], v[88:91]\n\t"
    "v_cvt_pk_bf16_f32 v147, v130, v131\n\t"
    "v_accvgpr_read_b32 v191, a51\n\t"
    "v_mul_f32 v184, v184, v173\n\t"
    "v_mul_f32 v185, v185, v173\n\t"
    "v_mul_f32 v186, v186, v173\n\t"
    "v_mul_f32 v187, v187, v173\n\t"
    "v_mul_f32 v188, v188, v173\n\t"
    "v_mul_f32 v189, v189, v173\n\t"
    "v_mul_f32 v190, v190, v173\n\t"
    "v_mul_f32 v191, v191, v173\n\t"
    "v_cvt_pk_bf16_f32 v196, v184, v185\n\t"
    "v_cvt_pk_bf16_f32 v197, v186, v187\n\t"
    "v_cvt_pk_bf16_f32 v198, v188, v189\n\t"
    "v_cvt_pk_bf16_f32 v199, v190, v191\n\t"
    "buffer_store_dwordx4 v[196:199], v248, s[16:19], s35 offen offset:1024 sc1\n\t"
    "v_dot2c_f32_bf16 v172, 0, v196\n\t"
    "v_dot2c_f32_bf16 v172, 0, v197\n\t"
    "v_dot2c_f32_bf16 v172, 0, v198\n\t"
    "v_dot2c_f32_bf16 v172, 0, v199\n\t"
    "v_accvgpr_read_b32 v176, a64\n\t"
    "v_accvgpr_read_b32 v177, a65\n\t"
    "v_accvgpr_read_b32 v178, a66\n\t"
    "v_accvgpr_read_b32 v179, a67\n\t"
    "v_accvgpr_read_b32 v180, a80\n\t"
    "v_accvgpr_read_b32 v181, a81\n\t"
    "v_accvgpr_read_b32 v182, a82\n\t"
    "v_mfma_f32_16x16x32_bf16 a[12:15], a[224:227], v[144:147], a[12:15]\n\t"
    "v_accvgpr_read_b32 v183, a83\n\t"
    "v_mul_f32 v176, v176, v173\n\t"
    "v_mfma_f32_16x16x32_bf16 a[28:31], a[228:231], v[144:147], a[28:31]\n\t"
    "v_mul_f32 v177, v177, v173\n\t"
    "v_mul_f32 v178, v178, v173\n\t"
    "v_mfma_f32_16x16x32_bf16 a[44:47], a[232:235], v[144:147], a[44:47]\n\t"
    "v_mul_f32 v179, v179, v173\n\t"
    "v_mul_f32 v180, v180, v173\n\t"
    "v_mfma_f32_16x16x32_bf16 a[60:63], a[236:239], v[144:147], a[60:63]\n\t"
    "v_mul_f32 v181, v181, v173\n\t"
    "v_mul_f32 v182, v182, v173\n\t"
    "v_mfma_f32_16x16x32_bf16 a[76:79], a[240:243], v[144:147], a[76:79]\n\t"
    "v_mul_f32 v183, v183, v173\n\t"
    "v_cvt_pk_bf16_f32 v200, v176, v177\n\t"
    "v_mfma_f32_16x16x32_bf16 a[92:95], a[244:247], v[144:147], a[92:95]\n\t"
    "v_cvt_pk_bf16_f32 v201, v178, v179\n\t"
    "v_cvt_pk_bf16_f32 v202, v180, v181\n\t"
    "v_mfma_f32_16x16x32_bf16 a[108:111], a[248:251], v[144:147], a[108:111]\n\t"
    "v_cvt_pk_bf16_f32 v203, v182, v183\n\t"
    "v_mfma_f32_16x16x32_bf16 a[124:127], a[252:255], v[144:147], a[124:127]\n\t"
    "buffer_store_dwordx4 v[200:203], v248, s[16:19], s35 offen offset:2048 sc1\n\t"
    "v_dot2c_f32_bf16 v172, 0, v200\n\t"
    "v_mfma_f32_16x16x32_bf16 v[92:95], v[96:99], v[144:147], v[92:95]\n\t"
    "v_dot2c_f32_bf16 v172, 0, v201\n\t"
    "v_dot2c_f32_bf16 v172, 0, v202\n\t"
    "v_dot2c_f32_bf16 v172, 0, v203\n\t"
    "v_accvgpr_read_b32 v184, a96\n\t"
    "v_accvgpr_read_b32 v185, a97\n\t"
    "v_accvgpr_read_b32 v186, a98\n\t"
    "v_accvgpr_read_b32 v187, a99\n\t"
    "v_accvgpr_read_b32 v188, a112\n\t"
    "v_accvgpr_read_b32 v189, a113\n\t"
    "v_accvgpr_read_b32 v190, a114\n\t"
    "v_accvgpr_read_b32 v191, a115\n\t"
    "v_mul_f32 v184, v184, v173\n\t"
    "v_mul_f32 v185, v185, v173\n\t"
    "v_mul_f32 v186, v186, v173\n\t"
    "v_mul_f32 v187, v187, v173\n\t"
    "v_mul_f32 v188, v188, v173\n\t"
    "v_mul_f32 v189, v189, v173\n\t"
    "v_mul_f32 v190, v190, v173\n\t"
    "v_mul_f32 v191, v191, v173\n\t"
    "v_cvt_pk_bf16_f32 v204, v184, v185\n\t"
    "v_cvt_pk_bf16_f32 v205, v186, v187\n\t"
    "v_cvt_pk_bf16_f32 v206, v188, v189\n\t"
    "v_cvt_pk_bf16_f32 v207, v190, v191\n\t"
    "buffer_store_dwordx4 v[204:207], v248, s[16:19], s35 offen offset:3072 sc1\n\t"
    "v_dot2c_f32_bf16 v172, 0, v204\n\t"
    "v_dot2c_f32_bf16 v172, 0, v205\n\t"
    "v_dot2c_f32_bf16 v172, 0, v206\n\t"
    "v_dot2c_f32_bf16 v172, 0, v207\n\t"
    "s_nop 7\n\t"
    "v_rcp_f32 v173, v84\n\t"
    "v_cmp_ngt_f32 vcc, 0x7149f2ca, v84\n\t"
    "s_or_b64 s[26:27], s[26:27], vcc\n\t"
    "s_mov_b32 s35, 131072\n\t"
    "v_accvgpr_read_b32 v176, a4\n\t"
    "v_accvgpr_read_b32 v177, a5\n\t"
    "v_accvgpr_read_b32 v178, a6\n\t"
    "v_accvgpr_read_b32 v179, a7\n\t"
    "v_accvgpr_read_b32 v180, a20\n\t"
    "v_accvgpr_read_b32 v181, a21\n\t"
    "v_accvgpr_read_b32 v182, a22\n\t"
    "v_accvgpr_read_b32 v183, a23\n\t"
    "v_mul_f32 v176, v176, v173\n\t"
    "v_mul_f32 v177, v177, v173\n\t"
    "v_mul_f32 v178, v178, v173\n\t"
    "v_mul_f32 v179, v179, v173\n\t"
    "v_mul_f32 v180, v180, v173\n\t"
    "v_mul_f32 v181, v181, v173\n\t"
    "v_mul_f32 v182, v182, v173\n\t"
    "v_mul_f32 v183, v183, v173\n\t"
    "v_cvt_pk_bf16_f32 v192, v176, v177\n\t"
    "v_cvt_pk_bf16_f32 v193, v178, v179\n\t"
    "v_cvt_pk_bf16_f32 v194, v180, v181\n\t"
    "v_cvt_pk_bf16_f32 v195, v182, v183\n\t"
    "buffer_store_dwordx4 v[192:195], v248, s[16:19], s35 offen offset:0 sc1\n\t"
    "v_dot2c_f32_bf16 v172, 0, v192\n\t"
    "v_dot2c_f32_bf16 v172, 0, v193\n\t"
    "v_dot2c_f32_bf16 v172, 0, v194\n\t"
    "v_dot2c_f32_bf16 v172, 0, v195\n\t"
    "v_accvgpr_read_b32 v184, a36\n\t"
    "v_accvgpr_read_b32 v185, a37\n\t"
    "v_accvgpr_read_b32 v186, a38\n\t"
    "v_accvgpr_read_b32 v187, a39\n\t"
    "v_accvgpr_read_b32 v188, a52\n\t"
    "v_accvgpr_read_b32 v189, a53\n\t"
    "v_accvgpr_read_b32 v190, a54\n\t"
    "v_accvgpr_read_b32 v191, a55\n\t"
    "v_mul_f32 v184, v184, v173\n\t"
    "v_mul_f32 v185, v185, v173\n\t"
    "v_mul_f32 v186, v186, v173\n\t"
    "v_mul_f32 v187, v187, v173\n\t"
    "v_mul_f32 v188, v188, v173\n\t"
    "v_mul_f32 v189, v189, v173\n\t"
    "v_mul_f32 v190, v190, v173\n\t"
    "v_mul_f32 v191, v191, v173\n\t"
    "v_cvt_pk_bf16_f32 v196, v184, v185\n\t"
    "v_cvt_pk_bf16_f32 v197, v186, v187\n\t"
    "v_cvt_pk_bf16_f32 v198, v188, v189\n\t"
    "v_cvt_pk_bf16_f32 v199, v190, v191\n\t"
    "buffer_store_dwordx4 v[196:199], v248, s[16:19], s35 offen offset:1024 sc1\n\t"
    "v_dot2c_f32_bf16 v172, 0, v196\n\t"
    "v_dot2c_f32_bf16 v172, 0, v197\n\t"
    "v_dot2c_f32_bf16 v172, 0, v198\n\t"
    "v_dot2c_f32_bf16 v172, 0, v199\n\t"
    "v_accvgpr_read_b32 v176, a68\n\t"
    "v_accvgpr_read_b32 v177, a69\n\t"
    "v_accvgpr_read_b32 v178, a70\n\t"
    "v_accvgpr_read_b32 v179, a71\n\t"
    "v_accvgpr_read_b32 v180, a84\n\t"
    "v_accvgpr_read_b32 v181, a85\n\t"
    "v_accvgpr_read_b32 v182, a86\n\t"
    "v_accvgpr_read_b32 v183, a87\n\t"
    "v_mul_f32 v176, v176, v173\n\t"
    "v_mul_f32 v177, v177, v173\n\t"
    "v_mul_f32 v178, v178, v173\n\t"
    "v_mul_f32 v179, v179, v173\n\t"
    "v_mul_f32 v180, v180, v173\n\t"
    "v_mul_f32 v181, v181, v173\n\t"
    "v_mul_f32 v182, v182, v173\n\t"
    "v_mul_f32 v183, v183, v173\n\t"
    "v_cvt_pk_bf16_f32 v200, v176, v177\n\t"
    "v_cvt_pk_bf16_f32 v201, v178, v179\n\t"
    "v_cvt_pk_bf16_f32 v202, v180, v181\n\t"
    "v_cvt_pk_bf16_f32 v203, v182, v183\n\t"
    "buffer_store_dwordx4 v[200:203], v248, s[16:19], s35 offen offset:2048 sc1\n\t"
    "v_dot2c_f32_bf16 v172, 0, v200\n\t"
    "v_dot2c_f32_bf16 v172, 0, v201\n\t"
    "v_dot2c_f32_bf16 v172, 0, v202\n\t"
    "v_dot2c_f32_bf16 v172, 0, v203\n\t"
    "v_accvgpr_read_b32 v184, a100\n\t"
    "v_accvgpr_read_b32 v185, a101\n\t"
    "v_accvgpr_read_b32 v186, a102\n\t"
    "v_accvgpr_read_b32 v187, a103\n\t"
    "v_accvgpr_read_b32 v188, a116\n\t"
    "v_accvgpr_read_b32 v189, a117\n\t"
    "v_accvgpr_read_b32 v190, a118\n\t"
    "v_accvgpr_read_b32 v191, a119\n\t"
    "v_mul_f32 v184, v184, v173\n\t"
    "v_mul_f32 v185, v185, v173\n\t"
    "v_mul_f32 v186, v186, v173\n\t"
    "v_mul_f32 v187, v187, v173\n\t"
    "v_mul_f32 v188, v188, v173\n\t"
    "v_mul_f32 v189, v189, v173\n\t"
    "v_mul_f32 v190, v190, v173\n\t"
    "v_mul_f32 v191, v191, v173\n\t"
    "v_cvt_pk_bf16_f32 v204, v184, v185\n\t"
    "v_cvt_pk_bf16_f32 v205, v186, v187\n\t"
    "v_cvt_pk_bf16_f32 v206, v188, v189\n\t"
    "v_cvt_pk_bf16_f32 v207, v190, v191\n\t"
    "buffer_store_dwordx4 v[204:207], v248, s[16:19], s35 offen offset:3072 sc1\n\t"
    "v_dot2c_f32_bf16 v172, 0, v204\n\t"
    "v_dot2c_f32_bf16 v172, 0, v205\n\t"
    "v_dot2c_f32_bf16 v172, 0, v206\n\t"
    "v_dot2c_f32_bf16 v172, 0, v207\n\t"
    "s_nop 7\n\t"
    "v_rcp_f32 v173, v88\n\t"
    "v_cmp_ngt_f32 vcc, 0x7149f2ca, v88\n\t"
    "s_or_b64 s[26:27], s[26:27], vcc\n\t"
    "s_mov_b32 s35, 262144\n\t"
    "v_accvgpr_read_b32 v176, a8\n\t"
    "v_accvgpr_read_b32 v177, a9\n\t"
    "v_accvgpr_read_b32 v178, a10\n\t"
    "v_accvgpr_read_b32 v179, a11\n\t"
    "v_accvgpr_read_b32 v180, a24\n\t"
    "v_accvgpr_read_b32 v181, a25\n\t"
    "v_accvgpr_read_b32 v182, a26\n\t"
    "v_accvgpr_read_b32 v183, a27\n\t"
    "v_mul_f32 v176, v176, v173\n\t"
    "v_mul_f32 v177, v177, v173\n\t"
    "v_mul_f32 v178, v178, v173\n\t"
    "v_mul_f32 v179, v179, v173\n\t"
    "v_mul_f32 v180, v180, v173\n\t"
    "v_mul_f32 v181, v181, v173\n\t"
    "v_mul_f32 v182, v182, v173\n\t"
    "v_mul_f32 v183, v183, v173\n\t"
    "v_cvt_pk_bf16_f32 v192, v176, v177\n\t"
    "v_cvt_pk_bf16_f32 v193, v178, v179\n\t"
    "v_cvt_pk_bf16_f32 v194, v180, v181\n\t"
    "v_cvt_pk_bf16_f32 v195, v182, v183\n\t"
    "buffer_store_dwordx4 v[192:195], v248, s[16:19], s35 offen offset:0 sc1\n\t"
    "v_dot2c_f32_bf16 v172, 0, v192\n\t"
    "v_dot2c_f32_bf16 v172, 0, v193\n\t"
    "v_dot2c_f32_bf16 v172, 0, v194\n\t"
    "v_dot2c_f32_bf16 v172, 0, v195\n\t"
    "v_accvgpr_read_b32 v184, a40\n\t"
    "v_accvgpr_read_b32 v185, a41\n\t"
    "v_accvgpr_read_b32 v186, a42\n\t"
    "v_accvgpr_read_b32 v187, a43\n\t"
    "v_accvgpr_read_b32 v188, a56\n\t"
    "v_accvgpr_read_b32 v189, a57\n\t"
    "v_accvgpr_read_b32 v190, a58\n\t"
    "v_accvgpr_read_b32 v191, a59\n\t"
    "v_mul_f32 v184, v184, v173\n\t"
    "v_mul_f32 v185, v185, v173\n\t"
    "v_mul_f32 v186, v186, v173\n\t"
    "v_mul_f32 v187, v187, v173\n\t"
    "v_mul_f32 v188, v188, v173\n\t"
    "v_mul_f32 v189, v189, v173\n\t"
    "v_mul_f32 v190, v190, v173\n\t"
    "v_mul_f32 v191, v191, v173\n\t"
    "v_cvt_pk_bf16_f32 v196, v184, v185\n\t"
    "v_cvt_pk_bf16_f32 v197, v186, v187\n\t"
    "v_cvt_pk_bf16_f32 v198, v188, v189\n\t"
    "v_cvt_pk_bf16_f32 v199, v190, v191\n\t"
    "buffer_store_dwordx4 v[196:199], v248, s[16:19], s35 offen offset:1024 sc1\n\t"
    "v_dot2c_f32_bf16 v172, 0, v196\n\t"
    "v_dot2c_f32_bf16 v172, 0, v197\n\t"
    "v_dot2c_f32_bf16 v172, 0, v198\n\t"
    "v_dot2c_f32_bf16 v172, 0, v199\n\t"
    "v_accvgpr_read_b32 v176, a72\n\t"
    "v_accvgpr_read_b32 v177, a73\n\t"
    "v_accvgpr_read_b32 v178, a74\n\t"
    "v_accvgpr_read_b32 v179, a75\n\t"
    "v_accvgpr_read_b32 v180, a88\n\t"
    "v_accvgpr_read_b32 v181, a89\n\t"
    "v_accvgpr_read_b32 v182, a90\n\t"
    "v_accvgpr_read_b32 v183, a91\n\t"
    "v_mul_f32 v176, v176, v173\n\t"
    "v_mul_f32 v177, v177, v173\n\t"
    "v_mul_f32 v178, v178, v173\n\t"
    "v_mul_f32 v179, v179, v173\n\t"
    "v_mul_f32 v180, v180, v173\n\t"
    "v_mul_f32 v181, v181, v173\n\t"
    "v_mul_f32 v182, v182, v173\n\t"
    "v_mul_f32 v183, v183, v173\n\t"
    "v_cvt_pk_bf16_f32 v200, v176, v177\n\t"
    "v_cvt_pk_bf16_f32 v201, v178, v179\n\t"
    "v_cvt_pk_bf16_f32 v202, v180, v181\n\t"
    "v_cvt_pk_bf16_f32 v203, v182, v183\n\t"
    "buffer_store_dwordx4 v[200:203], v248, s[16:19], s35 offen offset:2048 sc1\n\t"
    "v_dot2c_f32_bf16 v172, 0, v200\n\t"
    "v_dot2c_f32_bf16 v172, 0, v201\n\t"
    "v_dot2c_f32_bf16 v172, 0, v202\n\t"
    "v_dot2c_f32_bf16 v172, 0, v203\n\t"
    "v_accvgpr_read_b32 v184, a104\n\t"
    "v_accvgpr_read_b32 v185, a105\n\t"
    "v_accvgpr_read_b32 v186, a106\n\t"
    "v_accvgpr_read_b32 v187, a107\n\t"
    "v_accvgpr_read_b32 v188, a120\n\t"
    "v_accvgpr_read_b32 v189, a121\n\t"
    "v_accvgpr_read_b32 v190, a122\n\t"
    "v_accvgpr_read_b32 v191, a123\n\t"
    "v_mul_f32 v184, v184, v173\n\t"
    "v_mul_f32 v185, v185, v173\n\t"
    "v_mul_f32 v186, v186, v173\n\t"
    "v_mul_f32 v187, v187, v173\n\t"
    "v_mul_f32 v188, v188, v173\n\t"
    "v_mul_f32 v189, v189, v173\n\t"
    "v_mul_f32 v190, v190, v173\n\t"
    "v_mul_f32 v191, v191, v173\n\t"
    "v_cvt_pk_bf16_f32 v204, v184, v185\n\t"
    "v_cvt_pk_bf16_f32 v205, v186, v187\n\t"
    "v_cvt_pk_bf16_f32 v206, v188, v189\n\t"
    "v_cvt_pk_bf16_f32 v207, v190, v191\n\t"
    "buffer_store_dwordx4 v[204:207], v248, s[16:19], s35 offen offset:3072 sc1\n\t"
    "v_dot2c_f32_bf16 v172, 0, v204\n\t"
    "v_dot2c_f32_bf16 v172, 0, v205\n\t"
    "v_dot2c_f32_bf16 v172, 0, v206\n\t"
    "v_dot2c_f32_bf16 v172, 0, v207\n\t"
    "s_nop 7\n\t"
    "v_rcp_f32 v173, v92\n\t"
    "v_cmp_ngt_f32 vcc, 0x7149f2ca, v92\n\t"
    "s_or_b64 s[26:27], s[26:27], vcc\n\t"
    "s_mov_b32 s35, 393216\n\t"
    "v_accvgpr_read_b32 v176, a12\n\t"
    "v_accvgpr_read_b32 v177, a13\n\t"
    "v_accvgpr_read_b32 v178, a14\n\t"
    "v_accvgpr_read_b32 v179, a15\n\t"
    "v_accvgpr_read_b32 v180, a28\n\t"
    "v_accvgpr_read_b32 v181, a29\n\t"
    "v_accvgpr_read_b32 v182, a30\n\t"
    "v_accvgpr_read_b32 v183, a31\n\t"
    "v_mul_f32 v176, v176, v173\n\t"
    "v_mul_f32 v177, v177, v173\n\t"
    "v_mul_f32 v178, v178, v173\n\t"
    "v_mul_f32 v179, v179, v173\n\t"
    "v_mul_f32 v180, v180, v173\n\t"
    "v_mul_f32 v181, v181, v173\n\t"
    "v_mul_f32 v182, v182, v173\n\t"
    "v_mul_f32 v183, v183, v173\n\t"
    "v_cvt_pk_bf16_f32 v192, v176, v177\n\t"
    "v_cvt_pk_bf16_f32 v193, v178, v179\n\t"
    "v_cvt_pk_bf16_f32 v194, v180, v181\n\t"
    "v_cvt_pk_bf16_f32 v195, v182, v183\n\t"
    "buffer_store_dwordx4 v[192:195], v248, s[16:19], s35 offen offset:0 sc1\n\t"
    "v_dot2c_f32_bf16 v172, 0, v192\n\t"
    "v_dot2c_f32_bf16 v172, 0, v193\n\t"
    "v_dot2c_f32_bf16 v172, 0, v194\n\t"
    "v_dot2c_f32_bf16 v172, 0, v195\n\t"
    "v_accvgpr_read_b32 v184, a44\n\t"
    "v_accvgpr_read_b32 v185, a45\n\t"
    "v_accvgpr_read_b32 v186, a46\n\t"
    "v_accvgpr_read_b32 v187, a47\n\t"
    "v_accvgpr_read_b32 v188, a60\n\t"
    "v_accvgpr_read_b32 v189, a61\n\t"
    "v_accvgpr_read_b32 v190, a62\n\t"
    "v_accvgpr_read_b32 v191, a63\n\t"
    "v_mul_f32 v184, v184, v173\n\t"
    "v_mul_f32 v185, v185, v173\n\t"
    "v_mul_f32 v186, v186, v173\n\t"
    "v_mul_f32 v187, v187, v173\n\t"
    "v_mul_f32 v188, v188, v173\n\t"
    "v_mul_f32 v189, v189, v173\n\t"
    "v_mul_f32 v190, v190, v173\n\t"
    "v_mul_f32 v191, v191, v173\n\t"
    "v_cvt_pk_bf16_f32 v196, v184, v185\n\t"
    "v_cvt_pk_bf16_f32 v197, v186, v187\n\t"
    "v_cvt_pk_bf16_f32 v198, v188, v189\n\t"
    "v_cvt_pk_bf16_f32 v199, v190, v191\n\t"
    "buffer_store_dwordx4 v[196:199], v248, s[16:19], s35 offen offset:1024 sc1\n\t"
    "v_dot2c_f32_bf16 v172, 0, v196\n\t"
    "v_dot2c_f32_bf16 v172, 0, v197\n\t"
    "v_dot2c_f32_bf16 v172, 0, v198\n\t"
    "v_dot2c_f32_bf16 v172, 0, v199\n\t"
    "v_accvgpr_read_b32 v176, a76\n\t"
    "v_accvgpr_read_b32 v177, a77\n\t"
    "v_accvgpr_read_b32 v178, a78\n\t"
    "v_accvgpr_read_b32 v179, a79\n\t"
    "v_accvgpr_read_b32 v180, a92\n\t"
    "v_accvgpr_read_b32 v181, a93\n\t"
    "v_accvgpr_read_b32 v182, a94\n\t"
    "v_accvgpr_read_b32 v183, a95\n\t"
    "v_mul_f32 v176, v176, v173\n\t"
    "v_mul_f32 v177, v177, v173\n\t"
    "v_mul_f32 v178, v178, v173\n\t"
    "v_mul_f32 v179, v179, v173\n\t"
    "v_mul_f32 v180, v180, v173\n\t"
    "v_mul_f32 v181, v181, v173\n\t"
    "v_mul_f32 v182, v182, v173\n\t"
    "v_mul_f32 v183, v183, v173\n\t"
    "v_cvt_pk_bf16_f32 v200, v176, v177\n\t"
    "v_cvt_pk_bf16_f32 v201, v178, v179\n\t"
    "v_cvt_pk_bf16_f32 v202, v180, v181\n\t"
    "v_cvt_pk_bf16_f32 v203, v182, v183\n\t"
    "buffer_store_dwordx4 v[200:203], v248, s[16:19], s35 offen offset:2048 sc1\n\t"
    "v_dot2c_f32_bf16 v172, 0, v200\n\t"
    "v_dot2c_f32_bf16 v172, 0, v201\n\t"
    "v_dot2c_f32_bf16 v172, 0, v202\n\t"
    "v_dot2c_f32_bf16 v172, 0, v203\n\t"
    "v_accvgpr_read_b32 v184, a108\n\t"
    "v_accvgpr_read_b32 v185, a109\n\t"
    "v_accvgpr_read_b32 v186, a110\n\t"
    "v_accvgpr_read_b32 v187, a111\n\t"
    "v_accvgpr_read_b32 v188, a124\n\t"
    "v_accvgpr_read_b32 v189, a125\n\t"
    "v_accvgpr_read_b32 v190, a126\n\t"
    "v_accvgpr_read_b32 v191, a127\n\t"
    "v_mul_f32 v184, v184, v173\n\t"
    "v_mul_f32 v185, v185, v173\n\t"
    "v_mul_f32 v186, v186, v173\n\t"
    "v_mul_f32 v187, v187, v173\n\t"
    "v_mul_f32 v188, v188, v173\n\t"
    "v_mul_f32 v189, v189, v173\n\t"
    "v_mul_f32 v190, v190, v173\n\t"
    "v_mul_f32 v191, v191, v173\n\t"
    "v_cvt_pk_bf16_f32 v204, v184, v185\n\t"
    "v_cvt_pk_bf16_f32 v205, v186, v187\n\t"
    "v_cvt_pk_bf16_f32 v206, v188, v189\n\t"
    "v_cvt_pk_bf16_f32 v207, v190, v191\n\t"
    "buffer_store_dwordx4 v[204:207], v248, s[16:19], s35 offen offset:3072 sc1\n\t"
    "v_dot2c_f32_bf16 v172, 0, v204\n\t"
    "v_dot2c_f32_bf16 v172, 0, v205\n\t"
    "v_dot2c_f32_bf16 v172, 0, v206\n\t"
    "v_dot2c_f32_bf16 v172, 0, v207\n\t"
    "s_nop 3\n\t"
    "v_cmp_neq_f32 vcc, 0, v172\n\t"
    "s_or_b64 s[26:27], s[26:27], vcc\n\t"
    "s_waitcnt vmcnt(24)\n\t"
      : "={v[0:31]}"(qf0), "={v[32:63]}"(qf1), "={v[224:227]}"(ep), "={v168}"(fa), "={v169}"(fao), "+{a188}"(fo), "={s[26:27]}"(badm), "+{s24}"(sw3),
        "+{a184}"(w3[0]), "+{a185}"(w3[1]), "+{a186}"(w3[2]), "+{a187}"(w3[3]),
        "+{v228}"(vq_rd[0]), "+{v229}"(vq_rd[1]), "+{v230}"(vq_rd[2]), "+{v231}"(vq_rd[3]), "+{v232}"(vq_rd[4]), "+{v233}"(vq_rd[5]), "+{v234}"(vq_rd[6]), "+{v235}"(vq_rd[7]), "+{v236}"(vg_rd),
        "+{v237}"(vq_dma[0]), "+{v238}"(vq_dma[1]), "+{v239}"(vq_dma[2]), "+{v240}"(vq_dma[3]),
        "+{v241}"(vg_dma[0]), "+{v242}"(vg_dma[1]), "+{v243}"(vg_dma[2]), "+{v244}"(vg_dma[3]), "+{v245}"(vg_dma[4]), "+{v246}"(vg_dma[5]), "+{v247}"(vg_dma[6]),
        "+{v252}"(vtq), "+{v253}"(vtg),
        "+{a172}"(pk[0]), "+{a173}"(pk[1]), "+{a174}"(pk[2]), "+{a175}"(pk[3]), "+{a176}"(pk[4]), "+{a177}"(pk[5]),
        "+{a178}"(pk[6]), "+{a179}"(pk[7]), "+{a180}"(pk[8]), "+{a181}"(pk[9]), "+{a182}"(pk[10]), "+{a183}"(pk[11]),
        "+{s56}"(soffQ), "+{s57}"(soffG), "+{s58}"(soffX), "+{s59}"(scur), "+{s63}"(m0g), "+{s66}"(soffKV), "+{s68}"(sh), "+{s67}"(sown), "+{s64}"(spoll), "+{s[36:39]}"(srdQ)
      : "{v248}"(vx[0]), "{v249}"(vx[1]), "{v250}"(vx[2]), "{v251}"(vx[3]),
        "{s[40:43]}"(srdG), "{s[44:47]}"(srdX), "{s[48:51]}"(srdK), "{s[52:55]}"(srdV),
        "{s[80:83]}"(srdC), "{s[84:87]}"(srdH), "{s[88:91]}"(make_srd(wo, HD * DM * 2)), "{s[92:95]}"(make_srd(wd, FF * DM * 2)),
        "{s[16:19]}"(make_srd(ws + WS_O + ((size_t)(rb * 16 + w * 4) * 128 + c * 4) * 1024 - (size_t)w * 4 * 32768, w * 4 * 32768 + 4 * 131072)),
        "{s[20:23]}"(make_srd(ws + WS_CNT + 8192, (bid == 0 && w == 0) ? 32768u : 0u)),
        "{s[28:31]}"(make_srd(ws + WS_CNT, 8192 + 3 * 32768)), "{s25}"(tag),
        "{s62}"(m0q), "{s69}"(shi0), "{s34}"((unsigned)(w == 0 ? 40960 + (rb * 32 + c) * FST : 0x7fffff00u))
      : "v64", "v65", "v66", "v67", "v68", "v69", "v70", "v71", "v72", "v73", "v74", "v75", "v76", "v77", "v78", "v79", "v80", "v81", "v82", "v83", "v84", "v85", "v86", "v87", "v88", "v89", "v90", "v91", "v92", "v93", "v94", "v95", "v96", "v97", "v98", "v99", "v100", "v101", "v102", "v103", "v104", "v105", "v106", "v107", "v108", "v109", "v110", "v111", "v112", "v113", "v114", "v115", "v116", "v117", "v118", "v119", "v120", "v121", "v122", "v123", "v124", "v125", "v126", "v127", "v128", "v129", "v130", "v131", "v132", "v133", "v134", "v135", "v136", "v137", "v138", "v139", "v140", "v141", "v142", "v143", "v144", "v145", "v146", "v147", "v148", "v149", "v150", "v151", "v152", "v153", "v154", "v155", "v156", "v157", "v158", "v159", "v160", "v161", "v162", "v163", "v164", "v165", "v166", "v167", "v170", "v171", "v172", "v173", "v174", "v175", "v176", "v177", "v178", "v179", "v180", "v181", "v182", "v183", "v184", "v185", "v186", "v187", "v188", "v189", "v190", "v191", "v192", "v193", "v194", "v195", "v196", "v197", "v198", "v199", "v200", "v201", "v202", "v203", "v204", "v205", "v206", "v207", "v208", "v209", "v210", "v211", "v212", "v213", "v214", "v215", "v216", "v217", "v218", "v219", "v220", "v221", "v222", "v223", "a0", "a1", "a2", "a3", "a4", "a5", "a6", "a7", "a8", "a9", "a10", "a11", "a12", "a13", "a14", "a15", "a16", "a17", "a18", "a19", "a20", "a21", "a22", "a23", "a24", "a25", "a26", "a27", "a28", "a29", "a30", "a31", "a32", "a33", "a34", "a35", "a36", "a37", "a38", "a39", "a40", "a41", "a42", "a43", "a44", "a45", "a46", "a47", "a48", "a49", "a50", "a51", "a52", "a53", "a54", "a55", "a56", "a57", "a58", "a59", "a60", "a61", "a62", "a63", "a64", "a65", "a66", "a67", "a68", "a69", "a70", "a71", "a72", "a73", "a74", "a75", "a76", "a77", "a78", "a79", "a80", "a81", "a82", "a83", "a84", "a85", "a86", "a87", "a88", "a89", "a90", "a91", "a92", "a93", "a94", "a95", "a96", "a97", "a98", "a99", "a100", "a101", "a102", "a103", "a104", "a105", "a106", "a107", "a108", "a109", "a110", "a111", "a112", "a113", "a114", "a115", "a116", "a117", "a118", "a119", "a120", "a121", "a122", "a123", "a124", "a125", "a126", "a127", "a128", "a129", "a130", "a131", "a132", "a133", "a134", "a135", "a136", "a137", "a138", "a139", "a140", "a141", "a142", "a143", "a144", "a145", "a146", "a147", "a148", "a149", "a150", "a151", "a152", "a153", "a154", "a155", "a156", "a157", "a158", "a159", "a160", "a161", "a162", "a163", "a164", "a165", "a166", "a167", "a168", "a169", "a170", "a171", "a189", "a190", "a191", "a192", "a193", "a194", "a195", "a196", "a197", "a198", "a199", "a200", "a201", "a202", "a203", "a204", "a205", "a206", "a207", "a208", "a209", "a210", "a211", "a212", "a213", "a214", "a215", "a216", "a217", "a218", "a219", "a220", "a221", "a222", "a223", "a224", "a225", "a226", "a227", "a228", "a229", "a230", "a231", "a232", "a233", "a234", "a235", "a236", "a237", "a238", "a239", "a240", "a241", "a242", "a243", "a244", "a245", "a246", "a247", "a248", "a249", "a250", "a251", "a252", "a253", "a254", "a255", "s35", "s60", "s61", "s65", "vcc", "scc", "memory");
  }
  unsigned l3; asm volatile("v_mbcnt_lo_u32_b32 %0, -1, 0\n\tv_mbcnt_hi_u32_b32 %0, -1, %0" : "=v"(l3));
  if (__builtin_expect(badm != 0ull, 0)) {
    asm volatile("s_waitcnt vmcnt(0)" ::: "memory");
    unsigned* qs = (unsigned*)(ws + WS_QF + ((size_t)bid * 4 + w) * 16384);
    {
      const int g2 = l3 >> 4, m2 = l3 & 15, sg = ((g2 & 1) << 1) | (g2 >> 1);
#pragma unroll
      for (int qb = 0; qb < 4; ++qb)
#pragma unroll
        for (int ds = 0; ds < 4; ++ds) {
          u32x4 v;
#pragma unroll
          for (int e = 0; e < 4; ++e) v[e] = qb < 2 ? qf0[(qb & 1) * 16 + ds * 4 + e] : qf1[(qb & 1) * 16 + ds * 4 + e];
          *(u32x4*)(qs + (qb * 16 + m2) * 64 + ds * 16 + sg * 4) = v;
        }
    }
    asm volatile("s_waitcnt vmcnt(0)" ::: "memory");
    __builtin_amdgcn_fence(__ATOMIC_SEQ_CST, "workgroup");
    unsigned qr[64];
#pragma unroll
    for (int i = 0; i < 16; ++i) { const u32x4 v = *(const volatile u32x4*)(qs + l3 * 64 + i * 4); qr[4 * i] = v[0]; qr[4 * i + 1] = v[1]; qr[4 * i + 2] = v[2]; qr[4 * i + 3] = v[3]; }
    const unsigned* Kh = (const unsigned*)(Kc + (size_t)c * SEQ * DH);
    const unsigned* Vh = (const unsigned*)(Vc + (size_t)c * SEQ * DH);
    float mx = -__builtin_inff();
#pragma unroll 1
    for (int k = 0; k < SEQ; ++k) {
      float s = 0.f;
#pragma unroll
      for (int i = 0; i < 64; ++i) { const unsigned kv = Kh[k * 64 + i]; s = fmaf(bflo(qr[i]), bflo(kv), s); s = fmaf(bfhi(qr[i]), bfhi(kv), s); }
      mx = fmaxf(mx, s);
    }
    float l = 0.f, oa[128];
#pragma unroll
    for (int i = 0; i < 128; ++i) oa[i] = 0.f;
#pragma unroll 1
    for (int k = 0; k < SEQ; ++k) {
      float s = 0.f;
#pragma unroll
      for (int i = 0; i < 64; ++i) { const unsigned kv = Kh[k * 64 + i]; s = fmaf(bflo(qr[i]), bflo(kv), s); s = fmaf(bfhi(qr[i]), bfhi(kv), s); }
      const float pe = __builtin_amdgcn_exp2f(s - mx);
      l += pe;
#pragma unroll
      for (int i = 0; i < 64; ++i) { const unsigned vv = Vh[k * 64 + i]; oa[2 * i] = fmaf(pe, bflo(vv), oa[2 * i]); oa[2 * i + 1] = fmaf(pe, bfhi(vv), oa[2 * i + 1]); }
    }
    const float il = 1.0f / l;
    char* ob = ws + WS_O + ((size_t)(rb * 16 + w * 4 + (l3 >> 4)) * 128 + c * 4) * 1024 + (l3 & 15) * 16;
#pragma unroll
    for (int d4 = 0; d4 < 32; ++d4) {
      u32x2 o; o[0] = cvtpk(oa[4 * d4] * il, oa[4 * d4 + 1] * il); o[1] = cvtpk(oa[4 * d4 + 2] * il, oa[4 * d4 + 3] * il);
      __hip_atomic_store((unsigned long long*)(ob + (d4 >> 3) * 1024 + (d4 & 3) * 256 + ((d4 >> 2) & 1) * 8), ((unsigned long long)o[1] << 32) | o[0], __ATOMIC_RELAXED, __HIP_MEMORY_SCOPE_AGENT);
    }
  }

  {
    __amdgpu_buffer_rsrc_t fr = __builtin_amdgcn_make_buffer_rsrc(ws + WS_CNT, (short)0, 8192 + 3 * 32768, 0x00020000);
    if (__builtin_expect(!__all((int)(fa == tag)), 0)) {
#pragma unroll 1
      for (unsigned spins = 0; spins < SPIN_MAX; ++spins) {
        __builtin_amdgcn_s_sleep(4);
        asm volatile("" ::: "memory");
        if (__all((int)(__builtin_amdgcn_raw_buffer_load_b32(fr, fao, 0, 16) == tag))) break;
      }
      asm volatile("" ::: "memory");
    }
    if (bid == 0 && w == 0) {
      bool ok = ep[0] == tag && ep[1] == tag && ep[2] == tag && ep[3] == tag;
#pragma unroll 1
      for (unsigned spins = 0; !__all((int)ok) && spins < SPIN_MAX; ++spins) {
        __builtin_amdgcn_s_sleep(4);
        asm volatile("" ::: "memory");
        ok = true;
#pragma unroll
        for (int i = 0; i < 4; ++i) ok &= __builtin_amdgcn_raw_buffer_load_b32(fr, 8192 + (i * 64 + l3) * 128, 0, 16) == tag;
      }
      if (l3 == 0) __builtin_amdgcn_raw_buffer_store_b32(tag, fr, 4096, 0, 16);
    }
  }
  {
    const int g3 = l3 >> 4, q3 = (l3 >> 2) & 3, p3 = l3 & 3, m3 = l3 & 15;
    u32x4 srdA = make_srd(ws + WS_H + (size_t)rb3 * 16 * 114688 + q * 28 * 1024, 16 * 114688 - q * 28 * 1024);
    const u32x4 srdA2 = make_srd(ws + WS_O + (size_t)rb3 * 16 * 131072 + q * 32 * 1024, 16 * 131072 - q * 32 * 1024);
    u32x4 srdW = make_srd((const char*)wd + (size_t)q * 14 * 131072 + cb * 256, 14 * 131072u - cb * 256u);
    const u32x4 srdW2 = make_srd((const char*)wo + (size_t)q * 16 * 131072 + cb * 256, 16 * 131072u - cb * 256u);
    unsigned vw_rd[8], vw_dma[4], vx[4], vx2[4], vxo[4];
#pragma unroll
    for (int nb = 0; nb < 8; ++nb) { const int sg2 = nb >> 1, act = 2 * (q ^ (sg2 == 0 ? 1 : sg2 == 1 ? 0 : sg2)) + (nb & 1);
      vw_rd[nb] = ldsb + (4 * g3 + q3) * 256 + ((act ^ (4 * (g3 & 1) + q3)) * 32) + 8 * p3; }
#pragma unroll
    for (int i = 0; i < 4; ++i) { const int row = 16 * w + 4 * i + g3, sc = (((m3 >> 1) ^ (row & 7)) << 1) | (m3 & 1); vw_dma[i] = row * 2048 + sc * 16 - i * 1024; }
#pragma unroll
    for (int mb = 0; mb < 4; ++mb) { vx[mb] = (4 * w + mb) * 114688 + l3 * 16; vx2[mb] = (4 * w + mb) * 131072 + l3 * 16; vxo[mb] = ((w * 64 + mb * 16 + m3) * DM + (g3 & 1) * 16 + 8 * (g3 >> 1)) * 2; }
    unsigned soffX = 0, soffW = 2 * 131072;
    const size_t tbase = (size_t)rb3 * 256 * DM + cb * 128 + q * 32;
    const unsigned woff = w * 4096 + l3 * 16;
    char* const inb = ws + WS_G + (size_t)tile * 4 * 98304;
    unsigned vfo = (l3 & 7) * FST;
    asm volatile(
    "s_nop 4\n\t"
    "s_mov_b32 s52, 65536\n\t"
    "s_mov_b32 s53, 0\n\t"
    "s_mov_b32 s47, -2\n\t"
    "buffer_load_dwordx4 a[64:67], v140, s[36:39], s44 offen offset:0 sc1\n\t"
    "buffer_load_dwordx4 a[68:71], v141, s[36:39], s44 offen offset:0 sc1\n\t"
    "buffer_load_dwordx4 a[72:75], v142, s[36:39], s44 offen offset:0 sc1\n\t"
    "buffer_load_dwordx4 a[76:79], v143, s[36:39], s44 offen offset:0 sc1\n\t"
    "buffer_load_dwordx4 a[80:83], v140, s[36:39], s44 offen offset:1024 sc1\n\t"
    "buffer_load_dwordx4 a[84:87], v141, s[36:39], s44 offen offset:1024 sc1\n\t"
    "buffer_load_dwordx4 a[88:91], v142, s[36:39], s44 offen offset:1024 sc1\n\t"
    "buffer_load_dwordx4 a[92:95], v143, s[36:39], s44 offen offset:1024 sc1\n\t"
    "buffer_load_dwordx4 a[96:99], v140, s[36:39], s44 offen offset:2048 sc1\n\t"
    "buffer_load_dwordx4 a[100:103], v141, s[36:39], s44 offen offset:2048 sc1\n\t"
    "buffer_load_dwordx4 a[104:107], v142, s[36:39], s44 offen offset:2048 sc1\n\t"
    "buffer_load_dwordx4 a[108:111], v143, s[36:39], s44 offen offset:2048 sc1\n\t"
    "s_mov_b32 s64, 16384\n\t"
    "s_mov_b32 s65, 32768\n\t"
    "s_mov_b32 s66, 49152\n\t"
    "s_mov_b32 s67, 65536\n\t"
    "s_mov_b32 s68, 81920\n\t"
    "v_mov_b32 v245, s71\n\t"
    "s_waitcnt vmcnt(12)\n\t"
    "s_barrier\n\t"
    "buffer_store_dword v245, v240, s[96:99], 0 offen sc1\n\t"
    "ds_read_b64_tr_b16 a[0:1], v128 offset:32768\n\t"
    "ds_read_b64_tr_b16 a[2:3], v128 offset:36864\n\t"
    "ds_read_b64_tr_b16 a[4:5], v128 offset:40960\n\t"
    "ds_read_b64_tr_b16 a[6:7], v128 offset:45056\n\t"
    "ds_read_b64_tr_b16 a[8:9], v129 offset:32768\n\t"
    "ds_read_b64_tr_b16 a[10:11], v129 offset:36864\n\t"
    "ds_read_b64_tr_b16 a[12:13], v129 offset:40960\n\t"
    "ds_read_b64_tr_b16 a[14:15], v129 offset:45056\n\t"
    "ds_read_b64_tr_b16 a[16:17], v130 offset:32768\n\t"
    "ds_read_b64_tr_b16 a[18:19], v130 offset:36864\n\t"
    "ds_read_b64_tr_b16 a[20:21], v130 offset:40960\n\t"
    "ds_read_b64_tr_b16 a[22:23], v130 offset:45056\n\t"
    "ds_read_b64_tr_b16 a[24:25], v131 offset:32768\n\t"
    "ds_read_b64_tr_b16 a[26:27], v131 offset:36864\n\t"
    "ds_read_b64_tr_b16 a[28:29], v131 offset:40960\n\t"
    "ds_read_b64_tr_b16 a[30:31], v131 offset:45056\n\t"
    "ds_read_b64_tr_b16 a[32:33], v132 offset:32768\n\t"
    "ds_read_b64_tr_b16 a[34:35], v132 offset:36864\n\t"
    "ds_read_b64_tr_b16 a[36:37], v132 offset:40960\n\t"
    "ds_read_b64_tr_b16 a[38:39], v132 offset:45056\n\t"
    "ds_read_b64_tr_b16 a[40:41], v133 offset:32768\n\t"
    "ds_read_b64_tr_b16 a[42:43], v133 offset:36864\n\t"
    "ds_read_b64_tr_b16 a[44:45], v133 offset:40960\n\t"
    "ds_read_b64_tr_b16 a[46:47], v133 offset:45056\n\t"
    "s_waitcnt vmcnt(5) lgkmcnt(12)\n\t"
    "v_mfma_f32_16x16x32_bf16 v[0:3], a[0:3], a[64:67], 0\n\t"
    "ds_read_b64_tr_b16 a[48:49], v134 offset:32768\n\t"
    "v_mfma_f32_16x16x32_bf16 v[0:3], a[4:7], a[80:83], v[0:3]\n\t"
    "buffer_load_dwordx4 a[112:115], v140, s[36:39], s44 offen offset:3072 sc1\n\t"
    "v_mfma_f32_16x16x32_bf16 v[4:7], a[0:3], a[68:71], 0\n\t"
    "ds_read_b64_tr_b16 a[50:51], v134 offset:36864\n\t"
    "v_mfma_f32_16x16x32_bf16 v[4:7], a[4:7], a[84:87], v[4:7]\n\t"
    "v_mfma_f32_16x16x32_bf16 v[8:11], a[0:3], a[72:75], 0\n\t"
    "ds_read_b64_tr_b16 a[52:53], v134 offset:40960\n\t"
    "v_mfma_f32_16x16x32_bf16 v[8:11], a[4:7], a[88:91], v[8:11]\n\t"
    "buffer_load_dwordx4 a[116:119], v141, s[36:39], s44 offen offset:3072 sc1\n\t"
    "v_mfma_f32_16x16x32_bf16 v[12:15], a[0:3], a[76:79], 0\n\t"
    "ds_read_b64_tr_b16 a[54:55], v134 offset:45056\n\t"
    "v_mfma_f32_16x16x32_bf16 v[12:15], a[4:7], a[92:95], v[12:15]\n\t"
    "s_waitcnt lgkmcnt(12)\n\t"
    "v_mfma_f32_16x16x32_bf16 v[16:19], a[8:11], a[64:67], 0\n\t"
    "ds_read_b64_tr_b16 a[56:57], v135 offset:32768\n\t"
    "v_mfma_f32_16x16x32_bf16 v[16:19], a[12:15], a[80:83], v[16:19]\n\t"
    "buffer_load_dwordx4 a[120:123], v142, s[36:39], s44 offen offset:3072 sc1\n\t"
    "v_mfma_f32_16x16x32_bf16 v[20:23], a[8:11], a[68:71], 0\n\t"
    "ds_read_b64_tr_b16 a[58:59], v135 offset:36864\n\t"
    "v_mfma_f32_16x16x32_bf16 v[20:23], a[12:15], a[84:87], v[20:23]\n\t"
    "v_mfma_f32_16x16x32_bf16 v[24:27], a[8:11], a[72:75], 0\n\t"
    "ds_read_b64_tr_b16 a[60:61], v135 offset:40960\n\t"
    "v_mfma_f32_16x16x32_bf16 v[24:27], a[12:15], a[88:91], v[24:27]\n\t"
    "buffer_load_dwordx4 a[124:127], v143, s[36:39], s44 offen offset:3072 sc1\n\t"
    "s_add_u32 s44, s44, 4096\n\t"
    "v_mfma_f32_16x16x32_bf16 v[28:31], a[8:11], a[76:79], 0\n\t"
    "ds_read_b64_tr_b16 a[62:63], v135 offset:45056\n\t"
    "v_mfma_f32_16x16x32_bf16 v[28:31], a[12:15], a[92:95], v[28:31]\n\t"
    "s_waitcnt lgkmcnt(12)\n\t"
    "s_barrier\n\t"
    "v_mfma_f32_16x16x32_bf16 v[32:35], a[16:19], a[64:67], 0\n\t"
    "ds_read_b64_tr_b16 a[0:1], v128 offset:49152\n\t"
    "s_add_u32 m0, s52, s46\n\t"
    "v_mfma_f32_16x16x32_bf16 v[32:35], a[20:23], a[80:83], v[32:35]\n\t"
    "buffer_load_dwordx4 v136, s[40:43], s45 offen offset:0 lds\n\t"
    "v_mfma_f32_16x16x32_bf16 v[36:39], a[16:19], a[68:71], 0\n\t"
    "ds_read_b64_tr_b16 a[2:3], v128 offset:53248\n\t"
    "v_mfma_f32_16x16x32_bf16 v[36:39], a[20:23], a[84:87], v[36:39]\n\t"
    "v_mfma_f32_16x16x32_bf16 v[40:43], a[16:19], a[72:75], 0\n\t"
    "ds_read_b64_tr_b16 a[4:5], v128 offset:57344\n\t"
    "v_mfma_f32_16x16x32_bf16 v[40:43], a[20:23], a[88:91], v[40:43]\n\t"
    "buffer_load_dwordx4 v137, s[40:43], s45 offen offset:1024 lds\n\t"
    "v_mfma_f32_16x16x32_bf16 v[44:47], a[16:19], a[76:79], 0\n\t"
    "ds_read_b64_tr_b16 a[6:7], v128 offset:61440\n\t"
    "v_xor_b32 v128, 0x10000, v128\n\t"
    "v_mfma_f32_16x16x32_bf16 v[44:47], a[20:23], a[92:95], v[44:47]\n\t"
    "s_waitcnt lgkmcnt(12)\n\t"
    "v_mfma_f32_16x16x32_bf16 v[48:51], a[24:27], a[64:67], 0\n\t"
    "ds_read_b64_tr_b16 a[8:9], v129 offset:49152\n\t"
    "v_mfma_f32_16x16x32_bf16 v[48:51], a[28:31], a[80:83], v[48:51]\n\t"
    "buffer_load_dwordx4 v138, s[40:43], s45 offen offset:2048 lds\n\t"
    "v_mfma_f32_16x16x32_bf16 v[52:55], a[24:27], a[68:71], 0\n\t"
    "ds_read_b64_tr_b16 a[10:11], v129 offset:53248\n\t"
    "v_mfma_f32_16x16x32_bf16 v[52:55], a[28:31], a[84:87], v[52:55]\n\t"
    "v_mfma_f32_16x16x32_bf16 v[56:59], a[24:27], a[72:75], 0\n\t"
    "ds_read_b64_tr_b16 a[12:13], v129 offset:57344\n\t"
    "v_mfma_f32_16x16x32_bf16 v[56:59], a[28:31], a[88:91], v[56:59]\n\t"
    "buffer_load_dwordx4 v139, s[40:43], s45 offen offset:3072 lds\n\t"
    "s_add_u32 s45, s45, 131072\n\t"
    "v_mfma_f32_16x16x32_bf16 v[60:63], a[24:27], a[76:79], 0\n\t"
    "ds_read_b64_tr_b16 a[14:15], v129 offset:61440\n\t"
    "v_xor_b32 v129, 0x10000, v129\n\t"
    "s_add_u32 s52, s52, 16384\n\t"
    "v_mfma_f32_16x16x32_bf16 v[60:63], a[28:31], a[92:95], v[60:63]\n\t"
    "s_waitcnt lgkmcnt(12)\n\t"
    "s_and_b32 s52, s52, 0x1ffff\n\t"
    "v_mfma_f32_16x16x32_bf16 v[64:67], a[32:35], a[64:67], 0\n\t"
    "ds_read_b64_tr_b16 a[16:17], v130 offset:49152\n\t"
    "v_mfma_f32_16x16x32_bf16 v[64:67], a[36:39], a[80:83], v[64:67]\n\t"
    "buffer_load_dwordx4 a[128:131], v140, s[36:39], s44 offen offset:0 sc1\n\t"
    "v_mfma_f32_16x16x32_bf16 v[68:71], a[32:35], a[68:71], 0\n\t"
    "ds_read_b64_tr_b16 a[18:19], v130 offset:53248\n\t"
    "v_mfma_f32_16x16x32_bf16 v[68:71], a[36:39], a[84:87], v[68:71]\n\t"
    "v_mfma_f32_16x16x32_bf16 v[72:75], a[32:35], a[72:75], 0\n\t"
    "ds_read_b64_tr_b16 a[20:21], v130 offset:57344\n\t"
    "v_mfma_f32_16x16x32_bf16 v[72:75], a[36:39], a[88:91], v[72:75]\n\t"
    "buffer_load_dwordx4 a[132:135], v141, s[36:39], s44 offen offset:0 sc1\n\t"
    "v_mfma_f32_16x16x32_bf16 v[76:79], a[32:35], a[76:79], 0\n\t"
    "ds_read_b64_tr_b16 a[22:23], v130 offset:61440\n\t"
    "v_xor_b32 v130, 0x10000, v130\n\t"
    "v_mfma_f32_16x16x32_bf16 v[76:79], a[36:39], a[92:95], v[76:79]\n\t"
    "s_waitcnt lgkmcnt(12)\n\t"
    "v_mfma_f32_16x16x32_bf16 v[80:83], a[40:43], a[64:67], 0\n\t"
    "ds_read_b64_tr_b16 a[24:25], v131 offset:49152\n\t"
    "v_mfma_f32_16x16x32_bf16 v[80:83], a[44:47], a[80:83], v[80:83]\n\t"
    "buffer_load_dwordx4 a[136:139], v142, s[36:39], s44 offen offset:0 sc1\n\t"
    "v_mfma_f32_16x16x32_bf16 v[84:87], a[40:43], a[68:71], 0\n\t"
    "ds_read_b64_tr_b16 a[26:27], v131 offset:53248\n\t"
    "v_mfma_f32_16x16x32_bf16 v[84:87], a[44:47], a[84:87], v[84:87]\n\t"
    "v_mfma_f32_16x16x32_bf16 v[88:91], a[40:43], a[72:75], 0\n\t"
    "ds_read_b64_tr_b16 a[28:29], v131 offset:57344\n\t"
    "v_mfma_f32_16x16x32_bf16 v[88:91], a[44:47], a[88:91], v[88:91]\n\t"
    "buffer_load_dwordx4 a[140:143], v143, s[36:39], s44 offen offset:0 sc1\n\t"
    "v_mfma_f32_16x16x32_bf16 v[92:95], a[40:43], a[76:79], 0\n\t"
    "ds_read_b64_tr_b16 a[30:31], v131 offset:61440\n\t"
    "v_xor_b32 v131, 0x10000, v131\n\t"
    "v_mfma_f32_16x16x32_bf16 v[92:95], a[44:47], a[92:95], v[92:95]\n\t"
    "s_waitcnt lgkmcnt(12)\n\t"
    "v_mfma_f32_16x16x32_bf16 v[96:99], a[48:51], a[64:67], 0\n\t"
    "ds_read_b64_tr_b16 a[32:33], v132 offset:49152\n\t"
    "v_mfma_f32_16x16x32_bf16 v[96:99], a[52:55], a[80:83], v[96:99]\n\t"
    "buffer_load_dwordx4 a[144:147], v140, s[36:39], s44 offen offset:1024 sc1\n\t"
    "v_mfma_f32_16x16x32_bf16 v[100:103], a[48:51], a[68:71], 0\n\t"
    "ds_read_b64_tr_b16 a[34:35], v132 offset:53248\n\t"
    "v_mfma_f32_16x16x32_bf16 v[100:103], a[52:55], a[84:87], v[100:103]\n\t"
    "v_mfma_f32_16x16x32_bf16 v[104:107], a[48:51], a[72:75], 0\n\t"
    "ds_read_b64_tr_b16 a[36:37], v132 offset:57344\n\t"
    "v_mfma_f32_16x16x32_bf16 v[104:107], a[52:55], a[88:91], v[104:107]\n\t"
    "buffer_load_dwordx4 a[148:151], v141, s[36:39], s44 offen offset:1024 sc1\n\t"
    "v_mfma_f32_16x16x32_bf16 v[108:111], a[48:51], a[76:79], 0\n\t"
    "ds_read_b64_tr_b16 a[38:39], v132 offset:61440\n\t"
    "v_xor_b32 v132, 0x10000, v132\n\t"
    "v_mfma_f32_16x16x32_bf16 v[108:111], a[52:55], a[92:95], v[108:111]\n\t"
    "s_waitcnt lgkmcnt(12)\n\t"
    "v_mfma_f32_16x16x32_bf16 v[112:115], a[56:59], a[64:67], 0\n\t"
    "ds_read_b64_tr_b16 a[40:41], v133 offset:49152\n\t"
    "v_mfma_f32_16x16x32_bf16 v[112:115], a[60:63], a[80:83], v[112:115]\n\t"
    "buffer_load_dwordx4 a[152:155], v142, s[36:39], s44 offen offset:1024 sc1\n\t"
    "v_mfma_f32_16x16x32_bf16 v[116:119], a[56:59], a[68:71], 0\n\t"
    "ds_read_b64_tr_b16 a[42:43], v133 offset:53248\n\t"
    "v_mfma_f32_16x16x32_bf16 v[116:119], a[60:63], a[84:87], v[116:119]\n\t"
    "v_mfma_f32_16x16x32_bf16 v[120:123], a[56:59], a[72:75], 0\n\t"
    "ds_read_b64_tr_b16 a[44:45], v133 offset:57344\n\t"
    "v_mfma_f32_16x16x32_bf16 v[120:123], a[60:63], a[88:91], v[120:123]\n\t"
    "buffer_load_dwordx4 a[156:159], v143, s[36:39], s44 offen offset:1024 sc1\n\t"
    "v_mfma_f32_16x16x32_bf16 v[124:127], a[56:59], a[76:79], 0\n\t"
    "ds_read_b64_tr_b16 a[46:47], v133 offset:61440\n\t"
    "v_xor_b32 v133, 0x10000, v133\n\t"
    "v_mfma_f32_16x16x32_bf16 v[124:127], a[60:63], a[92:95], v[124:127]\n\t"
    "s_waitcnt vmcnt(12) lgkmcnt(12)\n\t"
    "v_mfma_f32_16x16x32_bf16 v[0:3], a[0:3], a[96:99], v[0:3]\n\t"
    "ds_read_b64_tr_b16 a[48:49], v134 offset:49152\n\t"
    "s_add_u32 m0, s52, s46\n\t"
    "v_mfma_f32_16x16x32_bf16 v[0:3], a[4:7], a[112:115], v[0:3]\n\t"
    "buffer_load_dwordx4 v136, s[40:43], s45 offen offset:0 lds\n\t"
    "v_mfma_f32_16x16x32_bf16 v[4:7], a[0:3], a[100:103], v[4:7]\n\t"
    "ds_read_b64_tr_b16 a[50:51], v134 offset:53248\n\t"
    "v_mfma_f32_16x16x32_bf16 v[4:7], a[4:7], a[116:119], v[4:7]\n\t"
    "v_mfma_f32_16x16x32_bf16 v[8:11], a[0:3], a[104:107], v[8:11]\n\t"
    "ds_read_b64_tr_b16 a[52:53], v134 offset:57344\n\t"
    "v_mfma_f32_16x16x32_bf16 v[8:11], a[4:7], a[120:123], v[8:11]\n\t"
    "buffer_load_dwordx4 v137, s[40:43], s45 offen offset:1024 lds\n\t"
    "v_mfma_f32_16x16x32_bf16 v[12:15], a[0:3], a[108:111], v[12:15]\n\t"
    "ds_read_b64_tr_b16 a[54:55], v134 offset:61440\n\t"
    "v_xor_b32 v134, 0x10000, v134\n\t"
    "v_mfma_f32_16x16x32_bf16 v[12:15], a[4:7], a[124:127], v[12:15]\n\t"
    "s_waitcnt lgkmcnt(12)\n\t"
    "v_mfma_f32_16x16x32_bf16 v[16:19], a[8:11], a[96:99], v[16:19]\n\t"
    "ds_read_b64_tr_b16 a[56:57], v135 offset:49152\n\t"
    "v_mfma_f32_16x16x32_bf16 v[16:19], a[12:15], a[112:115], v[16:19]\n\t"
    "buffer_load_dwordx4 v138, s[40:43], s45 offen offset:2048 lds\n\t"
    "v_mfma_f32_16x16x32_bf16 v[20:23], a[8:11], a[100:103], v[20:23]\n\t"
    "ds_read_b64_tr_b16 a[58:59], v135 offset:53248\n\t"
    "v_mfma_f32_16x16x32_bf16 v[20:23], a[12:15], a[116:119], v[20:23]\n\t"
    "v_mfma_f32_16x16x32_bf16 v[24:27], a[8:11], a[104:107], v[24:27]\n\t"
    "ds_read_b64_tr_b16 a[60:61], v135 offset:57344\n\t"
    "v_mfma_f32_16x16x32_bf16 v[24:27], a[12:15], a[120:123], v[24:27]\n\t"
    "buffer_load_dwordx4 v139, s[40:43], s45 offen offset:3072 lds\n\t"
    "s_add_u32 s45, s45, 131072\n\t"
    "v_mfma_f32_16x16x32_bf16 v[28:31], a[8:11], a[108:111], v[28:31]\n\t"
    "ds_read_b64_tr_b16 a[62:63], v135 offset:61440\n\t"
    "v_xor_b32 v135, 0x10000, v135\n\t"
    "s_add_u32 s52, s52, 16384\n\t"
    "v_mfma_f32_16x16x32_bf16 v[28:31], a[12:15], a[124:127], v[28:31]\n\t"
    "s_waitcnt lgkmcnt(12) vmcnt(12)\n\t"
    "s_barrier\n\t"
    "s_and_b32 s52, s52, 0x1ffff\n\t"
    "v_mfma_f32_16x16x32_bf16 v[32:35], a[16:19], a[96:99], v[32:35]\n\t"
    "ds_read_b64_tr_b16 a[0:1], v128 offset:0\n\t"
    "v_mfma_f32_16x16x32_bf16 v[32:35], a[20:23], a[112:115], v[32:35]\n\t"
    "buffer_load_dwordx4 a[160:163], v140, s[36:39], s44 offen offset:2048 sc1\n\t"
    "v_mfma_f32_16x16x32_bf16 v[36:39], a[16:19], a[100:103], v[36:39]\n\t"
    "ds_read_b64_tr_b16 a[2:3], v128 offset:4096\n\t"
    "v_mfma_f32_16x16x32_bf16 v[36:39], a[20:23], a[116:119], v[36:39]\n\t"
    "v_mfma_f32_16x16x32_bf16 v[40:43], a[16:19], a[104:107], v[40:43]\n\t"
    "ds_read_b64_tr_b16 a[4:5], v128 offset:8192\n\t"
    "v_mfma_f32_16x16x32_bf16 v[40:43], a[20:23], a[120:123], v[40:43]\n\t"
    "buffer_load_dwordx4 a[164:167], v141, s[36:39], s44 offen offset:2048 sc1\n\t"
    "v_mfma_f32_16x16x32_bf16 v[44:47], a[16:19], a[108:111], v[44:47]\n\t"
    "ds_read_b64_tr_b16 a[6:7], v128 offset:12288\n\t"
    "v_mfma_f32_16x16x32_bf16 v[44:47], a[20:23], a[124:127], v[44:47]\n\t"
    "s_waitcnt lgkmcnt(12)\n\t"
    "v_mfma_f32_16x16x32_bf16 v[48:51], a[24:27], a[96:99], v[48:51]\n\t"
    "ds_read_b64_tr_b16 a[8:9], v129 offset:0\n\t"
    "v_mfma_f32_16x16x32_bf16 v[48:51], a[28:31], a[112:115], v[48:51]\n\t"
    "buffer_load_dwordx4 a[168:171], v142, s[36:39], s44 offen offset:2048 sc1\n\t"
    "v_mfma_f32_16x16x32_bf16 v[52:55], a[24:27], a[100:103], v[52:55]\n\t"
    "ds_read_b64_tr_b16 a[10:11], v129 offset:4096\n\t"
    "v_mfma_f32_16x16x32_bf16 v[52:55], a[28:31], a[116:119], v[52:55]\n\t"
    "v_mfma_f32_16x16x32_bf16 v[56:59], a[24:27], a[104:107], v[56:59]\n\t"
    "ds_read_b64_tr_b16 a[12:13], v129 offset:8192\n\t"
    "v_mfma_f32_16x16x32_bf16 v[56:59], a[28:31], a[120:123], v[56:59]\n\t"
    "buffer_load_dwordx4 a[172:175], v143, s[36:39], s44 offen offset:2048 sc1\n\t"
    "v_mfma_f32_16x16x32_bf16 v[60:63], a[24:27], a[108:111], v[60:63]\n\t"
    "ds_read_b64_tr_b16 a[14:15], v129 offset:12288\n\t"
    "v_mfma_f32_16x16x32_bf16 v[60:63], a[28:31], a[124:127], v[60:63]\n\t"
    "s_waitcnt lgkmcnt(12)\n\t"
    "v_mfma_f32_16x16x32_bf16 v[64:67], a[32:35], a[96:99], v[64:67]\n\t"
    "ds_read_b64_tr_b16 a[16:17], v130 offset:0\n\t"
    "v_mfma_f32_16x16x32_bf16 v[64:67], a[36:39], a[112:115], v[64:67]\n\t"
    "buffer_load_dwordx4 a[176:179], v140, s[36:39], s44 offen offset:3072 sc1\n\t"
    "v_mfma_f32_16x16x32_bf16 v[68:71], a[32:35], a[100:103], v[68:71]\n\t"
    "ds_read_b64_tr_b16 a[18:19], v130 offset:4096\n\t"
    "v_mfma_f32_16x16x32_bf16 v[68:71], a[36:39], a[116:119], v[68:71]\n\t"
    "v_mfma_f32_16x16x32_bf16 v[72:75], a[32:35], a[104:107], v[72:75]\n\t"
    "ds_read_b64_tr_b16 a[20:21], v130 offset:8192\n\t"
    "v_mfma_f32_16x16x32_bf16 v[72:75], a[36:39], a[120:123], v[72:75]\n\t"
    "buffer_load_dwordx4 a[180:183], v141, s[36:39], s44 offen offset:3072 sc1\n\t"
    "v_mfma_f32_16x16x32_bf16 v[76:79], a[32:35], a[108:111], v[76:79]\n\t"
    "ds_read_b64_tr_b16 a[22:23], v130 offset:12288\n\t"
    "v_mfma_f32_16x16x32_bf16 v[76:79], a[36:39], a[124:127], v[76:79]\n\t"
    "s_waitcnt lgkmcnt(12)\n\t"
    "v_mfma_f32_16x16x32_bf16 v[80:83], a[40:43], a[96:99], v[80:83]\n\t"
    "ds_read_b64_tr_b16 a[24:25], v131 offset:0\n\t"
    "v_mfma_f32_16x16x32_bf16 v[80:83], a[44:47], a[112:115], v[80:83]\n\t"
    "buffer_load_dwordx4 a[184:187], v142, s[36:39], s44 offen offset:3072 sc1\n\t"
    "v_mfma_f32_16x16x32_bf16 v[84:87], a[40:43], a[100:103], v[84:87]\n\t"
    "ds_read_b64_tr_b16 a[26:27], v131 offset:4096\n\t"
    "v_mfma_f32_16x16x32_bf16 v[84:87], a[44:47], a[116:119], v[84:87]\n\t"
    "v_mfma_f32_16x16x32_bf16 v[88:91], a[40:43], a[104:107], v[88:91]\n\t"
    "ds_read_b64_tr_b16 a[28:29], v131 offset:8192\n\t"
    "v_mfma_f32_16x16x32_bf16 v[88:91], a[44:47], a[120:123], v[88:91]\n\t"
    "buffer_load_dwordx4 a[188:191], v143, s[36:39], s44 offen offset:3072 sc1\n\t"
    "s_add_u32 s44, s44, 4096\n\t"
    "v_mfma_f32_16x16x32_bf16 v[92:95], a[40:43], a[108:111], v[92:95]\n\t"
    "ds_read_b64_tr_b16 a[30:31], v131 offset:12288\n\t"
    "v_mfma_f32_16x16x32_bf16 v[92:95], a[44:47], a[124:127], v[92:95]\n\t"
    "s_waitcnt lgkmcnt(12)\n\t"
    "v_mfma_f32_16x16x32_bf16 v[96:99], a[48:51], a[96:99], v[96:99]\n\t"
    "ds_read_b64_tr_b16 a[32:33], v132 offset:0\n\t"
    "s_add_u32 m0, s52, s46\n\t"
    "v_mfma_f32_16x16x32_bf16 v[96:99], a[52:55], a[112:115], v[96:99]\n\t"
    "buffer_load_dwordx4 v136, s[40:43], s45 offen offset:0 lds\n\t"
    "v_mfma_f32_16x16x32_bf16 v[100:103], a[48:51], a[100:103], v[100:103]\n\t"
    "ds_read_b64_tr_b16 a[34:35], v132 offset:4096\n\t"
    "v_mfma_f32_16x16x32_bf16 v[100:103], a[52:55], a[116:119], v[100:103]\n\t"
    "v_mfma_f32_16x16x32_bf16 v[104:107], a[48:51], a[104:107], v[104:107]\n\t"
    "ds_read_b64_tr_b16 a[36:37], v132 offset:8192\n\t"
    "v_mfma_f32_16x16x32_bf16 v[104:107], a[52:55], a[120:123], v[104:107]\n\t"
    "buffer_load_dwordx4 v137, s[40:43], s45 offen offset:1024 lds\n\t"
    "v_mfma_f32_16x16x32_bf16 v[108:111], a[48:51], a[108:111], v[108:111]\n\t"
    "ds_read_b64_tr_b16 a[38:39], v132 offset:12288\n\t"
    "v_mfma_f32_16x16x32_bf16 v[108:111], a[52:55], a[124:127], v[108:111]\n\t"
    "s_waitcnt lgkmcnt(12)\n\t"
    "v_mfma_f32_16x16x32_bf16 v[112:115], a[56:59], a[96:99], v[112:115]\n\t"
    "ds_read_b64_tr_b16 a[40:41], v133 offset:0\n\t"
    "v_mfma_f32_16x16x32_bf16 v[112:115], a[60:63], a[112:115], v[112:115]\n\t"
    "buffer_load_dwordx4 v138, s[40:43], s45 offen offset:2048 lds\n\t"
    "v_mfma_f32_16x16x32_bf16 v[116:119], a[56:59], a[100:103], v[116:119]\n\t"
    "ds_read_b64_tr_b16 a[42:43], v133 offset:4096\n\t"
    "v_mfma_f32_16x16x32_bf16 v[116:119], a[60:63], a[116:119], v[116:119]\n\t"
    "v_mfma_f32_16x16x32_bf16 v[120:123], a[56:59], a[104:107], v[120:123]\n\t"
    "ds_read_b64_tr_b16 a[44:45], v133 offset:8192\n\t"
    "v_mfma_f32_16x16x32_bf16 v[120:123], a[60:63], a[120:123], v[120:123]\n\t"
    "buffer_load_dwordx4 v139, s[40:43], s45 offen offset:3072 lds\n\t"
    "s_add_u32 s45, s45, 131072\n\t"
    "v_mfma_f32_16x16x32_bf16 v[124:127], a[56:59], a[108:111], v[124:127]\n\t"
    "ds_read_b64_tr_b16 a[46:47], v133 offset:12288\n\t"
    "s_add_u32 s52, s52, 16384\n\t"
    "v_mfma_f32_16x16x32_bf16 v[124:127], a[60:63], a[124:127], v[124:127]\n\t"
    "s_waitcnt vmcnt(16) lgkmcnt(12)\n\t"
    "s_and_b32 s52, s52, 0x1ffff\n\t"
    "v_mfma_f32_16x16x32_bf16 v[0:3], a[0:3], a[128:131], v[0:3]\n\t"
    "ds_read_b64_tr_b16 a[48:49], v134 offset:0\n\t"
    "v_mfma_f32_16x16x32_bf16 v[0:3], a[4:7], a[144:147], v[0:3]\n\t"
    "buffer_load_dwordx4 a[64:67], v140, s[36:39], s44 offen offset:0 sc1\n\t"
    "v_mfma_f32_16x16x32_bf16 v[4:7], a[0:3], a[132:135], v[4:7]\n\t"
    "ds_read_b64_tr_b16 a[50:51], v134 offset:4096\n\t"
    "v_mfma_f32_16x16x32_bf16 v[4:7], a[4:7], a[148:151], v[4:7]\n\t"
    "v_mfma_f32_16x16x32_bf16 v[8:11], a[0:3], a[136:139], v[8:11]\n\t"
    "ds_read_b64_tr_b16 a[52:53], v134 offset:8192\n\t"
    "v_mfma_f32_16x16x32_bf16 v[8:11], a[4:7], a[152:155], v[8:11]\n\t"
    "buffer_load_dwordx4 a[68:71], v141, s[36:39], s44 offen offset:0 sc1\n\t"
    "v_mfma_f32_16x16x32_bf16 v[12:15], a[0:3], a[140:143], v[12:15]\n\t"
    "ds_read_b64_tr_b16 a[54:55], v134 offset:12288\n\t"
    "v_mfma_f32_16x16x32_bf16 v[12:15], a[4:7], a[156:159], v[12:15]\n\t"
    "s_waitcnt lgkmcnt(12)\n\t"
    "v_mfma_f32_16x16x32_bf16 v[16:19], a[8:11], a[128:131], v[16:19]\n\t"
    "ds_read_b64_tr_b16 a[56:57], v135 offset:0\n\t"
    "v_mfma_f32_16x16x32_bf16 v[16:19], a[12:15], a[144:147], v[16:19]\n\t"
    "buffer_load_dwordx4 a[72:75], v142, s[36:39], s44 offen offset:0 sc1\n\t"
    "v_mfma_f32_16x16x32_bf16 v[20:23], a[8:11], a[132:135], v[20:23]\n\t"
    "ds_read_b64_tr_b16 a[58:59], v135 offset:4096\n\t"
    "v_mfma_f32_16x16x32_bf16 v[20:23], a[12:15], a[148:151], v[20:23]\n\t"
    "v_mfma_f32_16x16x32_bf16 v[24:27], a[8:11], a[136:139], v[24:27]\n\t"
    "ds_read_b64_tr_b16 a[60:61], v135 offset:8192\n\t"
    "v_mfma_f32_16x16x32_bf16 v[24:27], a[12:15], a[152:155], v[24:27]\n\t"
    "buffer_load_dwordx4 a[76:79], v143, s[36:39], s44 offen offset:0 sc1\n\t"
    "v_mfma_f32_16x16x32_bf16 v[28:31], a[8:11], a[140:143], v[28:31]\n\t"
    "ds_read_b64_tr_b16 a[62:63], v135 offset:12288\n\t"
    "v_mfma_f32_16x16x32_bf16 v[28:31], a[12:15], a[156:159], v[28:31]\n\t"
    "s_waitcnt lgkmcnt(12) vmcnt(16)\n\t"
    "s_barrier\n\t"
    "v_mfma_f32_16x16x32_bf16 v[32:35], a[16:19], a[128:131], v[32:35]\n\t"
    "ds_read_b64_tr_b16 a[0:1], v128 offset:16384\n\t"
    "v_mfma_f32_16x16x32_bf16 v[32:35], a[20:23], a[144:147], v[32:35]\n\t"
    "buffer_load_dwordx4 a[80:83], v140, s[36:39], s44 offen offset:1024 sc1\n\t"
    "v_mfma_f32_16x16x32_bf16 v[36:39], a[16:19], a[132:135], v[36:39]\n\t"
    "ds_read_b64_tr_b16 a[2:3], v128 offset:20480\n\t"
    "v_mfma_f32_16x16x32_bf16 v[36:39], a[20:23], a[148:151], v[36:39]\n\t"
    "v_mfma_f32_16x16x32_bf16 v[40:43], a[16:19], a[136:139], v[40:43]\n\t"
    "ds_read_b64_tr_b16 a[4:5], v128 offset:24576\n\t"
    "v_mfma_f32_16x16x32_bf16 v[40:43], a[20:23], a[152:155], v[40:43]\n\t"
    "buffer_load_dwordx4 a[84:87], v141, s[36:39], s44 offen offset:1024 sc1\n\t"
    "v_mfma_f32_16x16x32_bf16 v[44:47], a[16:19], a[140:143], v[44:47]\n\t"
    "ds_read_b64_tr_b16 a[6:7], v128 offset:28672\n\t"
    "v_mfma_f32_16x16x32_bf16 v[44:47], a[20:23], a[156:159], v[44:47]\n\t"
    "s_waitcnt lgkmcnt(12)\n\t"
    "v_mfma_f32_16x16x32_bf16 v[48:51], a[24:27], a[128:131], v[48:51]\n\t"
    "ds_read_b64_tr_b16 a[8:9], v129 offset:16384\n\t"
    "v_mfma_f32_16x16x32_bf16 v[48:51], a[28:31], a[144:147], v[48:51]\n\t"
    "buffer_load_dwordx4 a[88:91], v142, s[36:39], s44 offen offset:1024 sc1\n\t"
    "v_mfma_f32_16x16x32_bf16 v[52:55], a[24:27], a[132:135], v[52:55]\n\t"
    "ds_read_b64_tr_b16 a[10:11], v129 offset:20480\n\t"
    "v_mfma_f32_16x16x32_bf16 v[52:55], a[28:31], a[148:151], v[52:55]\n\t"
    "v_mfma_f32_16x16x32_bf16 v[56:59], a[24:27], a[136:139], v[56:59]\n\t"
    "ds_read_b64_tr_b16 a[12:13], v129 offset:24576\n\t"
    "v_mfma_f32_16x16x32_bf16 v[56:59], a[28:31], a[152:155], v[56:59]\n\t"
    "buffer_load_dwordx4 a[92:95], v143, s[36:39], s44 offen offset:1024 sc1\n\t"
    "v_mfma_f32_16x16x32_bf16 v[60:63], a[24:27], a[140:143], v[60:63]\n\t"
    "ds_read_b64_tr_b16 a[14:15], v129 offset:28672\n\t"
    "v_mfma_f32_16x16x32_bf16 v[60:63], a[28:31], a[156:159], v[60:63]\n\t"
    "s_waitcnt lgkmcnt(12)\n\t"
    "v_mfma_f32_16x16x32_bf16 v[64:67], a[32:35], a[128:131], v[64:67]\n\t"
    "ds_read_b64_tr_b16 a[16:17], v130 offset:16384\n\t"
    "s_add_u32 m0, s52, s46\n\t"
    "v_mfma_f32_16x16x32_bf16 v[64:67], a[36:39], a[144:147], v[64:67]\n\t"
    "buffer_load_dwordx4 v136, s[40:43], s45 offen offset:0 lds\n\t"
    "v_mfma_f32_16x16x32_bf16 v[68:71], a[32:35], a[132:135], v[68:71]\n\t"
    "ds_read_b64_tr_b16 a[18:19], v130 offset:20480\n\t"
    "v_mfma_f32_16x16x32_bf16 v[68:71], a[36:39], a[148:151], v[68:71]\n\t"
    "v_mfma_f32_16x16x32_bf16 v[72:75], a[32:35], a[136:139], v[72:75]\n\t"
    "ds_read_b64_tr_b16 a[20:21], v130 offset:24576\n\t"
    "v_mfma_f32_16x16x32_bf16 v[72:75], a[36:39], a[152:155], v[72:75]\n\t"
    "buffer_load_dwordx4 v137, s[40:43], s45 offen offset:1024 lds\n\t"
    "v_mfma_f32_16x16x32_bf16 v[76:79], a[32:35], a[140:143], v[76:79]\n\t"
    "ds_read_b64_tr_b16 a[22:23], v130 offset:28672\n\t"
    "v_mfma_f32_16x16x32_bf16 v[76:79], a[36:39], a[156:159], v[76:79]\n\t"
    "s_waitcnt lgkmcnt(12)\n\t"
    "v_mfma_f32_16x16x32_bf16 v[80:83], a[40:43], a[128:131], v[80:83]\n\t"
    "ds_read_b64_tr_b16 a[24:25], v131 offset:16384\n\t"
    "v_mfma_f32_16x16x32_bf16 v[80:83], a[44:47], a[144:147], v[80:83]\n\t"
    "buffer_load_dwordx4 v138, s[40:43], s45 offen offset:2048 lds\n\t"
    "v_mfma_f32_16x16x32_bf16 v[84:87], a[40:43], a[132:135], v[84:87]\n\t"
    "ds_read_b64_tr_b16 a[26:27], v131 offset:20480\n\t"
    "v_mfma_f32_16x16x32_bf16 v[84:87], a[44:47], a[148:151], v[84:87]\n\t"
    "v_mfma_f32_16x16x32_bf16 v[88:91], a[40:43], a[136:139], v[88:91]\n\t"
    "ds_read_b64_tr_b16 a[28:29], v131 offset:24576\n\t"
    "v_mfma_f32_16x16x32_bf16 v[88:91], a[44:47], a[152:155], v[88:91]\n\t"
    "buffer_load_dwordx4 v139, s[40:43], s45 offen offset:3072 lds\n\t"
    "s_add_u32 s45, s45, 131072\n\t"
    "v_mfma_f32_16x16x32_bf16 v[92:95], a[40:43], a[140:143], v[92:95]\n\t"
    "ds_read_b64_tr_b16 a[30:31], v131 offset:28672\n\t"
    "s_add_u32 s52, s52, 16384\n\t"
    "v_mfma_f32_16x16x32_bf16 v[92:95], a[44:47], a[156:159], v[92:95]\n\t"
    "s_waitcnt lgkmcnt(12)\n\t"
    "s_and_b32 s52, s52, 0x1ffff\n\t"
    "v_mfma_f32_16x16x32_bf16 v[96:99], a[48:51], a[128:131], v[96:99]\n\t"
    "ds_read_b64_tr_b16 a[32:33], v132 offset:16384\n\t"
    "v_mfma_f32_16x16x32_bf16 v[96:99], a[52:55], a[144:147], v[96:99]\n\t"
    "buffer_load_dwordx4 a[96:99], v140, s[36:39], s44 offen offset:2048 sc1\n\t"
    "v_mfma_f32_16x16x32_bf16 v[100:103], a[48:51], a[132:135], v[100:103]\n\t"
    "ds_read_b64_tr_b16 a[34:35], v132 offset:20480\n\t"
    "v_mfma_f32_16x16x32_bf16 v[100:103], a[52:55], a[148:151], v[100:103]\n\t"
    "v_mfma_f32_16x16x32_bf16 v[104:107], a[48:51], a[136:139], v[104:107]\n\t"
    "ds_read_b64_tr_b16 a[36:37], v132 offset:24576\n\t"
    "v_mfma_f32_16x16x32_bf16 v[104:107], a[52:55], a[152:155], v[104:107]\n\t"
    "buffer_load_dwordx4 a[100:103], v141, s[36:39], s44 offen offset:2048 sc1\n\t"
    "v_mfma_f32_16x16x32_bf16 v[108:111], a[48:51], a[140:143], v[108:111]\n\t"
    "ds_read_b64_tr_b16 a[38:39], v132 offset:28672\n\t"
    "v_mfma_f32_16x16x32_bf16 v[108:111], a[52:55], a[156:159], v[108:111]\n\t"
    "s_waitcnt lgkmcnt(12)\n\t"
    "v_mfma_f32_16x16x32_bf16 v[112:115], a[56:59], a[128:131], v[112:115]\n\t"
    "ds_read_b64_tr_b16 a[40:41], v133 offset:16384\n\t"
    "v_mfma_f32_16x16x32_bf16 v[112:115], a[60:63], a[144:147], v[112:115]\n\t"
    "buffer_load_dwordx4 a[104:107], v142, s[36:39], s44 offen offset:2048 sc1\n\t"
    "v_mfma_f32_16x16x32_bf16 v[116:119], a[56:59], a[132:135], v[116:119]\n\t"
    "ds_read_b64_tr_b16 a[42:43], v133 offset:20480\n\t"
    "v_mfma_f32_16x16x32_bf16 v[116:119], a[60:63], a[148:151], v[116:119]\n\t"
    "v_mfma_f32_16x16x32_bf16 v[120:123], a[56:59], a[136:139], v[120:123]\n\t"
    "ds_read_b64_tr_b16 a[44:45], v133 offset:24576\n\t"
    "v_mfma_f32_16x16x32_bf16 v[120:123], a[60:63], a[152:155], v[120:123]\n\t"
    "buffer_load_dwordx4 a[108:111], v143, s[36:39], s44 offen offset:2048 sc1\n\t"
    "v_mfma_f32_16x16x32_bf16 v[124:127], a[56:59], a[140:143], v[124:127]\n\t"
    "ds_read_b64_tr_b16 a[46:47], v133 offset:28672\n\t"
    "v_mfma_f32_16x16x32_bf16 v[124:127], a[60:63], a[156:159], v[124:127]\n\t"
    "s_waitcnt vmcnt(20) lgkmcnt(12)\n\t"
    "v_mfma_f32_16x16x32_bf16 v[0:3], a[0:3], a[160:163], v[0:3]\n\t"
    "ds_read_b64_tr_b16 a[48:49], v134 offset:16384\n\t"
    "v_mfma_f32_16x16x32_bf16 v[0:3], a[4:7], a[176:179], v[0:3]\n\t"
    "v_mfma_f32_16x16x32_bf16 v[4:7], a[0:3], a[164:167], v[4:7]\n\t"
    "ds_read_b64_tr_b16 a[50:51], v134 offset:20480\n\t"
    "v_mfma_f32_16x16x32_bf16 v[4:7], a[4:7], a[180:183], v[4:7]\n\t"
    "v_mfma_f32_16x16x32_bf16 v[8:11], a[0:3], a[168:171], v[8:11]\n\t"
    "ds_read_b64_tr_b16 a[52:53], v134 offset:24576\n\t"
    "v_mfma_f32_16x16x32_bf16 v[8:11], a[4:7], a[184:187], v[8:11]\n\t"
    "v_mfma_f32_16x16x32_bf16 v[12:15], a[0:3], a[172:175], v[12:15]\n\t"
    "ds_read_b64_tr_b16 a[54:55], v134 offset:28672\n\t"
    "v_mfma_f32_16x16x32_bf16 v[12:15], a[4:7], a[188:191], v[12:15]\n\t"
    "s_waitcnt lgkmcnt(12)\n\t"
    "v_mfma_f32_16x16x32_bf16 v[16:19], a[8:11], a[160:163], v[16:19]\n\t"
    "ds_read_b64_tr_b16 a[56:57], v135 offset:16384\n\t"
    "v_mfma_f32_16x16x32_bf16 v[16:19], a[12:15], a[176:179], v[16:19]\n\t"
    "v_mfma_f32_16x16x32_bf16 v[20:23], a[8:11], a[164:167], v[20:23]\n\t"
    "ds_read_b64_tr_b16 a[58:59], v135 offset:20480\n\t"
    "v_mfma_f32_16x16x32_bf16 v[20:23], a[12:15], a[180:183], v[20:23]\n\t"
    "v_mfma_f32_16x16x32_bf16 v[24:27], a[8:11], a[168:171], v[24:27]\n\t"
    "ds_read_b64_tr_b16 a[60:61], v135 offset:24576\n\t"
    "v_mfma_f32_16x16x32_bf16 v[24:27], a[12:15], a[184:187], v[24:27]\n\t"
    "v_mfma_f32_16x16x32_bf16 v[28:31], a[8:11], a[172:175], v[28:31]\n\t"
    "ds_read_b64_tr_b16 a[62:63], v135 offset:28672\n\t"
    "v_mfma_f32_16x16x32_bf16 v[28:31], a[12:15], a[188:191], v[28:31]\n\t"
    "s_waitcnt lgkmcnt(12) vmcnt(16)\n\t"
    "s_barrier\n\t"
    "L_p3_loop%=:\n\t"
    "v_mfma_f32_16x16x32_bf16 v[32:35], a[16:19], a[160:163], v[32:35]\n\t"
    "ds_read_b64_tr_b16 a[0:1], v128 offset:32768\n\t"
    "v_mfma_f32_16x16x32_bf16 v[32:35], a[20:23], a[176:179], v[32:35]\n\t"
    "buffer_load_dwordx4 a[112:115], v140, s[36:39], s44 offen offset:3072 sc1\n\t"
    "v_mfma_f32_16x16x32_bf16 v[36:39], a[16:19], a[164:167], v[36:39]\n\t"
    "ds_read_b64_tr_b16 a[2:3], v128 offset:36864\n\t"
    "v_mfma_f32_16x16x32_bf16 v[36:39], a[20:23], a[180:183], v[36:39]\n\t"
    "v_mfma_f32_16x16x32_bf16 v[40:43], a[16:19], a[168:171], v[40:43]\n\t"
    "ds_read_b64_tr_b16 a[4:5], v128 offset:40960\n\t"
    "s_add_u32 m0, s52, s46\n\t"
    "v_mfma_f32_16x16x32_bf16 v[40:43], a[20:23], a[184:187], v[40:43]\n\t"
    "buffer_load_dwordx4 v136, s[40:43], s45 offen offset:0 lds\n\t"
    "v_mfma_f32_16x16x32_bf16 v[44:47], a[16:19], a[172:175], v[44:47]\n\t"
    "ds_read_b64_tr_b16 a[6:7], v128 offset:45056\n\t"
    "v_mfma_f32_16x16x32_bf16 v[44:47], a[20:23], a[188:191], v[44:47]\n\t"
    "s_waitcnt lgkmcnt(12)\n\t"
    "v_mfma_f32_16x16x32_bf16 v[48:51], a[24:27], a[160:163], v[48:51]\n\t"
    "ds_read_b64_tr_b16 a[8:9], v129 offset:32768\n\t"
    "v_mfma_f32_16x16x32_bf16 v[48:51], a[28:31], a[176:179], v[48:51]\n\t"
    "buffer_load_dwordx4 a[116:119], v141, s[36:39], s44 offen offset:3072 sc1\n\t"
    "v_mfma_f32_16x16x32_bf16 v[52:55], a[24:27], a[164:167], v[52:55]\n\t"
    "ds_read_b64_tr_b16 a[10:11], v129 offset:36864\n\t"
    "v_mfma_f32_16x16x32_bf16 v[52:55], a[28:31], a[180:183], v[52:55]\n\t"
    "v_mfma_f32_16x16x32_bf16 v[56:59], a[24:27], a[168:171], v[56:59]\n\t"
    "ds_read_b64_tr_b16 a[12:13], v129 offset:40960\n\t"
    "v_mfma_f32_16x16x32_bf16 v[56:59], a[28:31], a[184:187], v[56:59]\n\t"
    "v_mfma_f32_16x16x32_bf16 v[60:63], a[24:27], a[172:175], v[60:63]\n\t"
    "ds_read_b64_tr_b16 a[14:15], v129 offset:45056\n\t"
    "v_mfma_f32_16x16x32_bf16 v[60:63], a[28:31], a[188:191], v[60:63]\n\t"
    "s_waitcnt lgkmcnt(12)\n\t"
    "v_mfma_f32_16x16x32_bf16 v[64:67], a[32:35], a[160:163], v[64:67]\n\t"
    "ds_read_b64_tr_b16 a[16:17], v130 offset:32768\n\t"
    "v_mfma_f32_16x16x32_bf16 v[64:67], a[36:39], a[176:179], v[64:67]\n\t"
    "buffer_load_dwordx4 a[120:123], v142, s[36:39], s44 offen offset:3072 sc1\n\t"
    "v_mfma_f32_16x16x32_bf16 v[68:71], a[32:35], a[164:167], v[68:71]\n\t"
    "ds_read_b64_tr_b16 a[18:19], v130 offset:36864\n\t"
    "v_mfma_f32_16x16x32_bf16 v[68:71], a[36:39], a[180:183], v[68:71]\n\t"
    "v_mfma_f32_16x16x32_bf16 v[72:75], a[32:35], a[168:171], v[72:75]\n\t"
    "ds_read_b64_tr_b16 a[20:21], v130 offset:40960\n\t"
    "v_mfma_f32_16x16x32_bf16 v[72:75], a[36:39], a[184:187], v[72:75]\n\t"
    "buffer_load_dwordx4 v137, s[40:43], s45 offen offset:1024 lds\n\t"
    "v_mfma_f32_16x16x32_bf16 v[76:79], a[32:35], a[172:175], v[76:79]\n\t"
    "ds_read_b64_tr_b16 a[22:23], v130 offset:45056\n\t"
    "v_mfma_f32_16x16x32_bf16 v[76:79], a[36:39], a[188:191], v[76:79]\n\t"
    "s_waitcnt lgkmcnt(12)\n\t"
    "v_mfma_f32_16x16x32_bf16 v[80:83], a[40:43], a[160:163], v[80:83]\n\t"
    "ds_read_b64_tr_b16 a[24:25], v131 offset:32768\n\t"
    "v_mfma_f32_16x16x32_bf16 v[80:83], a[44:47], a[176:179], v[80:83]\n\t"
    "buffer_load_dwordx4 a[124:127], v143, s[36:39], s44 offen offset:3072 sc1\n\t"
    "s_add_u32 s44, s44, 4096\n\t"
    "v_mfma_f32_16x16x32_bf16 v[84:87], a[40:43], a[164:167], v[84:87]\n\t"
    "ds_read_b64_tr_b16 a[26:27], v131 offset:36864\n\t"
    "v_mfma_f32_16x16x32_bf16 v[84:87], a[44:47], a[180:183], v[84:87]\n\t"
    "v_mfma_f32_16x16x32_bf16 v[88:91], a[40:43], a[168:171], v[88:91]\n\t"
    "ds_read_b64_tr_b16 a[28:29], v131 offset:40960\n\t"
    "v_mfma_f32_16x16x32_bf16 v[88:91], a[44:47], a[184:187], v[88:91]\n\t"
    "v_mfma_f32_16x16x32_bf16 v[92:95], a[40:43], a[172:175], v[92:95]\n\t"
    "ds_read_b64_tr_b16 a[30:31], v131 offset:45056\n\t"
    "v_mfma_f32_16x16x32_bf16 v[92:95], a[44:47], a[188:191], v[92:95]\n\t"
    "s_waitcnt lgkmcnt(12)\n\t"
    "v_mfma_f32_16x16x32_bf16 v[96:99], a[48:51], a[160:163], v[96:99]\n\t"
    "ds_read_b64_tr_b16 a[32:33], v132 offset:32768\n\t"
    "v_mfma_f32_16x16x32_bf16 v[96:99], a[52:55], a[176:179], v[96:99]\n\t"
    "buffer_load_dwordx4 a[128:131], v140, s[36:39], s44 offen offset:0 sc1\n\t"
    "v_mfma_f32_16x16x32_bf16 v[100:103], a[48:51], a[164:167], v[100:103]\n\t"
    "ds_read_b64_tr_b16 a[34:35], v132 offset:36864\n\t"
    "v_mfma_f32_16x16x32_bf16 v[100:103], a[52:55], a[180:183], v[100:103]\n\t"
    "v_mfma_f32_16x16x32_bf16 v[104:107], a[48:51], a[168:171], v[104:107]\n\t"
    "ds_read_b64_tr_b16 a[36:37], v132 offset:40960\n\t"
    "v_mfma_f32_16x16x32_bf16 v[104:107], a[52:55], a[184:187], v[104:107]\n\t"
    "buffer_load_dwordx4 v138, s[40:43], s45 offen offset:2048 lds\n\t"
    "v_mfma_f32_16x16x32_bf16 v[108:111], a[48:51], a[172:175], v[108:111]\n\t"
    "ds_read_b64_tr_b16 a[38:39], v132 offset:45056\n\t"
    "v_mfma_f32_16x16x32_bf16 v[108:111], a[52:55], a[188:191], v[108:111]\n\t"
    "s_waitcnt lgkmcnt(12)\n\t"
    "v_mfma_f32_16x16x32_bf16 v[112:115], a[56:59], a[160:163], v[112:115]\n\t"
    "ds_read_b64_tr_b16 a[40:41], v133 offset:32768\n\t"
    "v_mfma_f32_16x16x32_bf16 v[112:115], a[60:63], a[176:179], v[112:115]\n\t"
    "buffer_load_dwordx4 a[132:135], v141, s[36:39], s44 offen offset:0 sc1\n\t"
    "v_mfma_f32_16x16x32_bf16 v[116:119], a[56:59], a[164:167], v[116:119]\n\t"
    "ds_read_b64_tr_b16 a[42:43], v133 offset:36864\n\t"
    "v_mfma_f32_16x16x32_bf16 v[116:119], a[60:63], a[180:183], v[116:119]\n\t"
    "v_mfma_f32_16x16x32_bf16 v[120:123], a[56:59], a[168:171], v[120:123]\n\t"
    "ds_read_b64_tr_b16 a[44:45], v133 offset:40960\n\t"
    "v_mfma_f32_16x16x32_bf16 v[120:123], a[60:63], a[184:187], v[120:123]\n\t"
    "v_mfma_f32_16x16x32_bf16 v[124:127], a[56:59], a[172:175], v[124:127]\n\t"
    "ds_read_b64_tr_b16 a[46:47], v133 offset:45056\n\t"
    "v_mfma_f32_16x16x32_bf16 v[124:127], a[60:63], a[188:191], v[124:127]\n\t"
    "s_waitcnt vmcnt(15) lgkmcnt(12)\n\t"
    "v_mfma_f32_16x16x32_bf16 v[0:3], a[0:3], a[64:67], v[0:3]\n\t"
    "ds_read_b64_tr_b16 a[48:49], v134 offset:32768\n\t"
    "v_mfma_f32_16x16x32_bf16 v[0:3], a[4:7], a[80:83], v[0:3]\n\t"
    "buffer_load_dwordx4 a[136:139], v142, s[36:39], s44 offen offset:0 sc1\n\t"
    "v_mfma_f32_16x16x32_bf16 v[4:7], a[0:3], a[68:71], v[4:7]\n\t"
    "ds_read_b64_tr_b16 a[50:51], v134 offset:36864\n\t"
    "v_mfma_f32_16x16x32_bf16 v[4:7], a[4:7], a[84:87], v[4:7]\n\t"
    "v_mfma_f32_16x16x32_bf16 v[8:11], a[0:3], a[72:75], v[8:11]\n\t"
    "ds_read_b64_tr_b16 a[52:53], v134 offset:40960\n\t"
    "v_mfma_f32_16x16x32_bf16 v[8:11], a[4:7], a[88:91], v[8:11]\n\t"
    "buffer_load_dwordx4 v139, s[40:43], s45 offen offset:3072 lds\n\t"
    "s_add_u32 s45, s45, 131072\n\t"
    "v_mfma_f32_16x16x32_bf16 v[12:15], a[0:3], a[76:79], v[12:15]\n\t"
    "ds_read_b64_tr_b16 a[54:55], v134 offset:45056\n\t"
    "s_add_u32 s52, s52, 16384\n\t"
    "v_mfma_f32_16x16x32_bf16 v[12:15], a[4:7], a[92:95], v[12:15]\n\t"
    "s_waitcnt lgkmcnt(12)\n\t"
    "s_and_b32 s52, s52, 0x1ffff\n\t"
    "v_mfma_f32_16x16x32_bf16 v[16:19], a[8:11], a[64:67], v[16:19]\n\t"
    "ds_read_b64_tr_b16 a[56:57], v135 offset:32768\n\t"
    "v_mfma_f32_16x16x32_bf16 v[16:19], a[12:15], a[80:83], v[16:19]\n\t"
    "buffer_load_dwordx4 a[140:143], v143, s[36:39], s44 offen offset:0 sc1\n\t"
    "v_mfma_f32_16x16x32_bf16 v[20:23], a[8:11], a[68:71], v[20:23]\n\t"
    "ds_read_b64_tr_b16 a[58:59], v135 offset:36864\n\t"
    "v_mfma_f32_16x16x32_bf16 v[20:23], a[12:15], a[84:87], v[20:23]\n\t"
    "v_mfma_f32_16x16x32_bf16 v[24:27], a[8:11], a[72:75], v[24:27]\n\t"
    "ds_read_b64_tr_b16 a[60:61], v135 offset:40960\n\t"
    "v_mfma_f32_16x16x32_bf16 v[24:27], a[12:15], a[88:91], v[24:27]\n\t"
    "v_mfma_f32_16x16x32_bf16 v[28:31], a[8:11], a[76:79], v[28:31]\n\t"
    "ds_read_b64_tr_b16 a[62:63], v135 offset:45056\n\t"
    "v_mfma_f32_16x16x32_bf16 v[28:31], a[12:15], a[92:95], v[28:31]\n\t"
    "s_waitcnt lgkmcnt(12) vmcnt(13)\n\t"
    "s_barrier\n\t"
    "v_mfma_f32_16x16x32_bf16 v[32:35], a[16:19], a[64:67], v[32:35]\n\t"
    "ds_read_b64_tr_b16 a[0:1], v128 offset:49152\n\t"
    "v_mfma_f32_16x16x32_bf16 v[32:35], a[20:23], a[80:83], v[32:35]\n\t"
    "buffer_load_dwordx4 a[144:147], v140, s[36:39], s44 offen offset:1024 sc1\n\t"
    "v_mfma_f32_16x16x32_bf16 v[36:39], a[16:19], a[68:71], v[36:39]\n\t"
    "ds_read_b64_tr_b16 a[2:3], v128 offset:53248\n\t"
    "v_mfma_f32_16x16x32_bf16 v[36:39], a[20:23], a[84:87], v[36:39]\n\t"
    "v_mfma_f32_16x16x32_bf16 v[40:43], a[16:19], a[72:75], v[40:43]\n\t"
    "ds_read_b64_tr_b16 a[4:5], v128 offset:57344\n\t"
    "s_add_u32 m0, s52, s46\n\t"
    "v_mfma_f32_16x16x32_bf16 v[40:43], a[20:23], a[88:91], v[40:43]\n\t"
    "buffer_load_dwordx4 v136, s[40:43], s45 offen offset:0 lds\n\t"
    "v_mfma_f32_16x16x32_bf16 v[44:47], a[16:19], a[76:79], v[44:47]\n\t"
    "ds_read_b64_tr_b16 a[6:7], v128 offset:61440\n\t"
    "v_xor_b32 v128, 0x10000, v128\n\t"
    "v_mfma_f32_16x16x32_bf16 v[44:47], a[20:23], a[92:95], v[44:47]\n\t"
    "s_waitcnt lgkmcnt(12)\n\t"
    "v_mfma_f32_16x16x32_bf16 v[48:51], a[24:27], a[64:67], v[48:51]\n\t"
    "ds_read_b64_tr_b16 a[8:9], v129 offset:49152\n\t"
    "v_mfma_f32_16x16x32_bf16 v[48:51], a[28:31], a[80:83], v[48:51]\n\t"
    "buffer_load_dwordx4 a[148:151], v141, s[36:39], s44 offen offset:1024 sc1\n\t"
    "v_mfma_f32_16x16x32_bf16 v[52:55], a[24:27], a[68:71], v[52:55]\n\t"
    "ds_read_b64_tr_b16 a[10:11], v129 offset:53248\n\t"
    "v_mfma_f32_16x16x32_bf16 v[52:55], a[28:31], a[84:87], v[52:55]\n\t"
    "v_mfma_f32_16x16x32_bf16 v[56:59], a[24:27], a[72:75], v[56:59]\n\t"
    "ds_read_b64_tr_b16 a[12:13], v129 offset:57344\n\t"
    "v_mfma_f32_16x16x32_bf16 v[56:59], a[28:31], a[88:91], v[56:59]\n\t"
    "v_mfma_f32_16x16x32_bf16 v[60:63], a[24:27], a[76:79], v[60:63]\n\t"
    "ds_read_b64_tr_b16 a[14:15], v129 offset:61440\n\t"
    "v_xor_b32 v129, 0x10000, v129\n\t"
    "v_mfma_f32_16x16x32_bf16 v[60:63], a[28:31], a[92:95], v[60:63]\n\t"
    "s_waitcnt lgkmcnt(12)\n\t"
    "v_mfma_f32_16x16x32_bf16 v[64:67], a[32:35], a[64:67], v[64:67]\n\t"
    "ds_read_b64_tr_b16 a[16:17], v130 offset:49152\n\t"
    "v_mfma_f32_16x16x32_bf16 v[64:67], a[36:39], a[80:83], v[64:67]\n\t"
    "buffer_load_dwordx4 a[152:155], v142, s[36:39], s44 offen offset:1024 sc1\n\t"
    "v_mfma_f32_16x16x32_bf16 v[68:71], a[32:35], a[68:71], v[68:71]\n\t"
    "ds_read_b64_tr_b16 a[18:19], v130 offset:53248\n\t"
    "v_mfma_f32_16x16x32_bf16 v[68:71], a[36:39], a[84:87], v[68:71]\n\t"
    "v_mfma_f32_16x16x32_bf16 v[72:75], a[32:35], a[72:75], v[72:75]\n\t"
    "ds_read_b64_tr_b16 a[20:21], v130 offset:57344\n\t"
    "v_mfma_f32_16x16x32_bf16 v[72:75], a[36:39], a[88:91], v[72:75]\n\t"
    "buffer_load_dwordx4 v137, s[40:43], s45 offen offset:1024 lds\n\t"
    "v_mfma_f32_16x16x32_bf16 v[76:79], a[32:35], a[76:79], v[76:79]\n\t"
    "ds_read_b64_tr_b16 a[22:23], v130 offset:61440\n\t"
    "v_xor_b32 v130, 0x10000, v130\n\t"
    "v_mfma_f32_16x16x32_bf16 v[76:79], a[36:39], a[92:95], v[76:79]\n\t"
    "s_waitcnt lgkmcnt(12)\n\t"
    "v_mfma_f32_16x16x32_bf16 v[80:83], a[40:43], a[64:67], v[80:83]\n\t"
    "ds_read_b64_tr_b16 a[24:25], v131 offset:49152\n\t"
    "v_mfma_f32_16x16x32_bf16 v[80:83], a[44:47], a[80:83], v[80:83]\n\t"
    "buffer_load_dwordx4 a[156:159], v143, s[36:39], s44 offen offset:1024 sc1\n\t"
    "v_mfma_f32_16x16x32_bf16 v[84:87], a[40:43], a[68:71], v[84:87]\n\t"
    "ds_read_b64_tr_b16 a[26:27], v131 offset:53248\n\t"
    "v_mfma_f32_16x16x32_bf16 v[84:87], a[44:47], a[84:87], v[84:87]\n\t"
    "v_mfma_f32_16x16x32_bf16 v[88:91], a[40:43], a[72:75], v[88:91]\n\t"
    "ds_read_b64_tr_b16 a[28:29], v131 offset:57344\n\t"
    "v_mfma_f32_16x16x32_bf16 v[88:91], a[44:47], a[88:91], v[88:91]\n\t"
    "v_mfma_f32_16x16x32_bf16 v[92:95], a[40:43], a[76:79], v[92:95]\n\t"
    "ds_read_b64_tr_b16 a[30:31], v131 offset:61440\n\t"
    "v_xor_b32 v131, 0x10000, v131\n\t"
    "v_mfma_f32_16x16x32_bf16 v[92:95], a[44:47], a[92:95], v[92:95]\n\t"
    "s_waitcnt lgkmcnt(12)\n\t"
    "v_mfma_f32_16x16x32_bf16 v[96:99], a[48:51], a[64:67], v[96:99]\n\t"
    "ds_read_b64_tr_b16 a[32:33], v132 offset:49152\n\t"
    "v_mfma_f32_16x16x32_bf16 v[96:99], a[52:55], a[80:83], v[96:99]\n\t"
    "buffer_load_dwordx4 a[160:163], v140, s[36:39], s44 offen offset:2048 sc1\n\t"
    "v_mfma_f32_16x16x32_bf16 v[100:103], a[48:51], a[68:71], v[100:103]\n\t"
    "ds_read_b64_tr_b16 a[34:35], v132 offset:53248\n\t"
    "v_mfma_f32_16x16x32_bf16 v[100:103], a[52:55], a[84:87], v[100:103]\n\t"
    "v_mfma_f32_16x16x32_bf16 v[104:107], a[48:51], a[72:75], v[104:107]\n\t"
    "ds_read_b64_tr_b16 a[36:37], v132 offset:57344\n\t"
    "v_mfma_f32_16x16x32_bf16 v[104:107], a[52:55], a[88:91], v[104:107]\n\t"
    "buffer_load_dwordx4 v138, s[40:43], s45 offen offset:2048 lds\n\t"
    "v_mfma_f32_16x16x32_bf16 v[108:111], a[48:51], a[76:79], v[108:111]\n\t"
    "ds_read_b64_tr_b16 a[38:39], v132 offset:61440\n\t"
    "v_xor_b32 v132, 0x10000, v132\n\t"
    "v_mfma_f32_16x16x32_bf16 v[108:111], a[52:55], a[92:95], v[108:111]\n\t"
    "s_waitcnt lgkmcnt(12)\n\t"
    "v_mfma_f32_16x16x32_bf16 v[112:115], a[56:59], a[64:67], v[112:115]\n\t"
    "ds_read_b64_tr_b16 a[40:41], v133 offset:49152\n\t"
    "v_mfma_f32_16x16x32_bf16 v[112:115], a[60:63], a[80:83], v[112:115]\n\t"
    "buffer_load_dwordx4 a[164:167], v141, s[36:39], s44 offen offset:2048 sc1\n\t"
    "v_mfma_f32_16x16x32_bf16 v[116:119], a[56:59], a[68:71], v[116:119]\n\t"
    "ds_read_b64_tr_b16 a[42:43], v133 offset:53248\n\t"
    "v_mfma_f32_16x16x32_bf16 v[116:119], a[60:63], a[84:87], v[116:119]\n\t"
    "v_mfma_f32_16x16x32_bf16 v[120:123], a[56:59], a[72:75], v[120:123]\n\t"
    "ds_read_b64_tr_b16 a[44:45], v133 offset:57344\n\t"
    "v_mfma_f32_16x16x32_bf16 v[120:123], a[60:63], a[88:91], v[120:123]\n\t"
    "v_mfma_f32_16x16x32_bf16 v[124:127], a[56:59], a[76:79], v[124:127]\n\t"
    "ds_read_b64_tr_b16 a[46:47], v133 offset:61440\n\t"
    "v_xor_b32 v133, 0x10000, v133\n\t"
    "v_mfma_f32_16x16x32_bf16 v[124:127], a[60:63], a[92:95], v[124:127]\n\t"
    "s_waitcnt vmcnt(15) lgkmcnt(12)\n\t"
    "v_mfma_f32_16x16x32_bf16 v[0:3], a[0:3], a[96:99], v[0:3]\n\t"
    "ds_read_b64_tr_b16 a[48:49], v134 offset:49152\n\t"
    "v_mfma_f32_16x16x32_bf16 v[0:3], a[4:7], a[112:115], v[0:3]\n\t"
    "buffer_load_dwordx4 a[168:171], v142, s[36:39], s44 offen offset:2048 sc1\n\t"
    "v_mfma_f32_16x16x32_bf16 v[4:7], a[0:3], a[100:103], v[4:7]\n\t"
    "ds_read_b64_tr_b16 a[50:51], v134 offset:53248\n\t"
    "v_mfma_f32_16x16x32_bf16 v[4:7], a[4:7], a[116:119], v[4:7]\n\t"
    "v_mfma_f32_16x16x32_bf16 v[8:11], a[0:3], a[104:107], v[8:11]\n\t"
    "ds_read_b64_tr_b16 a[52:53], v134 offset:57344\n\t"
    "v_mfma_f32_16x16x32_bf16 v[8:11], a[4:7], a[120:123], v[8:11]\n\t"
    "buffer_load_dwordx4 v139, s[40:43], s45 offen offset:3072 lds\n\t"
    "s_add_u32 s45, s45, 131072\n\t"
    "v_mfma_f32_16x16x32_bf16 v[12:15], a[0:3], a[108:111], v[12:15]\n\t"
    "ds_read_b64_tr_b16 a[54:55], v134 offset:61440\n\t"
    "v_xor_b32 v134, 0x10000, v134\n\t"
    "s_add_u32 s52, s52, 16384\n\t"
    "v_mfma_f32_16x16x32_bf16 v[12:15], a[4:7], a[124:127], v[12:15]\n\t"
    "s_waitcnt lgkmcnt(12)\n\t"
    "s_and_b32 s52, s52, 0x1ffff\n\t"
    "v_mfma_f32_16x16x32_bf16 v[16:19], a[8:11], a[96:99], v[16:19]\n\t"
    "ds_read_b64_tr_b16 a[56:57], v135 offset:49152\n\t"
    "v_mfma_f32_16x16x32_bf16 v[16:19], a[12:15], a[112:115], v[16:19]\n\t"
    "buffer_load_dwordx4 a[172:175], v143, s[36:39], s44 offen offset:2048 sc1\n\t"
    "v_mfma_f32_16x16x32_bf16 v[20:23], a[8:11], a[100:103], v[20:23]\n\t"
    "ds_read_b64_tr_b16 a[58:59], v135 offset:53248\n\t"
    "v_mfma_f32_16x16x32_bf16 v[20:23], a[12:15], a[116:119], v[20:23]\n\t"
    "v_mfma_f32_16x16x32_bf16 v[24:27], a[8:11], a[104:107], v[24:27]\n\t"
    "ds_read_b64_tr_b16 a[60:61], v135 offset:57344\n\t"
    "v_mfma_f32_16x16x32_bf16 v[24:27], a[12:15], a[120:123], v[24:27]\n\t"
    "v_mfma_f32_16x16x32_bf16 v[28:31], a[8:11], a[108:111], v[28:31]\n\t"
    "ds_read_b64_tr_b16 a[62:63], v135 offset:61440\n\t"
    "v_xor_b32 v135, 0x10000, v135\n\t"
    "v_mfma_f32_16x16x32_bf16 v[28:31], a[12:15], a[124:127], v[28:31]\n\t"
    "s_waitcnt lgkmcnt(12) vmcnt(13)\n\t"
    "s_barrier\n\t"
    "v_mfma_f32_16x16x32_bf16 v[32:35], a[16:19], a[96:99], v[32:35]\n\t"
    "ds_read_b64_tr_b16 a[0:1], v128 offset:0\n\t"
    "v_mfma_f32_16x16x32_bf16 v[32:35], a[20:23], a[112:115], v[32:35]\n\t"
    "buffer_load_dwordx4 a[176:179], v140, s[36:39], s44 offen offset:3072 sc1\n\t"
    "v_mfma_f32_16x16x32_bf16 v[36:39], a[16:19], a[100:103], v[36:39]\n\t"
    "ds_read_b64_tr_b16 a[2:3], v128 offset:4096\n\t"
    "v_mfma_f32_16x16x32_bf16 v[36:39], a[20:23], a[116:119], v[36:39]\n\t"
    "v_mfma_f32_16x16x32_bf16 v[40:43], a[16:19], a[104:107], v[40:43]\n\t"
    "ds_read_b64_tr_b16 a[4:5], v128 offset:8192\n\t"
    "s_add_u32 m0, s52, s46\n\t"
    "v_mfma_f32_16x16x32_bf16 v[40:43], a[20:23], a[120:123], v[40:43]\n\t"
    "buffer_load_dwordx4 v136, s[40:43], s45 offen offset:0 lds\n\t"
    "v_mfma_f32_16x16x32_bf16 v[44:47], a[16:19], a[108:111], v[44:47]\n\t"
    "ds_read_b64_tr_b16 a[6:7], v128 offset:12288\n\t"
    "v_mfma_f32_16x16x32_bf16 v[44:47], a[20:23], a[124:127], v[44:47]\n\t"
    "s_waitcnt lgkmcnt(12)\n\t"
    "v_mfma_f32_16x16x32_bf16 v[48:51], a[24:27], a[96:99], v[48:51]\n\t"
    "ds_read_b64_tr_b16 a[8:9], v129 offset:0\n\t"
    "v_mfma_f32_16x16x32_bf16 v[48:51], a[28:31], a[112:115], v[48:51]\n\t"
    "buffer_load_dwordx4 a[180:183], v141, s[36:39], s44 offen offset:3072 sc1\n\t"
    "v_mfma_f32_16x16x32_bf16 v[52:55], a[24:27], a[100:103], v[52:55]\n\t"
    "ds_read_b64_tr_b16 a[10:11], v129 offset:4096\n\t"
    "v_mfma_f32_16x16x32_bf16 v[52:55], a[28:31], a[116:119], v[52:55]\n\t"
    "v_mfma_f32_16x16x32_bf16 v[56:59], a[24:27], a[104:107], v[56:59]\n\t"
    "ds_read_b64_tr_b16 a[12:13], v129 offset:8192\n\t"
    "v_mfma_f32_16x16x32_bf16 v[56:59], a[28:31], a[120:123], v[56:59]\n\t"
    "v_mfma_f32_16x16x32_bf16 v[60:63], a[24:27], a[108:111], v[60:63]\n\t"
    "ds_read_b64_tr_b16 a[14:15], v129 offset:12288\n\t"
    "v_mfma_f32_16x16x32_bf16 v[60:63], a[28:31], a[124:127], v[60:63]\n\t"
    "s_waitcnt lgkmcnt(12)\n\t"
    "v_mfma_f32_16x16x32_bf16 v[64:67], a[32:35], a[96:99], v[64:67]\n\t"
    "ds_read_b64_tr_b16 a[16:17], v130 offset:0\n\t"
    "v_mfma_f32_16x16x32_bf16 v[64:67], a[36:39], a[112:115], v[64:67]\n\t"
    "buffer_load_dwordx4 a[184:187], v142, s[36:39], s44 offen offset:3072 sc1\n\t"
    "v_mfma_f32_16x16x32_bf16 v[68:71], a[32:35], a[100:103], v[68:71]\n\t"
    "ds_read_b64_tr_b16 a[18:19], v130 offset:4096\n\t"
    "v_mfma_f32_16x16x32_bf16 v[68:71], a[36:39], a[116:119], v[68:71]\n\t"
    "v_mfma_f32_16x16x32_bf16 v[72:75], a[32:35], a[104:107], v[72:75]\n\t"
    "ds_read_b64_tr_b16 a[20:21], v130 offset:8192\n\t"
    "v_mfma_f32_16x16x32_bf16 v[72:75], a[36:39], a[120:123], v[72:75]\n\t"
    "buffer_load_dwordx4 v137, s[40:43], s45 offen offset:1024 lds\n\t"
    "v_mfma_f32_16x16x32_bf16 v[76:79], a[32:35], a[108:111], v[76:79]\n\t"
    "ds_read_b64_tr_b16 a[22:23], v130 offset:12288\n\t"
    "v_mfma_f32_16x16x32_bf16 v[76:79], a[36:39], a[124:127], v[76:79]\n\t"
    "s_waitcnt lgkmcnt(12)\n\t"
    "v_mfma_f32_16x16x32_bf16 v[80:83], a[40:43], a[96:99], v[80:83]\n\t"
    "ds_read_b64_tr_b16 a[24:25], v131 offset:0\n\t"
    "v_mfma_f32_16x16x32_bf16 v[80:83], a[44:47], a[112:115], v[80:83]\n\t"
    "buffer_load_dwordx4 a[188:191], v143, s[36:39], s44 offen offset:3072 sc1\n\t"
    "s_add_u32 s44, s44, 4096\n\t"
    "v_mfma_f32_16x16x32_bf16 v[84:87], a[40:43], a[100:103], v[84:87]\n\t"
    "ds_read_b64_tr_b16 a[26:27], v131 offset:4096\n\t"
    "v_mfma_f32_16x16x32_bf16 v[84:87], a[44:47], a[116:119], v[84:87]\n\t"
    "v_mfma_f32_16x16x32_bf16 v[88:91], a[40:43], a[104:107], v[88:91]\n\t"
    "ds_read_b64_tr_b16 a[28:29], v131 offset:8192\n\t"
    "v_mfma_f32_16x16x32_bf16 v[88:91], a[44:47], a[120:123], v[88:91]\n\t"
    "v_mfma_f32_16x16x32_bf16 v[92:95], a[40:43], a[108:111], v[92:95]\n\t"
    "ds_read_b64_tr_b16 a[30:31], v131 offset:12288\n\t"
    "v_mfma_f32_16x16x32_bf16 v[92:95], a[44:47], a[124:127], v[92:95]\n\t"
    "s_waitcnt lgkmcnt(12)\n\t"
    "v_mfma_f32_16x16x32_bf16 v[96:99], a[48:51], a[96:99], v[96:99]\n\t"
    "ds_read_b64_tr_b16 a[32:33], v132 offset:0\n\t"
    "v_mfma_f32_16x16x32_bf16 v[96:99], a[52:55], a[112:115], v[96:99]\n\t"
    "buffer_load_dwordx4 a[64:67], v140, s[36:39], s44 offen offset:0 sc1\n\t"
    "v_mfma_f32_16x16x32_bf16 v[100:103], a[48:51], a[100:103], v[100:103]\n\t"
    "ds_read_b64_tr_b16 a[34:35], v132 offset:4096\n\t"
    "v_mfma_f32_16x16x32_bf16 v[100:103], a[52:55], a[116:119], v[100:103]\n\t"
    "v_mfma_f32_16x16x32_bf16 v[104:107], a[48:51], a[104:107], v[104:107]\n\t"
    "ds_read_b64_tr_b16 a[36:37], v132 offset:8192\n\t"
    "v_mfma_f32_16x16x32_bf16 v[104:107], a[52:55], a[120:123], v[104:107]\n\t"
    "buffer_load_dwordx4 v138, s[40:43], s45 offen offset:2048 lds\n\t"
    "v_mfma_f32_16x16x32_bf16 v[108:111], a[48:51], a[108:111], v[108:111]\n\t"
    "ds_read_b64_tr_b16 a[38:39], v132 offset:12288\n\t"
    "v_mfma_f32_16x16x32_bf16 v[108:111], a[52:55], a[124:127], v[108:111]\n\t"
    "s_waitcnt lgkmcnt(12)\n\t"
    "v_mfma_f32_16x16x32_bf16 v[112:115], a[56:59], a[96:99], v[112:115]\n\t"
    "ds_read_b64_tr_b16 a[40:41], v133 offset:0\n\t"
    "v_mfma_f32_16x16x32_bf16 v[112:115], a[60:63], a[112:115], v[112:115]\n\t"
    "buffer_load_dwordx4 a[68:71], v141, s[36:39], s44 offen offset:0 sc1\n\t"
    "v_mfma_f32_16x16x32_bf16 v[116:119], a[56:59], a[100:103], v[116:119]\n\t"
    "ds_read_b64_tr_b16 a[42:43], v133 offset:4096\n\t"
    "v_mfma_f32_16x16x32_bf16 v[116:119], a[60:63], a[116:119], v[116:119]\n\t"
    "v_mfma_f32_16x16x32_bf16 v[120:123], a[56:59], a[104:107], v[120:123]\n\t"
    "ds_read_b64_tr_b16 a[44:45], v133 offset:8192\n\t"
    "v_mfma_f32_16x16x32_bf16 v[120:123], a[60:63], a[120:123], v[120:123]\n\t"
    "v_mfma_f32_16x16x32_bf16 v[124:127], a[56:59], a[108:111], v[124:127]\n\t"
    "ds_read_b64_tr_b16 a[46:47], v133 offset:12288\n\t"
    "v_mfma_f32_16x16x32_bf16 v[124:127], a[60:63], a[124:127], v[124:127]\n\t"
    "s_waitcnt vmcnt(15) lgkmcnt(12)\n\t"
    "v_mfma_f32_16x16x32_bf16 v[0:3], a[0:3], a[128:131], v[0:3]\n\t"
    "ds_read_b64_tr_b16 a[48:49], v134 offset:0\n\t"
    "v_mfma_f32_16x16x32_bf16 v[0:3], a[4:7], a[144:147], v[0:3]\n\t"
    "buffer_load_dwordx4 a[72:75], v142, s[36:39], s44 offen offset:0 sc1\n\t"
    "v_mfma_f32_16x16x32_bf16 v[4:7], a[0:3], a[132:135], v[4:7]\n\t"
    "ds_read_b64_tr_b16 a[50:51], v134 offset:4096\n\t"
    "v_mfma_f32_16x16x32_bf16 v[4:7], a[4:7], a[148:151], v[4:7]\n\t"
    "v_mfma_f32_16x16x32_bf16 v[8:11], a[0:3], a[136:139], v[8:11]\n\t"
    "ds_read_b64_tr_b16 a[52:53], v134 offset:8192\n\t"
    "v_mfma_f32_16x16x32_bf16 v[8:11], a[4:7], a[152:155], v[8:11]\n\t"
    "buffer_load_dwordx4 v139, s[40:43], s45 offen offset:3072 lds\n\t"
    "s_add_u32 s45, s45, 131072\n\t"
    "v_mfma_f32_16x16x32_bf16 v[12:15], a[0:3], a[140:143], v[12:15]\n\t"
    "ds_read_b64_tr_b16 a[54:55], v134 offset:12288\n\t"
    "s_add_u32 s52, s52, 16384\n\t"
    "v_mfma_f32_16x16x32_bf16 v[12:15], a[4:7], a[156:159], v[12:15]\n\t"
    "s_waitcnt lgkmcnt(12)\n\t"
    "s_and_b32 s52, s52, 0x1ffff\n\t"
    "v_mfma_f32_16x16x32_bf16 v[16:19], a[8:11], a[128:131], v[16:19]\n\t"
    "ds_read_b64_tr_b16 a[56:57], v135 offset:0\n\t"
    "v_mfma_f32_16x16x32_bf16 v[16:19], a[12:15], a[144:147], v[16:19]\n\t"
    "buffer_load_dwordx4 a[76:79], v143, s[36:39], s44 offen offset:0 sc1\n\t"
    "v_mfma_f32_16x16x32_bf16 v[20:23], a[8:11], a[132:135], v[20:23]\n\t"
    "ds_read_b64_tr_b16 a[58:59], v135 offset:4096\n\t"
    "v_mfma_f32_16x16x32_bf16 v[20:23], a[12:15], a[148:151], v[20:23]\n\t"
    "v_mfma_f32_16x16x32_bf16 v[24:27], a[8:11], a[136:139], v[24:27]\n\t"
    "ds_read_b64_tr_b16 a[60:61], v135 offset:8192\n\t"
    "v_mfma_f32_16x16x32_bf16 v[24:27], a[12:15], a[152:155], v[24:27]\n\t"
    "v_mfma_f32_16x16x32_bf16 v[28:31], a[8:11], a[140:143], v[28:31]\n\t"
    "ds_read_b64_tr_b16 a[62:63], v135 offset:12288\n\t"
    "v_mfma_f32_16x16x32_bf16 v[28:31], a[12:15], a[156:159], v[28:31]\n\t"
    "s_waitcnt lgkmcnt(12) vmcnt(13)\n\t"
    "s_barrier\n\t"
    "v_mfma_f32_16x16x32_bf16 v[32:35], a[16:19], a[128:131], v[32:35]\n\t"
    "ds_read_b64_tr_b16 a[0:1], v128 offset:16384\n\t"
    "v_mfma_f32_16x16x32_bf16 v[32:35], a[20:23], a[144:147], v[32:35]\n\t"
    "buffer_load_dwordx4 a[80:83], v140, s[36:39], s44 offen offset:1024 sc1\n\t"
    "v_mfma_f32_16x16x32_bf16 v[36:39], a[16:19], a[132:135], v[36:39]\n\t"
    "ds_read_b64_tr_b16 a[2:3], v128 offset:20480\n\t"
    "v_mfma_f32_16x16x32_bf16 v[36:39], a[20:23], a[148:151], v[36:39]\n\t"
    "v_mfma_f32_16x16x32_bf16 v[40:43], a[16:19], a[136:139], v[40:43]\n\t"
    "ds_read_b64_tr_b16 a[4:5], v128 offset:24576\n\t"
    "s_add_u32 m0, s52, s46\n\t"
    "v_mfma_f32_16x16x32_bf16 v[40:43], a[20:23], a[152:155], v[40:43]\n\t"
    "buffer_load_dwordx4 v136, s[40:43], s45 offen offset:0 lds\n\t"
    "v_mfma_f32_16x16x32_bf16 v[44:47], a[16:19], a[140:143], v[44:47]\n\t"
    "ds_read_b64_tr_b16 a[6:7], v128 offset:28672\n\t"
    "v_mfma_f32_16x16x32_bf16 v[44:47], a[20:23], a[156:159], v[44:47]\n\t"
    "s_waitcnt lgkmcnt(12)\n\t"
    "v_mfma_f32_16x16x32_bf16 v[48:51], a[24:27], a[128:131], v[48:51]\n\t"
    "ds_read_b64_tr_b16 a[8:9], v129 offset:16384\n\t"
    "v_mfma_f32_16x16x32_bf16 v[48:51], a[28:31], a[144:147], v[48:51]\n\t"
    "buffer_load_dwordx4 a[84:87], v141, s[36:39], s44 offen offset:1024 sc1\n\t"
    "v_mfma_f32_16x16x32_bf16 v[52:55], a[24:27], a[132:135], v[52:55]\n\t"
    "ds_read_b64_tr_b16 a[10:11], v129 offset:20480\n\t"
    "v_mfma_f32_16x16x32_bf16 v[52:55], a[28:31], a[148:151], v[52:55]\n\t"
    "v_mfma_f32_16x16x32_bf16 v[56:59], a[24:27], a[136:139], v[56:59]\n\t"
    "ds_read_b64_tr_b16 a[12:13], v129 offset:24576\n\t"
    "v_mfma_f32_16x16x32_bf16 v[56:59], a[28:31], a[152:155], v[56:59]\n\t"
    "buffer_load_dword v245, v246, s[92:95], 0 offen sc1\n\t"
    "v_mfma_f32_16x16x32_bf16 v[60:63], a[24:27], a[140:143], v[60:63]\n\t"
    "ds_read_b64_tr_b16 a[14:15], v129 offset:28672\n\t"
    "v_mfma_f32_16x16x32_bf16 v[60:63], a[28:31], a[156:159], v[60:63]\n\t"
    "s_waitcnt lgkmcnt(12)\n\t"
    "v_mfma_f32_16x16x32_bf16 v[64:67], a[32:35], a[128:131], v[64:67]\n\t"
    "ds_read_b64_tr_b16 a[16:17], v130 offset:16384\n\t"
    "v_mfma_f32_16x16x32_bf16 v[64:67], a[36:39], a[144:147], v[64:67]\n\t"
    "buffer_load_dwordx4 a[88:91], v142, s[36:39], s44 offen offset:1024 sc1\n\t"
    "v_mfma_f32_16x16x32_bf16 v[68:71], a[32:35], a[132:135], v[68:71]\n\t"
    "ds_read_b64_tr_b16 a[18:19], v130 offset:20480\n\t"
    "v_mfma_f32_16x16x32_bf16 v[68:71], a[36:39], a[148:151], v[68:71]\n\t"
    "v_mfma_f32_16x16x32_bf16 v[72:75], a[32:35], a[136:139], v[72:75]\n\t"
    "ds_read_b64_tr_b16 a[20:21], v130 offset:24576\n\t"
    "v_mfma_f32_16x16x32_bf16 v[72:75], a[36:39], a[152:155], v[72:75]\n\t"
    "buffer_load_dwordx4 v137, s[40:43], s45 offen offset:1024 lds\n\t"
    "v_mfma_f32_16x16x32_bf16 v[76:79], a[32:35], a[140:143], v[76:79]\n\t"
    "ds_read_b64_tr_b16 a[22:23], v130 offset:28672\n\t"
    "v_mfma_f32_16x16x32_bf16 v[76:79], a[36:39], a[156:159], v[76:79]\n\t"
    "s_waitcnt lgkmcnt(12)\n\t"
    "v_mfma_f32_16x16x32_bf16 v[80:83], a[40:43], a[128:131], v[80:83]\n\t"
    "ds_read_b64_tr_b16 a[24:25], v131 offset:16384\n\t"
    "v_mfma_f32_16x16x32_bf16 v[80:83], a[44:47], a[144:147], v[80:83]\n\t"
    "buffer_load_dwordx4 a[92:95], v143, s[36:39], s44 offen offset:1024 sc1\n\t"
    "v_mfma_f32_16x16x32_bf16 v[84:87], a[40:43], a[132:135], v[84:87]\n\t"
    "ds_read_b64_tr_b16 a[26:27], v131 offset:20480\n\t"
    "v_mfma_f32_16x16x32_bf16 v[84:87], a[44:47], a[148:151], v[84:87]\n\t"
    "v_mfma_f32_16x16x32_bf16 v[88:91], a[40:43], a[136:139], v[88:91]\n\t"
    "ds_read_b64_tr_b16 a[28:29], v131 offset:24576\n\t"
    "v_mfma_f32_16x16x32_bf16 v[88:91], a[44:47], a[152:155], v[88:91]\n\t"
    "v_mfma_f32_16x16x32_bf16 v[92:95], a[40:43], a[140:143], v[92:95]\n\t"
    "ds_read_b64_tr_b16 a[30:31], v131 offset:28672\n\t"
    "v_mfma_f32_16x16x32_bf16 v[92:95], a[44:47], a[156:159], v[92:95]\n\t"
    "s_waitcnt lgkmcnt(12)\n\t"
    "v_mfma_f32_16x16x32_bf16 v[96:99], a[48:51], a[128:131], v[96:99]\n\t"
    "ds_read_b64_tr_b16 a[32:33], v132 offset:16384\n\t"
    "v_mfma_f32_16x16x32_bf16 v[96:99], a[52:55], a[144:147], v[96:99]\n\t"
    "buffer_load_dwordx4 a[96:99], v140, s[36:39], s44 offen offset:2048 sc1\n\t"
    "v_mfma_f32_16x16x32_bf16 v[100:103], a[48:51], a[132:135], v[100:103]\n\t"
    "ds_read_b64_tr_b16 a[34:35], v132 offset:20480\n\t"
    "v_mfma_f32_16x16x32_bf16 v[100:103], a[52:55], a[148:151], v[100:103]\n\t"
    "v_mfma_f32_16x16x32_bf16 v[104:107], a[48:51], a[136:139], v[104:107]\n\t"
    "ds_read_b64_tr_b16 a[36:37], v132 offset:24576\n\t"
    "v_mfma_f32_16x16x32_bf16 v[104:107], a[52:55], a[152:155], v[104:107]\n\t"
    "buffer_load_dwordx4 v138, s[40:43], s45 offen offset:2048 lds\n\t"
    "v_mfma_f32_16x16x32_bf16 v[108:111], a[48:51], a[140:143], v[108:111]\n\t"
    "ds_read_b64_tr_b16 a[38:39], v132 offset:28672\n\t"
    "v_mfma_f32_16x16x32_bf16 v[108:111], a[52:55], a[156:159], v[108:111]\n\t"
    "s_waitcnt lgkmcnt(12)\n\t"
    "v_mfma_f32_16x16x32_bf16 v[112:115], a[56:59], a[128:131], v[112:115]\n\t"
    "ds_read_b64_tr_b16 a[40:41], v133 offset:16384\n\t"
    "v_mfma_f32_16x16x32_bf16 v[112:115], a[60:63], a[144:147], v[112:115]\n\t"
    "buffer_load_dwordx4 a[100:103], v141, s[36:39], s44 offen offset:2048 sc1\n\t"
    "v_mfma_f32_16x16x32_bf16 v[116:119], a[56:59], a[132:135], v[116:119]\n\t"
    "ds_read_b64_tr_b16 a[42:43], v133 offset:20480\n\t"
    "v_mfma_f32_16x16x32_bf16 v[116:119], a[60:63], a[148:151], v[116:119]\n\t"
    "v_mfma_f32_16x16x32_bf16 v[120:123], a[56:59], a[136:139], v[120:123]\n\t"
    "ds_read_b64_tr_b16 a[44:45], v133 offset:24576\n\t"
    "v_mfma_f32_16x16x32_bf16 v[120:123], a[60:63], a[152:155], v[120:123]\n\t"
    "v_mfma_f32_16x16x32_bf16 v[124:127], a[56:59], a[140:143], v[124:127]\n\t"
    "ds_read_b64_tr_b16 a[46:47], v133 offset:28672\n\t"
    "v_mfma_f32_16x16x32_bf16 v[124:127], a[60:63], a[156:159], v[124:127]\n\t"
    "s_waitcnt vmcnt(16) lgkmcnt(12)\n\t"
    "v_mfma_f32_16x16x32_bf16 v[0:3], a[0:3], a[160:163], v[0:3]\n\t"
    "ds_read_b64_tr_b16 a[48:49], v134 offset:16384\n\t"
    "v_mfma_f32_16x16x32_bf16 v[0:3], a[4:7], a[176:179], v[0:3]\n\t"
    "buffer_load_dwordx4 a[104:107], v142, s[36:39], s44 offen offset:2048 sc1\n\t"
    "v_mfma_f32_16x16x32_bf16 v[4:7], a[0:3], a[164:167], v[4:7]\n\t"
    "ds_read_b64_tr_b16 a[50:51], v134 offset:20480\n\t"
    "v_mfma_f32_16x16x32_bf16 v[4:7], a[4:7], a[180:183], v[4:7]\n\t"
    "v_mfma_f32_16x16x32_bf16 v[8:11], a[0:3], a[168:171], v[8:11]\n\t"
    "ds_read_b64_tr_b16 a[52:53], v134 offset:24576\n\t"
    "v_mfma_f32_16x16x32_bf16 v[8:11], a[4:7], a[184:187], v[8:11]\n\t"
    "buffer_load_dwordx4 v139, s[40:43], s45 offen offset:3072 lds\n\t"
    "s_add_u32 s45, s45, 131072\n\t"
    "v_mfma_f32_16x16x32_bf16 v[12:15], a[0:3], a[172:175], v[12:15]\n\t"
    "ds_read_b64_tr_b16 a[54:55], v134 offset:28672\n\t"
    "s_add_u32 s52, s52, 16384\n\t"
    "v_mfma_f32_16x16x32_bf16 v[12:15], a[4:7], a[188:191], v[12:15]\n\t"
    "s_waitcnt lgkmcnt(12)\n\t"
    "s_and_b32 s52, s52, 0x1ffff\n\t"
    "v_mfma_f32_16x16x32_bf16 v[16:19], a[8:11], a[160:163], v[16:19]\n\t"
    "ds_read_b64_tr_b16 a[56:57], v135 offset:16384\n\t"
    "v_mfma_f32_16x16x32_bf16 v[16:19], a[12:15], a[176:179], v[16:19]\n\t"
    "buffer_load_dwordx4 a[108:111], v143, s[36:39], s44 offen offset:2048 sc1\n\t"
    "v_mfma_f32_16x16x32_bf16 v[20:23], a[8:11], a[164:167], v[20:23]\n\t"
    "ds_read_b64_tr_b16 a[58:59], v135 offset:20480\n\t"
    "v_mfma_f32_16x16x32_bf16 v[20:23], a[12:15], a[180:183], v[20:23]\n\t"
    "v_mfma_f32_16x16x32_bf16 v[24:27], a[8:11], a[168:171], v[24:27]\n\t"
    "ds_read_b64_tr_b16 a[60:61], v135 offset:24576\n\t"
    "v_mfma_f32_16x16x32_bf16 v[24:27], a[12:15], a[184:187], v[24:27]\n\t"
    "v_mfma_f32_16x16x32_bf16 v[28:31], a[8:11], a[172:175], v[28:31]\n\t"
    "ds_read_b64_tr_b16 a[62:63], v135 offset:28672\n\t"
    "v_mfma_f32_16x16x32_bf16 v[28:31], a[12:15], a[188:191], v[28:31]\n\t"
    "s_waitcnt lgkmcnt(12) vmcnt(14)\n\t"
    "s_barrier\n\t"
    "s_add_u32 s47, s47, 1\n\t"
    "s_cbranch_scc0 L_p3_loop%=\n\t"
    "s_cmp_lg_u32 s53, 0\n\t"
    "s_cbranch_scc1 L_p3_tail%=\n\t"
    "v_mfma_f32_16x16x32_bf16 v[32:35], a[16:19], a[160:163], v[32:35]\n\t"
    "ds_read_b64_tr_b16 a[0:1], v128 offset:32768\n\t"
    "v_mfma_f32_16x16x32_bf16 v[32:35], a[20:23], a[176:179], v[32:35]\n\t"
    "buffer_load_dwordx4 a[112:115], v140, s[36:39], s44 offen offset:3072 sc1\n\t"
    "v_mfma_f32_16x16x32_bf16 v[36:39], a[16:19], a[164:167], v[36:39]\n\t"
    "ds_read_b64_tr_b16 a[2:3], v128 offset:36864\n\t"
    "s_mov_b32 s40, s76\n\t"
    "s_mov_b32 s41, s77\n\t"
    "s_mov_b32 s42, s78\n\t"
    "v_mfma_f32_16x16x32_bf16 v[36:39], a[20:23], a[180:183], v[36:39]\n\t"
    "s_mov_b32 s45, 0\n\t"
    "v_mfma_f32_16x16x32_bf16 v[40:43], a[16:19], a[168:171], v[40:43]\n\t"
    "ds_read_b64_tr_b16 a[4:5], v128 offset:40960\n\t"
    "s_add_u32 m0, s52, s46\n\t"
    "v_mfma_f32_16x16x32_bf16 v[40:43], a[20:23], a[184:187], v[40:43]\n\t"
    "buffer_load_dwordx4 v136, s[40:43], s45 offen offset:0 lds\n\t"
    "v_mfma_f32_16x16x32_bf16 v[44:47], a[16:19], a[172:175], v[44:47]\n\t"
    "ds_read_b64_tr_b16 a[6:7], v128 offset:45056\n\t"
    "v_mfma_f32_16x16x32_bf16 v[44:47], a[20:23], a[188:191], v[44:47]\n\t"
    "s_waitcnt lgkmcnt(12)\n\t"
    "v_mfma_f32_16x16x32_bf16 v[48:51], a[24:27], a[160:163], v[48:51]\n\t"
    "ds_read_b64_tr_b16 a[8:9], v129 offset:32768\n\t"
    "v_mfma_f32_16x16x32_bf16 v[48:51], a[28:31], a[176:179], v[48:51]\n\t"
    "buffer_load_dwordx4 a[116:119], v141, s[36:39], s44 offen offset:3072 sc1\n\t"
    "v_mfma_f32_16x16x32_bf16 v[52:55], a[24:27], a[164:167], v[52:55]\n\t"
    "ds_read_b64_tr_b16 a[10:11], v129 offset:36864\n\t"
    "v_mfma_f32_16x16x32_bf16 v[52:55], a[28:31], a[180:183], v[52:55]\n\t"
    "v_mfma_f32_16x16x32_bf16 v[56:59], a[24:27], a[168:171], v[56:59]\n\t"
    "ds_read_b64_tr_b16 a[12:13], v129 offset:40960\n\t"
    "v_mfma_f32_16x16x32_bf16 v[56:59], a[28:31], a[184:187], v[56:59]\n\t"
    "v_mfma_f32_16x16x32_bf16 v[60:63], a[24:27], a[172:175], v[60:63]\n\t"
    "ds_read_b64_tr_b16 a[14:15], v129 offset:45056\n\t"
    "v_mfma_f32_16x16x32_bf16 v[60:63], a[28:31], a[188:191], v[60:63]\n\t"
    "s_waitcnt lgkmcnt(12)\n\t"
    "v_mfma_f32_16x16x32_bf16 v[64:67], a[32:35], a[160:163], v[64:67]\n\t"
    "ds_read_b64_tr_b16 a[16:17], v130 offset:32768\n\t"
    "v_mfma_f32_16x16x32_bf16 v[64:67], a[36:39], a[176:179], v[64:67]\n\t"
    "buffer_load_dwordx4 a[120:123], v142, s[36:39], s44 offen offset:3072 sc1\n\t"
    "v_mfma_f32_16x16x32_bf16 v[68:71], a[32:35], a[164:167], v[68:71]\n\t"
    "ds_read_b64_tr_b16 a[18:19], v130 offset:36864\n\t"
    "v_mfma_f32_16x16x32_bf16 v[68:71], a[36:39], a[180:183], v[68:71]\n\t"
    "v_mfma_f32_16x16x32_bf16 v[72:75], a[32:35], a[168:171], v[72:75]\n\t"
    "ds_read_b64_tr_b16 a[20:21], v130 offset:40960\n\t"
    "v_mfma_f32_16x16x32_bf16 v[72:75], a[36:39], a[184:187], v[72:75]\n\t"
    "buffer_load_dwordx4 v137, s[40:43], s45 offen offset:1024 lds\n\t"
    "v_mfma_f32_16x16x32_bf16 v[76:79], a[32:35], a[172:175], v[76:79]\n\t"
    "ds_read_b64_tr_b16 a[22:23], v130 offset:45056\n\t"
    "v_mfma_f32_16x16x32_bf16 v[76:79], a[36:39], a[188:191], v[76:79]\n\t"
    "s_waitcnt lgkmcnt(12)\n\t"
    "v_mfma_f32_16x16x32_bf16 v[80:83], a[40:43], a[160:163], v[80:83]\n\t"
    "ds_read_b64_tr_b16 a[24:25], v131 offset:32768\n\t"
    "v_mfma_f32_16x16x32_bf16 v[80:83], a[44:47], a[176:179], v[80:83]\n\t"
    "buffer_load_dwordx4 a[124:127], v143, s[36:39], s44 offen offset:3072 sc1\n\t"
    "s_add_u32 s44, s44, 4096\n\t"
    "v_mfma_f32_16x16x32_bf16 v[84:87], a[40:43], a[164:167], v[84:87]\n\t"
    "ds_read_b64_tr_b16 a[26:27], v131 offset:36864\n\t"
    "v_mfma_f32_16x16x32_bf16 v[84:87], a[44:47], a[180:183], v[84:87]\n\t"
    "v_mov_b32 v140, v248\n\t"
    "v_mfma_f32_16x16x32_bf16 v[88:91], a[40:43], a[168:171], v[88:91]\n\t"
    "ds_read_b64_tr_b16 a[28:29], v131 offset:40960\n\t"
    "s_mov_b32 s36, s72\n\t"
    "v_mov_b32 v141, v249\n\t"
    "v_mfma_f32_16x16x32_bf16 v[88:91], a[44:47], a[184:187], v[88:91]\n\t"
    "s_mov_b32 s37, s73\n\t"
    "v_mov_b32 v142, v250\n\t"
    "v_mfma_f32_16x16x32_bf16 v[92:95], a[40:43], a[172:175], v[92:95]\n\t"
    "ds_read_b64_tr_b16 a[30:31], v131 offset:45056\n\t"
    "s_mov_b32 s38, s74\n\t"
    "v_mov_b32 v143, v251\n\t"
    "v_mfma_f32_16x16x32_bf16 v[92:95], a[44:47], a[188:191], v[92:95]\n\t"
    "s_waitcnt lgkmcnt(12)\n\t"
    "s_mov_b32 s44, 0\n\t"
    "s_waitcnt vmcnt(15)\n\t"
    "v_cmp_ne_u32 vcc, s71, v245\n\t"
    "s_cbranch_vccz L_p3_ok%=\n\t"
    "s_mov_b32 s47, 0\n\t"
    "L_p3_poll%=:\n\t"
    "s_sleep 4\n\t"
    "buffer_load_dword v245, v246, s[92:95], 0 offen sc1\n\t"
    "s_waitcnt vmcnt(0)\n\t"
    "s_add_u32 s47, s47, 1\n\t"
    "s_cmp_gt_u32 s47, 0x400000\n\t"
    "s_cbranch_scc1 L_p3_ok%=\n\t"
    "v_cmp_ne_u32 vcc, s71, v245\n\t"
    "s_cbranch_vccnz L_p3_poll%=\n\t"
    "L_p3_ok%=:\n\t"
    "v_mfma_f32_16x16x32_bf16 v[96:99], a[48:51], a[160:163], v[96:99]\n\t"
    "ds_read_b64_tr_b16 a[32:33], v132 offset:32768\n\t"
    "v_mfma_f32_16x16x32_bf16 v[96:99], a[52:55], a[176:179], v[96:99]\n\t"
    "buffer_load_dwordx4 a[128:131], v140, s[36:39], s44 offen offset:0 sc1\n\t"
    "v_mfma_f32_16x16x32_bf16 v[100:103], a[48:51], a[164:167], v[100:103]\n\t"
    "ds_read_b64_tr_b16 a[34:35], v132 offset:36864\n\t"
    "v_mfma_f32_16x16x32_bf16 v[100:103], a[52:55], a[180:183], v[100:103]\n\t"
    "v_mfma_f32_16x16x32_bf16 v[104:107], a[48:51], a[168:171], v[104:107]\n\t"
    "ds_read_b64_tr_b16 a[36:37], v132 offset:40960\n\t"
    "v_mfma_f32_16x16x32_bf16 v[104:107], a[52:55], a[184:187], v[104:107]\n\t"
    "buffer_load_dwordx4 v138, s[40:43], s45 offen offset:2048 lds\n\t"
    "v_mfma_f32_16x16x32_bf16 v[108:111], a[48:51], a[172:175], v[108:111]\n\t"
    "ds_read_b64_tr_b16 a[38:39], v132 offset:45056\n\t"
    "v_mfma_f32_16x16x32_bf16 v[108:111], a[52:55], a[188:191], v[108:111]\n\t"
    "s_waitcnt lgkmcnt(12)\n\t"
    "v_mfma_f32_16x16x32_bf16 v[112:115], a[56:59], a[160:163], v[112:115]\n\t"
    "ds_read_b64_tr_b16 a[40:41], v133 offset:32768\n\t"
    "v_mfma_f32_16x16x32_bf16 v[112:115], a[60:63], a[176:179], v[112:115]\n\t"
    "buffer_load_dwordx4 a[132:135], v141, s[36:39], s44 offen offset:0 sc1\n\t"
    "v_mfma_f32_16x16x32_bf16 v[116:119], a[56:59], a[164:167], v[116:119]\n\t"
    "ds_read_b64_tr_b16 a[42:43], v133 offset:36864\n\t"
    "v_mfma_f32_16x16x32_bf16 v[116:119], a[60:63], a[180:183], v[116:119]\n\t"
    "v_mfma_f32_16x16x32_bf16 v[120:123], a[56:59], a[168:171], v[120:123]\n\t"
    "ds_read_b64_tr_b16 a[44:45], v133 offset:40960\n\t"
    "v_mfma_f32_16x16x32_bf16 v[120:123], a[60:63], a[184:187], v[120:123]\n\t"
    "v_mfma_f32_16x16x32_bf16 v[124:127], a[56:59], a[172:175], v[124:127]\n\t"
    "ds_read_b64_tr_b16 a[46:47], v133 offset:45056\n\t"
    "v_mfma_f32_16x16x32_bf16 v[124:127], a[60:63], a[188:191], v[124:127]\n\t"
    "s_waitcnt vmcnt(15) lgkmcnt(12)\n\t"
    "v_mfma_f32_16x16x32_bf16 v[0:3], a[0:3], a[64:67], v[0:3]\n\t"
    "ds_read_b64_tr_b16 a[48:49], v134 offset:32768\n\t"
    "v_mfma_f32_16x16x32_bf16 v[0:3], a[4:7], a[80:83], v[0:3]\n\t"
    "buffer_load_dwordx4 a[136:139], v142, s[36:39], s44 offen offset:0 sc1\n\t"
    "v_mfma_f32_16x16x32_bf16 v[4:7], a[0:3], a[68:71], v[4:7]\n\t"
    "ds_read_b64_tr_b16 a[50:51], v134 offset:36864\n\t"
    "v_mfma_f32_16x16x32_bf16 v[4:7], a[4:7], a[84:87], v[4:7]\n\t"
    "v_mfma_f32_16x16x32_bf16 v[8:11], a[0:3], a[72:75], v[8:11]\n\t"
    "ds_read_b64_tr_b16 a[52:53], v134 offset:40960\n\t"
    "v_mfma_f32_16x16x32_bf16 v[8:11], a[4:7], a[88:91], v[8:11]\n\t"
    "buffer_load_dwordx4 v139, s[40:43], s45 offen offset:3072 lds\n\t"
    "s_add_u32 s45, s45, 131072\n\t"
    "v_mfma_f32_16x16x32_bf16 v[12:15], a[0:3], a[76:79], v[12:15]\n\t"
    "ds_read_b64_tr_b16 a[54:55], v134 offset:45056\n\t"
    "s_add_u32 s52, s52, 16384\n\t"
    "v_mfma_f32_16x16x32_bf16 v[12:15], a[4:7], a[92:95], v[12:15]\n\t"
    "s_waitcnt lgkmcnt(12)\n\t"
    "s_and_b32 s52, s52, 0x1ffff\n\t"
    "v_mfma_f32_16x16x32_bf16 v[16:19], a[8:11], a[64:67], v[16:19]\n\t"
    "ds_read_b64_tr_b16 a[56:57], v135 offset:32768\n\t"
    "v_mfma_f32_16x16x32_bf16 v[16:19], a[12:15], a[80:83], v[16:19]\n\t"
    "buffer_load_dwordx4 a[140:143], v143, s[36:39], s44 offen offset:0 sc1\n\t"
    "v_mfma_f32_16x16x32_bf16 v[20:23], a[8:11], a[68:71], v[20:23]\n\t"
    "ds_read_b64_tr_b16 a[58:59], v135 offset:36864\n\t"
    "v_mfma_f32_16x16x32_bf16 v[20:23], a[12:15], a[84:87], v[20:23]\n\t"
    "v_mfma_f32_16x16x32_bf16 v[24:27], a[8:11], a[72:75], v[24:27]\n\t"
    "ds_read_b64_tr_b16 a[60:61], v135 offset:40960\n\t"
    "v_mfma_f32_16x16x32_bf16 v[24:27], a[12:15], a[88:91], v[24:27]\n\t"
    "v_mfma_f32_16x16x32_bf16 v[28:31], a[8:11], a[76:79], v[28:31]\n\t"
    "ds_read_b64_tr_b16 a[62:63], v135 offset:45056\n\t"
    "v_mfma_f32_16x16x32_bf16 v[28:31], a[12:15], a[92:95], v[28:31]\n\t"
    "s_waitcnt lgkmcnt(12) vmcnt(13)\n\t"
    "s_barrier\n\t"
    "v_mfma_f32_16x16x32_bf16 v[32:35], a[16:19], a[64:67], v[32:35]\n\t"
    "ds_read_b64_tr_b16 a[0:1], v128 offset:49152\n\t"
    "v_mfma_f32_16x16x32_bf16 v[32:35], a[20:23], a[80:83], v[32:35]\n\t"
    "buffer_load_dwordx4 a[144:147], v140, s[36:39], s44 offen offset:1024 sc1\n\t"
    "v_mfma_f32_16x16x32_bf16 v[36:39], a[16:19], a[68:71], v[36:39]\n\t"
    "ds_read_b64_tr_b16 a[2:3], v128 offset:53248\n\t"
    "v_mfma_f32_16x16x32_bf16 v[36:39], a[20:23], a[84:87], v[36:39]\n\t"
    "v_mfma_f32_16x16x32_bf16 v[40:43], a[16:19], a[72:75], v[40:43]\n\t"
    "ds_read_b64_tr_b16 a[4:5], v128 offset:57344\n\t"
    "s_add_u32 m0, s52, s46\n\t"
    "v_mfma_f32_16x16x32_bf16 v[40:43], a[20:23], a[88:91], v[40:43]\n\t"
    "buffer_load_dwordx4 v136, s[40:43], s45 offen offset:0 lds\n\t"
    "v_mfma_f32_16x16x32_bf16 v[44:47], a[16:19], a[76:79], v[44:47]\n\t"
    "ds_read_b64_tr_b16 a[6:7], v128 offset:61440\n\t"
    "v_xor_b32 v128, 0x10000, v128\n\t"
    "v_mfma_f32_16x16x32_bf16 v[44:47], a[20:23], a[92:95], v[44:47]\n\t"
    "s_waitcnt lgkmcnt(12)\n\t"
    "v_mfma_f32_16x16x32_bf16 v[48:51], a[24:27], a[64:67], v[48:51]\n\t"
    "ds_read_b64_tr_b16 a[8:9], v129 offset:49152\n\t"
    "v_mfma_f32_16x16x32_bf16 v[48:51], a[28:31], a[80:83], v[48:51]\n\t"
    "buffer_load_dwordx4 a[148:151], v141, s[36:39], s44 offen offset:1024 sc1\n\t"
    "v_mfma_f32_16x16x32_bf16 v[52:55], a[24:27], a[68:71], v[52:55]\n\t"
    "ds_read_b64_tr_b16 a[10:11], v129 offset:53248\n\t"
    "v_mfma_f32_16x16x32_bf16 v[52:55], a[28:31], a[84:87], v[52:55]\n\t"
    "v_mfma_f32_16x16x32_bf16 v[56:59], a[24:27], a[72:75], v[56:59]\n\t"
    "ds_read_b64_tr_b16 a[12:13], v129 offset:57344\n\t"
    "v_mfma_f32_16x16x32_bf16 v[56:59], a[28:31], a[88:91], v[56:59]\n\t"
    "v_mfma_f32_16x16x32_bf16 v[60:63], a[24:27], a[76:79], v[60:63]\n\t"
    "ds_read_b64_tr_b16 a[14:15], v129 offset:61440\n\t"
    "v_xor_b32 v129, 0x10000, v129\n\t"
    "v_mfma_f32_16x16x32_bf16 v[60:63], a[28:31], a[92:95], v[60:63]\n\t"
    "s_waitcnt lgkmcnt(12)\n\t"
    "v_mfma_f32_16x16x32_bf16 v[64:67], a[32:35], a[64:67], v[64:67]\n\t"
    "ds_read_b64_tr_b16 a[16:17], v130 offset:49152\n\t"
    "v_mfma_f32_16x16x32_bf16 v[64:67], a[36:39], a[80:83], v[64:67]\n\t"
    "buffer_load_dwordx4 a[152:155], v142, s[36:39], s44 offen offset:1024 sc1\n\t"
    "v_mfma_f32_16x16x32_bf16 v[68:71], a[32:35], a[68:71], v[68:71]\n\t"
    "ds_read_b64_tr_b16 a[18:19], v130 offset:53248\n\t"
    "v_mfma_f32_16x16x32_bf16 v[68:71], a[36:39], a[84:87], v[68:71]\n\t"
    "v_mfma_f32_16x16x32_bf16 v[72:75], a[32:35], a[72:75], v[72:75]\n\t"
    "ds_read_b64_tr_b16 a[20:21], v130 offset:57344\n\t"
    "v_mfma_f32_16x16x32_bf16 v[72:75], a[36:39], a[88:91], v[72:75]\n\t"
    "buffer_load_dwordx4 v137, s[40:43], s45 offen offset:1024 lds\n\t"
    "v_mfma_f32_16x16x32_bf16 v[76:79], a[32:35], a[76:79], v[76:79]\n\t"
    "ds_read_b64_tr_b16 a[22:23], v130 offset:61440\n\t"
    "v_xor_b32 v130, 0x10000, v130\n\t"
    "v_mfma_f32_16x16x32_bf16 v[76:79], a[36:39], a[92:95], v[76:79]\n\t"
    "s_waitcnt lgkmcnt(12)\n\t"
    "v_mfma_f32_16x16x32_bf16 v[80:83], a[40:43], a[64:67], v[80:83]\n\t"
    "ds_read_b64_tr_b16 a[24:25], v131 offset:49152\n\t"
    "v_mfma_f32_16x16x32_bf16 v[80:83], a[44:47], a[80:83], v[80:83]\n\t"
    "buffer_load_dwordx4 a[156:159], v143, s[36:39], s44 offen offset:1024 sc1\n\t"
    "v_mfma_f32_16x16x32_bf16 v[84:87], a[40:43], a[68:71], v[84:87]\n\t"
    "ds_read_b64_tr_b16 a[26:27], v131 offset:53248\n\t"
    "v_mfma_f32_16x16x32_bf16 v[84:87], a[44:47], a[84:87], v[84:87]\n\t"
    "v_mfma_f32_16x16x32_bf16 v[88:91], a[40:43], a[72:75], v[88:91]\n\t"
    "ds_read_b64_tr_b16 a[28:29], v131 offset:57344\n\t"
    "v_mfma_f32_16x16x32_bf16 v[88:91], a[44:47], a[88:91], v[88:91]\n\t"
    "v_mfma_f32_16x16x32_bf16 v[92:95], a[40:43], a[76:79], v[92:95]\n\t"
    "ds_read_b64_tr_b16 a[30:31], v131 offset:61440\n\t"
    "v_xor_b32 v131, 0x10000, v131\n\t"
    "v_mfma_f32_16x16x32_bf16 v[92:95], a[44:47], a[92:95], v[92:95]\n\t"
    "s_waitcnt lgkmcnt(12)\n\t"
    "v_mfma_f32_16x16x32_bf16 v[96:99], a[48:51], a[64:67], v[96:99]\n\t"
    "ds_read_b64_tr_b16 a[32:33], v132 offset:49152\n\t"
    "v_mfma_f32_16x16x32_bf16 v[96:99], a[52:55], a[80:83], v[96:99]\n\t"
    "buffer_load_dwordx4 a[160:163], v140, s[36:39], s44 offen offset:2048 sc1\n\t"
    "v_mfma_f32_16x16x32_bf16 v[100:103], a[48:51], a[68:71], v[100:103]\n\t"
    "ds_read_b64_tr_b16 a[34:35], v132 offset:53248\n\t"
    "v_mfma_f32_16x16x32_bf16 v[100:103], a[52:55], a[84:87], v[100:103]\n\t"
    "v_mfma_f32_16x16x32_bf16 v[104:107], a[48:51], a[72:75], v[104:107]\n\t"
    "ds_read_b64_tr_b16 a[36:37], v132 offset:57344\n\t"
    "v_mfma_f32_16x16x32_bf16 v[104:107], a[52:55], a[88:91], v[104:107]\n\t"
    "buffer_load_dwordx4 v138, s[40:43], s45 offen offset:2048 lds\n\t"
    "v_mfma_f32_16x16x32_bf16 v[108:111], a[48:51], a[76:79], v[108:111]\n\t"
    "ds_read_b64_tr_b16 a[38:39], v132 offset:61440\n\t"
    "v_xor_b32 v132, 0x10000, v132\n\t"
    "v_mfma_f32_16x16x32_bf16 v[108:111], a[52:55], a[92:95], v[108:111]\n\t"
    "s_waitcnt lgkmcnt(12)\n\t"
    "v_mfma_f32_16x16x32_bf16 v[112:115], a[56:59], a[64:67], v[112:115]\n\t"
    "ds_read_b64_tr_b16 a[40:41], v133 offset:49152\n\t"
    "v_mfma_f32_16x16x32_bf16 v[112:115], a[60:63], a[80:83], v[112:115]\n\t"
    "buffer_load_dwordx4 a[164:167], v141, s[36:39], s44 offen offset:2048 sc1\n\t"
    "v_mfma_f32_16x16x32_bf16 v[116:119], a[56:59], a[68:71], v[116:119]\n\t"
    "ds_read_b64_tr_b16 a[42:43], v133 offset:53248\n\t"
    "v_mfma_f32_16x16x32_bf16 v[116:119], a[60:63], a[84:87], v[116:119]\n\t"
    "v_mfma_f32_16x16x32_bf16 v[120:123], a[56:59], a[72:75], v[120:123]\n\t"
    "ds_read_b64_tr_b16 a[44:45], v133 offset:57344\n\t"
    "v_mfma_f32_16x16x32_bf16 v[120:123], a[60:63], a[88:91], v[120:123]\n\t"
    "v_mfma_f32_16x16x32_bf16 v[124:127], a[56:59], a[76:79], v[124:127]\n\t"
    "ds_read_b64_tr_b16 a[46:47], v133 offset:61440\n\t"
    "v_xor_b32 v133, 0x10000, v133\n\t"
    "v_mfma_f32_16x16x32_bf16 v[124:127], a[60:63], a[92:95], v[124:127]\n\t"
    "s_waitcnt vmcnt(15) lgkmcnt(12)\n\t"
    "v_mfma_f32_16x16x32_bf16 v[0:3], a[0:3], a[96:99], v[0:3]\n\t"
    "ds_read_b64_tr_b16 a[48:49], v134 offset:49152\n\t"
    "v_mfma_f32_16x16x32_bf16 v[0:3], a[4:7], a[112:115], v[0:3]\n\t"
    "buffer_load_dwordx4 a[168:171], v142, s[36:39], s44 offen offset:2048 sc1\n\t"
    "v_mfma_f32_16x16x32_bf16 v[4:7], a[0:3], a[100:103], v[4:7]\n\t"
    "ds_read_b64_tr_b16 a[50:51], v134 offset:53248\n\t"
    "v_mfma_f32_16x16x32_bf16 v[4:7], a[4:7], a[116:119], v[4:7]\n\t"
    "v_mfma_f32_16x16x32_bf16 v[8:11], a[0:3], a[104:107], v[8:11]\n\t"
    "ds_read_b64_tr_b16 a[52:53], v134 offset:57344\n\t"
    "v_mfma_f32_16x16x32_bf16 v[8:11], a[4:7], a[120:123], v[8:11]\n\t"
    "buffer_load_dwordx4 v139, s[40:43], s45 offen offset:3072 lds\n\t"
    "s_add_u32 s45, s45, 131072\n\t"
    "v_mfma_f32_16x16x32_bf16 v[12:15], a[0:3], a[108:111], v[12:15]\n\t"
    "ds_read_b64_tr_b16 a[54:55], v134 offset:61440\n\t"
    "v_xor_b32 v134, 0x10000, v134\n\t"
    "s_add_u32 s52, s52, 16384\n\t"
    "v_mfma_f32_16x16x32_bf16 v[12:15], a[4:7], a[124:127], v[12:15]\n\t"
    "s_waitcnt lgkmcnt(12)\n\t"
    "s_and_b32 s52, s52, 0x1ffff\n\t"
    "v_mfma_f32_16x16x32_bf16 v[16:19], a[8:11], a[96:99], v[16:19]\n\t"
    "ds_read_b64_tr_b16 a[56:57], v135 offset:49152\n\t"
    "v_mfma_f32_16x16x32_bf16 v[16:19], a[12:15], a[112:115], v[16:19]\n\t"
    "buffer_load_dwordx4 a[172:175], v143, s[36:39], s44 offen offset:2048 sc1\n\t"
    "v_mfma_f32_16x16x32_bf16 v[20:23], a[8:11], a[100:103], v[20:23]\n\t"
    "ds_read_b64_tr_b16 a[58:59], v135 offset:53248\n\t"
    "v_mfma_f32_16x16x32_bf16 v[20:23], a[12:15], a[116:119], v[20:23]\n\t"
    "v_mfma_f32_16x16x32_bf16 v[24:27], a[8:11], a[104:107], v[24:27]\n\t"
    "ds_read_b64_tr_b16 a[60:61], v135 offset:57344\n\t"
    "v_mfma_f32_16x16x32_bf16 v[24:27], a[12:15], a[120:123], v[24:27]\n\t"
    "v_mfma_f32_16x16x32_bf16 v[28:31], a[8:11], a[108:111], v[28:31]\n\t"
    "ds_read_b64_tr_b16 a[62:63], v135 offset:61440\n\t"
    "v_xor_b32 v135, 0x10000, v135\n\t"
    "v_mfma_f32_16x16x32_bf16 v[28:31], a[12:15], a[124:127], v[28:31]\n\t"
    "s_waitcnt lgkmcnt(12) vmcnt(13)\n\t"
    "s_barrier\n\t"
    "v_mfma_f32_16x16x32_bf16 v[32:35], a[16:19], a[96:99], v[32:35]\n\t"
    "ds_read_b64_tr_b16 a[0:1], v128 offset:0\n\t"
    "v_mfma_f32_16x16x32_bf16 v[32:35], a[20:23], a[112:115], v[32:35]\n\t"
    "buffer_load_dwordx4 a[176:179], v140, s[36:39], s44 offen offset:3072 sc1\n\t"
    "v_mfma_f32_16x16x32_bf16 v[36:39], a[16:19], a[100:103], v[36:39]\n\t"
    "ds_read_b64_tr_b16 a[2:3], v128 offset:4096\n\t"
    "v_mfma_f32_16x16x32_bf16 v[36:39], a[20:23], a[116:119], v[36:39]\n\t"
    "v_mfma_f32_16x16x32_bf16 v[40:43], a[16:19], a[104:107], v[40:43]\n\t"
    "ds_read_b64_tr_b16 a[4:5], v128 offset:8192\n\t"
    "s_add_u32 m0, s52, s46\n\t"
    "v_mfma_f32_16x16x32_bf16 v[40:43], a[20:23], a[120:123], v[40:43]\n\t"
    "buffer_load_dwordx4 v136, s[40:43], s45 offen offset:0 lds\n\t"
    "v_mfma_f32_16x16x32_bf16 v[44:47], a[16:19], a[108:111], v[44:47]\n\t"
    "ds_read_b64_tr_b16 a[6:7], v128 offset:12288\n\t"
    "v_mfma_f32_16x16x32_bf16 v[44:47], a[20:23], a[124:127], v[44:47]\n\t"
    "s_waitcnt lgkmcnt(12)\n\t"
    "v_mfma_f32_16x16x32_bf16 v[48:51], a[24:27], a[96:99], v[48:51]\n\t"
    "ds_read_b64_tr_b16 a[8:9], v129 offset:0\n\t"
    "v_mfma_f32_16x16x32_bf16 v[48:51], a[28:31], a[112:115], v[48:51]\n\t"
    "buffer_load_dwordx4 a[180:183], v141, s[36:39], s44 offen offset:3072 sc1\n\t"
    "v_cvt_pk_bf16_f32 v144, v0, v1\n\t"
    "v_mfma_f32_16x16x32_bf16 v[52:55], a[24:27], a[100:103], v[52:55]\n\t"
    "ds_read_b64_tr_b16 a[10:11], v129 offset:4096\n\t"
    "v_min_u32 v144, 0xfffffffe, v144\n\t"
    "v_cvt_pk_bf16_f32 v145, v2, v3\n\t"
    "v_mfma_f32_16x16x32_bf16 v[52:55], a[28:31], a[116:119], v[52:55]\n\t"
    "v_min_u32 v145, 0xfffffffe, v145\n\t"
    "v_cvt_pk_bf16_f32 v146, v16, v17\n\t"
    "v_mfma_f32_16x16x32_bf16 v[56:59], a[24:27], a[104:107], v[56:59]\n\t"
    "ds_read_b64_tr_b16 a[12:13], v129 offset:8192\n\t"
    "v_min_u32 v146, 0xfffffffe, v146\n\t"
    "v_cvt_pk_bf16_f32 v147, v18, v19\n\t"
    "v_mfma_f32_16x16x32_bf16 v[56:59], a[28:31], a[120:123], v[56:59]\n\t"
    "v_min_u32 v147, 0xfffffffe, v147\n\t"
    "v_cvt_pk_bf16_f32 v148, v4, v5\n\t"
    "v_mfma_f32_16x16x32_bf16 v[60:63], a[24:27], a[108:111], v[60:63]\n\t"
    "ds_read_b64_tr_b16 a[14:15], v129 offset:12288\n\t"
    "v_min_u32 v148, 0xfffffffe, v148\n\t"
    "v_cvt_pk_bf16_f32 v149, v6, v7\n\t"
    "v_mfma_f32_16x16x32_bf16 v[60:63], a[28:31], a[124:127], v[60:63]\n\t"
    "buffer_store_dwordx4 v[144:147], v240, s[48:51], 0 offen offset:0 sc1\n\t"
    "s_waitcnt lgkmcnt(12)\n\t"
    "v_min_u32 v149, 0xfffffffe, v149\n\t"
    "v_cvt_pk_bf16_f32 v150, v20, v21\n\t"
    "v_mfma_f32_16x16x32_bf16 v[64:67], a[32:35], a[96:99], v[64:67]\n\t"
    "ds_read_b64_tr_b16 a[16:17], v130 offset:0\n\t"
    "v_min_u32 v150, 0xfffffffe, v150\n\t"
    "v_cvt_pk_bf16_f32 v151, v22, v23\n\t"
    "v_mfma_f32_16x16x32_bf16 v[64:67], a[36:39], a[112:115], v[64:67]\n\t"
    "buffer_load_dwordx4 a[184:187], v142, s[36:39], s44 offen offset:3072 sc1\n\t"
    "v_min_u32 v151, 0xfffffffe, v151\n\t"
    "v_cvt_pk_bf16_f32 v152, v8, v9\n\t"
    "v_mfma_f32_16x16x32_bf16 v[68:71], a[32:35], a[100:103], v[68:71]\n\t"
    "ds_read_b64_tr_b16 a[18:19], v130 offset:4096\n\t"
    "v_min_u32 v152, 0xfffffffe, v152\n\t"
    "v_cvt_pk_bf16_f32 v153, v10, v11\n\t"
    "v_mfma_f32_16x16x32_bf16 v[68:71], a[36:39], a[116:119], v[68:71]\n\t"
    "v_min_u32 v153, 0xfffffffe, v153\n\t"
    "v_cvt_pk_bf16_f32 v154, v24, v25\n\t"
    "v_mfma_f32_16x16x32_bf16 v[72:75], a[32:35], a[104:107], v[72:75]\n\t"
    "ds_read_b64_tr_b16 a[20:21], v130 offset:8192\n\t"
    "v_min_u32 v154, 0xfffffffe, v154\n\t"
    "v_cvt_pk_bf16_f32 v155, v26, v27\n\t"
    "v_mfma_f32_16x16x32_bf16 v[72:75], a[36:39], a[120:123], v[72:75]\n\t"
    "buffer_load_dwordx4 v137, s[40:43], s45 offen offset:1024 lds\n\t"
    "v_min_u32 v155, 0xfffffffe, v155\n\t"
    "v_cvt_pk_bf16_f32 v156, v12, v13\n\t"
    "v_mfma_f32_16x16x32_bf16 v[76:79], a[32:35], a[108:111], v[76:79]\n\t"
    "ds_read_b64_tr_b16 a[22:23], v130 offset:12288\n\t"
    "v_min_u32 v156, 0xfffffffe, v156\n\t"
    "v_cvt_pk_bf16_f32 v157, v14, v15\n\t"
    "v_mfma_f32_16x16x32_bf16 v[76:79], a[36:39], a[124:127], v[76:79]\n\t"
    "buffer_store_dwordx4 v[148:151], v240, s[48:51], 0 offen offset:1024 sc1\n\t"
    "s_waitcnt lgkmcnt(12)\n\t"
    "v_min_u32 v157, 0xfffffffe, v157\n\t"
    "v_cvt_pk_bf16_f32 v158, v28, v29\n\t"
    "v_mfma_f32_16x16x32_bf16 v[80:83], a[40:43], a[96:99], v[80:83]\n\t"
    "ds_read_b64_tr_b16 a[24:25], v131 offset:0\n\t"
    "v_min_u32 v158, 0xfffffffe, v158\n\t"
    "v_cvt_pk_bf16_f32 v159, v30, v31\n\t"
    "v_mfma_f32_16x16x32_bf16 v[80:83], a[44:47], a[112:115], v[80:83]\n\t"
    "buffer_load_dwordx4 a[188:191], v143, s[36:39], s44 offen offset:3072 sc1\n\t"
    "s_add_u32 s44, s44, 4096\n\t"
    "v_min_u32 v159, 0xfffffffe, v159\n\t"
    "v_mov_b32 v192, v32\n\t"
    "v_mov_b32 v193, v33\n\t"
    "v_mfma_f32_16x16x32_bf16 v[84:87], a[40:43], a[100:103], v[84:87]\n\t"
    "ds_read_b64_tr_b16 a[26:27], v131 offset:4096\n\t"
    "v_mov_b32 v194, v34\n\t"
    "v_mov_b32 v195, v35\n\t"
    "v_mfma_f32_16x16x32_bf16 v[84:87], a[44:47], a[116:119], v[84:87]\n\t"
    "v_mov_b32 v196, v36\n\t"
    "v_mov_b32 v197, v37\n\t"
    "v_mfma_f32_16x16x32_bf16 v[88:91], a[40:43], a[104:107], v[88:91]\n\t"
    "ds_read_b64_tr_b16 a[28:29], v131 offset:8192\n\t"
    "v_mov_b32 v198, v38\n\t"
    "v_mov_b32 v199, v39\n\t"
    "v_mfma_f32_16x16x32_bf16 v[88:91], a[44:47], a[120:123], v[88:91]\n\t"
    "v_mov_b32 v200, v40\n\t"
    "v_mov_b32 v201, v41\n\t"
    "v_mfma_f32_16x16x32_bf16 v[92:95], a[40:43], a[108:111], v[92:95]\n\t"
    "ds_read_b64_tr_b16 a[30:31], v131 offset:12288\n\t"
    "v_mov_b32 v202, v42\n\t"
    "v_mov_b32 v203, v43\n\t"
    "v_mfma_f32_16x16x32_bf16 v[92:95], a[44:47], a[124:127], v[92:95]\n\t"
    "buffer_store_dwordx4 v[152:155], v240, s[48:51], 0 offen offset:2048 sc1\n\t"
    "s_waitcnt lgkmcnt(12)\n\t"
    "v_mov_b32 v204, v44\n\t"
    "v_mov_b32 v205, v45\n\t"
    "v_mfma_f32_16x16x32_bf16 v[96:99], a[48:51], a[96:99], v[96:99]\n\t"
    "ds_read_b64_tr_b16 a[32:33], v132 offset:0\n\t"
    "v_mov_b32 v206, v46\n\t"
    "v_mov_b32 v207, v47\n\t"
    "v_mfma_f32_16x16x32_bf16 v[96:99], a[52:55], a[112:115], v[96:99]\n\t"
    "buffer_load_dwordx4 a[64:67], v140, s[36:39], s44 offen offset:0 sc1\n\t"
    "v_mov_b32 v208, v48\n\t"
    "v_mov_b32 v209, v49\n\t"
    "v_mfma_f32_16x16x32_bf16 v[100:103], a[48:51], a[100:103], v[100:103]\n\t"
    "ds_read_b64_tr_b16 a[34:35], v132 offset:4096\n\t"
    "v_mov_b32 v210, v50\n\t"
    "v_mov_b32 v211, v51\n\t"
    "v_mfma_f32_16x16x32_bf16 v[100:103], a[52:55], a[116:119], v[100:103]\n\t"
    "v_mov_b32 v212, v52\n\t"
    "v_mov_b32 v213, v53\n\t"
    "v_mfma_f32_16x16x32_bf16 v[104:107], a[48:51], a[104:107], v[104:107]\n\t"
    "ds_read_b64_tr_b16 a[36:37], v132 offset:8192\n\t"
    "v_mov_b32 v214, v54\n\t"
    "v_mov_b32 v215, v55\n\t"
    "v_mfma_f32_16x16x32_bf16 v[104:107], a[52:55], a[120:123], v[104:107]\n\t"
    "buffer_load_dwordx4 v138, s[40:43], s45 offen offset:2048 lds\n\t"
    "v_mov_b32 v216, v56\n\t"
    "v_mov_b32 v217, v57\n\t"
    "v_mfma_f32_16x16x32_bf16 v[108:111], a[48:51], a[108:111], v[108:111]\n\t"
    "ds_read_b64_tr_b16 a[38:39], v132 offset:12288\n\t"
    "v_mov_b32 v218, v58\n\t"
    "v_mov_b32 v219, v59\n\t"
    "v_mfma_f32_16x16x32_bf16 v[108:111], a[52:55], a[124:127], v[108:111]\n\t"
    "buffer_store_dwordx4 v[156:159], v240, s[48:51], 0 offen offset:3072 sc1\n\t"
    "s_waitcnt lgkmcnt(12)\n\t"
    "v_mov_b32 v220, v60\n\t"
    "v_mov_b32 v221, v61\n\t"
    "v_mfma_f32_16x16x32_bf16 v[112:115], a[56:59], a[96:99], v[112:115]\n\t"
    "ds_read_b64_tr_b16 a[40:41], v133 offset:0\n\t"
    "v_mov_b32 v222, v62\n\t"
    "v_mov_b32 v223, v63\n\t"
    "v_mfma_f32_16x16x32_bf16 v[112:115], a[60:63], a[112:115], v[112:115]\n\t"
    "buffer_load_dwordx4 a[68:71], v141, s[36:39], s44 offen offset:0 sc1\n\t"
    "v_cvt_pk_bf16_f32 v160, v64, v65\n\t"
    "v_mfma_f32_16x16x32_bf16 v[116:119], a[56:59], a[100:103], v[116:119]\n\t"
    "ds_read_b64_tr_b16 a[42:43], v133 offset:4096\n\t"
    "v_min_u32 v160, 0xfffffffe, v160\n\t"
    "v_cvt_pk_bf16_f32 v161, v66, v67\n\t"
    "v_mfma_f32_16x16x32_bf16 v[116:119], a[60:63], a[116:119], v[116:119]\n\t"
    "v_min_u32 v161, 0xfffffffe, v161\n\t"
    "v_cvt_pk_bf16_f32 v162, v80, v81\n\t"
    "v_mfma_f32_16x16x32_bf16 v[120:123], a[56:59], a[104:107], v[120:123]\n\t"
    "ds_read_b64_tr_b16 a[44:45], v133 offset:8192\n\t"
    "v_min_u32 v162, 0xfffffffe, v162\n\t"
    "v_cvt_pk_bf16_f32 v163, v82, v83\n\t"
    "v_mfma_f32_16x16x32_bf16 v[120:123], a[60:63], a[120:123], v[120:123]\n\t"
    "v_min_u32 v163, 0xfffffffe, v163\n\t"
    "v_cvt_pk_bf16_f32 v164, v68, v69\n\t"
    "v_mfma_f32_16x16x32_bf16 v[124:127], a[56:59], a[108:111], v[124:127]\n\t"
    "ds_read_b64_tr_b16 a[46:47], v133 offset:12288\n\t"
    "v_min_u32 v164, 0xfffffffe, v164\n\t"
    "v_cvt_pk_bf16_f32 v165, v70, v71\n\t"
    "v_mfma_f32_16x16x32_bf16 v[124:127], a[60:63], a[124:127], v[124:127]\n\t"
    "buffer_store_dwordx4 v[160:163], v240, s[80:83], 0 offen offset:0 sc1\n\t"
    "s_waitcnt vmcnt(20) lgkmcnt(12)\n\t"
    "v_min_u32 v165, 0xfffffffe, v165\n\t"
    "v_cvt_pk_bf16_f32 v166, v84, v85\n\t"
    "v_mfma_f32_16x16x32_bf16 v[0:3], a[0:3], a[128:131], 0\n\t"
    "ds_read_b64_tr_b16 a[48:49], v134 offset:0\n\t"
    "v_min_u32 v166, 0xfffffffe, v166\n\t"
    "v_cvt_pk_bf16_f32 v167, v86, v87\n\t"
    "v_mfma_f32_16x16x32_bf16 v[0:3], a[4:7], a[144:147], v[0:3]\n\t"
    "buffer_load_dwordx4 a[72:75], v142, s[36:39], s44 offen offset:0 sc1\n\t"
    "v_min_u32 v167, 0xfffffffe, v167\n\t"
    "v_cvt_pk_bf16_f32 v168, v72, v73\n\t"
    "v_mfma_f32_16x16x32_bf16 v[4:7], a[0:3], a[132:135], 0\n\t"
    "ds_read_b64_tr_b16 a[50:51], v134 offset:4096\n\t"
    "v_min_u32 v168, 0xfffffffe, v168\n\t"
    "v_cvt_pk_bf16_f32 v169, v74, v75\n\t"
    "v_mfma_f32_16x16x32_bf16 v[4:7], a[4:7], a[148:151], v[4:7]\n\t"
    "v_min_u32 v169, 0xfffffffe, v169\n\t"
    "v_cvt_pk_bf16_f32 v170, v88, v89\n\t"
    "v_mfma_f32_16x16x32_bf16 v[8:11], a[0:3], a[136:139], 0\n\t"
    "ds_read_b64_tr_b16 a[52:53], v134 offset:8192\n\t"
    "v_min_u32 v170, 0xfffffffe, v170\n\t"
    "v_cvt_pk_bf16_f32 v171, v90, v91\n\t"
    "v_mfma_f32_16x16x32_bf16 v[8:11], a[4:7], a[152:155], v[8:11]\n\t"
    "buffer_load_dwordx4 v139, s[40:43], s45 offen offset:3072 lds\n\t"
    "s_add_u32 s45, s45, 131072\n\t"
    "v_min_u32 v171, 0xfffffffe, v171\n\t"
    "v_cvt_pk_bf16_f32 v172, v76, v77\n\t"
    "v_mfma_f32_16x16x32_bf16 v[12:15], a[0:3], a[140:143], 0\n\t"
    "ds_read_b64_tr_b16 a[54:55], v134 offset:12288\n\t"
    "s_add_u32 s52, s52, 16384\n\t"
    "v_min_u32 v172, 0xfffffffe, v172\n\t"
    "v_cvt_pk_bf16_f32 v173, v78, v79\n\t"
    "v_mfma_f32_16x16x32_bf16 v[12:15], a[4:7], a[156:159], v[12:15]\n\t"
    "buffer_store_dwordx4 v[164:167], v240, s[80:83], 0 offen offset:1024 sc1\n\t"
    "s_waitcnt lgkmcnt(12)\n\t"
    "s_and_b32 s52, s52, 0x1ffff\n\t"
    "v_min_u32 v173, 0xfffffffe, v173\n\t"
    "v_cvt_pk_bf16_f32 v174, v92, v93\n\t"
    "v_mfma_f32_16x16x32_bf16 v[16:19], a[8:11], a[128:131], 0\n\t"
    "ds_read_b64_tr_b16 a[56:57], v135 offset:0\n\t"
    "v_min_u32 v174, 0xfffffffe, v174\n\t"
    "v_cvt_pk_bf16_f32 v175, v94, v95\n\t"
    "v_mfma_f32_16x16x32_bf16 v[16:19], a[12:15], a[144:147], v[16:19]\n\t"
    "buffer_load_dwordx4 a[76:79], v143, s[36:39], s44 offen offset:0 sc1\n\t"
    "v_min_u32 v175, 0xfffffffe, v175\n\t"
    "v_cvt_pk_bf16_f32 v224, v96, v97\n\t"
    "v_mfma_f32_16x16x32_bf16 v[20:23], a[8:11], a[132:135], 0\n\t"
    "ds_read_b64_tr_b16 a[58:59], v135 offset:4096\n\t"
    "v_min_u32 v224, 0xfffffffe, v224\n\t"
    "v_cvt_pk_bf16_f32 v225, v98, v99\n\t"
    "v_mfma_f32_16x16x32_bf16 v[20:23], a[12:15], a[148:151], v[20:23]\n\t"
    "v_min_u32 v225, 0xfffffffe, v225\n\t"
    "v_cvt_pk_bf16_f32 v226, v112, v113\n\t"
    "v_mfma_f32_16x16x32_bf16 v[24:27], a[8:11], a[136:139], 0\n\t"
    "ds_read_b64_tr_b16 a[60:61], v135 offset:8192\n\t"
    "v_min_u32 v226, 0xfffffffe, v226\n\t"
    "v_cvt_pk_bf16_f32 v227, v114, v115\n\t"
    "v_mfma_f32_16x16x32_bf16 v[24:27], a[12:15], a[152:155], v[24:27]\n\t"
    "v_min_u32 v227, 0xfffffffe, v227\n\t"
    "v_cvt_pk_bf16_f32 v228, v100, v101\n\t"
    "v_mfma_f32_16x16x32_bf16 v[28:31], a[8:11], a[140:143], 0\n\t"
    "ds_read_b64_tr_b16 a[62:63], v135 offset:12288\n\t"
    "v_min_u32 v228, 0xfffffffe, v228\n\t"
    "v_cvt_pk_bf16_f32 v229, v102, v103\n\t"
    "v_mfma_f32_16x16x32_bf16 v[28:31], a[12:15], a[156:159], v[28:31]\n\t"
    "buffer_store_dwordx4 v[168:171], v240, s[80:83], 0 offen offset:2048 sc1\n\t"
    "s_waitcnt lgkmcnt(12) vmcnt(20)\n\t"
    "s_barrier\n\t"
    "v_min_u32 v229, 0xfffffffe, v229\n\t"
    "v_cvt_pk_bf16_f32 v230, v116, v117\n\t"
    "v_mfma_f32_16x16x32_bf16 v[32:35], a[16:19], a[128:131], 0\n\t"
    "ds_read_b64_tr_b16 a[0:1], v128 offset:16384\n\t"
    "v_min_u32 v230, 0xfffffffe, v230\n\t"
    "v_cvt_pk_bf16_f32 v231, v118, v119\n\t"
    "v_mfma_f32_16x16x32_bf16 v[32:35], a[20:23], a[144:147], v[32:35]\n\t"
    "buffer_load_dwordx4 a[80:83], v140, s[36:39], s44 offen offset:1024 sc1\n\t"
    "v_min_u32 v231, 0xfffffffe, v231\n\t"
    "v_cvt_pk_bf16_f32 v232, v104, v105\n\t"
    "v_mfma_f32_16x16x32_bf16 v[36:39], a[16:19], a[132:135], 0\n\t"
    "ds_read_b64_tr_b16 a[2:3], v128 offset:20480\n\t"
    "v_min_u32 v232, 0xfffffffe, v232\n\t"
    "v_cvt_pk_bf16_f32 v233, v106, v107\n\t"
    "v_mfma_f32_16x16x32_bf16 v[36:39], a[20:23], a[148:151], v[36:39]\n\t"
    "v_min_u32 v233, 0xfffffffe, v233\n\t"
    "v_cvt_pk_bf16_f32 v234, v120, v121\n\t"
    "v_mfma_f32_16x16x32_bf16 v[40:43], a[16:19], a[136:139], 0\n\t"
    "ds_read_b64_tr_b16 a[4:5], v128 offset:24576\n\t"
    "s_add_u32 m0, s52, s46\n\t"
    "v_min_u32 v234, 0xfffffffe, v234\n\t"
    "v_cvt_pk_bf16_f32 v235, v122, v123\n\t"
    "v_mfma_f32_16x16x32_bf16 v[40:43], a[20:23], a[152:155], v[40:43]\n\t"
    "buffer_load_dwordx4 v136, s[40:43], s45 offen offset:0 lds\n\t"
    "v_min_u32 v235, 0xfffffffe, v235\n\t"
    "v_cvt_pk_bf16_f32 v236, v108, v109\n\t"
    "v_mfma_f32_16x16x32_bf16 v[44:47], a[16:19], a[140:143], 0\n\t"
    "ds_read_b64_tr_b16 a[6:7], v128 offset:28672\n\t"
    "v_min_u32 v236, 0xfffffffe, v236\n\t"
    "v_cvt_pk_bf16_f32 v237, v110, v111\n\t"
    "v_mfma_f32_16x16x32_bf16 v[44:47], a[20:23], a[156:159], v[44:47]\n\t"
    "buffer_store_dwordx4 v[172:175], v240, s[80:83], 0 offen offset:3072 sc1\n\t"
    "s_waitcnt lgkmcnt(12)\n\t"
    "v_min_u32 v237, 0xfffffffe, v237\n\t"
    "v_cvt_pk_bf16_f32 v238, v124, v125\n\t"
    "v_mfma_f32_16x16x32_bf16 v[48:51], a[24:27], a[128:131], 0\n\t"
    "ds_read_b64_tr_b16 a[8:9], v129 offset:16384\n\t"
    "v_min_u32 v238, 0xfffffffe, v238\n\t"
    "v_cvt_pk_bf16_f32 v239, v126, v127\n\t"
    "v_mfma_f32_16x16x32_bf16 v[48:51], a[28:31], a[144:147], v[48:51]\n\t"
    "buffer_load_dwordx4 a[84:87], v141, s[36:39], s44 offen offset:1024 sc1\n\t"
    "v_min_u32 v239, 0xfffffffe, v239\n\t"
    "v_mfma_f32_16x16x32_bf16 v[52:55], a[24:27], a[132:135], 0\n\t"
    "ds_read_b64_tr_b16 a[10:11], v129 offset:20480\n\t"
    "v_mfma_f32_16x16x32_bf16 v[52:55], a[28:31], a[148:151], v[52:55]\n\t"
    "v_mfma_f32_16x16x32_bf16 v[56:59], a[24:27], a[136:139], 0\n\t"
    "ds_read_b64_tr_b16 a[12:13], v129 offset:24576\n\t"
    "v_mfma_f32_16x16x32_bf16 v[56:59], a[28:31], a[152:155], v[56:59]\n\t"
    "buffer_load_dword v245, v246, s[92:95], 0 offen sc1\n\t"
    "v_mfma_f32_16x16x32_bf16 v[60:63], a[24:27], a[140:143], 0\n\t"
    "ds_read_b64_tr_b16 a[14:15], v129 offset:28672\n\t"
    "v_mfma_f32_16x16x32_bf16 v[60:63], a[28:31], a[156:159], v[60:63]\n\t"
    "buffer_store_dwordx4 v[224:227], v240, s[84:87], 0 offen offset:0 sc1\n\t"
    "s_waitcnt lgkmcnt(12)\n\t"
    "v_mfma_f32_16x16x32_bf16 v[64:67], a[32:35], a[128:131], 0\n\t"
    "ds_read_b64_tr_b16 a[16:17], v130 offset:16384\n\t"
    "v_mfma_f32_16x16x32_bf16 v[64:67], a[36:39], a[144:147], v[64:67]\n\t"
    "buffer_load_dwordx4 a[88:91], v142, s[36:39], s44 offen offset:1024 sc1\n\t"
    "v_mfma_f32_16x16x32_bf16 v[68:71], a[32:35], a[132:135], 0\n\t"
    "ds_read_b64_tr_b16 a[18:19], v130 offset:20480\n\t"
    "v_mfma_f32_16x16x32_bf16 v[68:71], a[36:39], a[148:151], v[68:71]\n\t"
    "v_mfma_f32_16x16x32_bf16 v[72:75], a[32:35], a[136:139], 0\n\t"
    "ds_read_b64_tr_b16 a[20:21], v130 offset:24576\n\t"
    "v_mfma_f32_16x16x32_bf16 v[72:75], a[36:39], a[152:155], v[72:75]\n\t"
    "buffer_load_dwordx4 v137, s[40:43], s45 offen offset:1024 lds\n\t"
    "v_mfma_f32_16x16x32_bf16 v[76:79], a[32:35], a[140:143], 0\n\t"
    "ds_read_b64_tr_b16 a[22:23], v130 offset:28672\n\t"
    "v_mfma_f32_16x16x32_bf16 v[76:79], a[36:39], a[156:159], v[76:79]\n\t"
    "buffer_store_dwordx4 v[228:231], v240, s[84:87], 0 offen offset:1024 sc1\n\t"
    "s_waitcnt lgkmcnt(12)\n\t"
    "v_mfma_f32_16x16x32_bf16 v[80:83], a[40:43], a[128:131], 0\n\t"
    "ds_read_b64_tr_b16 a[24:25], v131 offset:16384\n\t"
    "v_mfma_f32_16x16x32_bf16 v[80:83], a[44:47], a[144:147], v[80:83]\n\t"
    "buffer_load_dwordx4 a[92:95], v143, s[36:39], s44 offen offset:1024 sc1\n\t"
    "v_mfma_f32_16x16x32_bf16 v[84:87], a[40:43], a[132:135], 0\n\t"
    "ds_read_b64_tr_b16 a[26:27], v131 offset:20480\n\t"
    "v_mfma_f32_16x16x32_bf16 v[84:87], a[44:47], a[148:151], v[84:87]\n\t"
    "v_mfma_f32_16x16x32_bf16 v[88:91], a[40:43], a[136:139], 0\n\t"
    "ds_read_b64_tr_b16 a[28:29], v131 offset:24576\n\t"
    "v_mfma_f32_16x16x32_bf16 v[88:91], a[44:47], a[152:155], v[88:91]\n\t"
    "v_mfma_f32_16x16x32_bf16 v[92:95], a[40:43], a[140:143], 0\n\t"
    "ds_read_b64_tr_b16 a[30:31], v131 offset:28672\n\t"
    "v_mfma_f32_16x16x32_bf16 v[92:95], a[44:47], a[156:159], v[92:95]\n\t"
    "buffer_store_dwordx4 v[232:235], v240, s[84:87], 0 offen offset:2048 sc1\n\t"
    "s_waitcnt lgkmcnt(12)\n\t"
    "v_mfma_f32_16x16x32_bf16 v[96:99], a[48:51], a[128:131], 0\n\t"
    "ds_read_b64_tr_b16 a[32:33], v132 offset:16384\n\t"
    "v_mfma_f32_16x16x32_bf16 v[96:99], a[52:55], a[144:147], v[96:99]\n\t"
    "buffer_load_dwordx4 a[96:99], v140, s[36:39], s44 offen offset:2048 sc1\n\t"
    "v_mfma_f32_16x16x32_bf16 v[100:103], a[48:51], a[132:135], 0\n\t"
    "ds_read_b64_tr_b16 a[34:35], v132 offset:20480\n\t"
    "v_mfma_f32_16x16x32_bf16 v[100:103], a[52:55], a[148:151], v[100:103]\n\t"
    "v_mfma_f32_16x16x32_bf16 v[104:107], a[48:51], a[136:139], 0\n\t"
    "ds_read_b64_tr_b16 a[36:37], v132 offset:24576\n\t"
    "v_mfma_f32_16x16x32_bf16 v[104:107], a[52:55], a[152:155], v[104:107]\n\t"
    "buffer_load_dwordx4 v138, s[40:43], s45 offen offset:2048 lds\n\t"
    "v_mfma_f32_16x16x32_bf16 v[108:111], a[48:51], a[140:143], 0\n\t"
    "ds_read_b64_tr_b16 a[38:39], v132 offset:28672\n\t"
    "v_mfma_f32_16x16x32_bf16 v[108:111], a[52:55], a[156:159], v[108:111]\n\t"
    "buffer_store_dwordx4 v[236:239], v240, s[84:87], 0 offen offset:3072 sc1\n\t"
    "s_waitcnt lgkmcnt(12)\n\t"
    "v_mfma_f32_16x16x32_bf16 v[112:115], a[56:59], a[128:131], 0\n\t"
    "ds_read_b64_tr_b16 a[40:41], v133 offset:16384\n\t"
    "v_mfma_f32_16x16x32_bf16 v[112:115], a[60:63], a[144:147], v[112:115]\n\t"
    "buffer_load_dwordx4 a[100:103], v141, s[36:39], s44 offen offset:2048 sc1\n\t"
    "v_mfma_f32_16x16x32_bf16 v[116:119], a[56:59], a[132:135], 0\n\t"
    "ds_read_b64_tr_b16 a[42:43], v133 offset:20480\n\t"
    "v_mfma_f32_16x16x32_bf16 v[116:119], a[60:63], a[148:151], v[116:119]\n\t"
    "v_mfma_f32_16x16x32_bf16 v[120:123], a[56:59], a[136:139], 0\n\t"
    "ds_read_b64_tr_b16 a[44:45], v133 offset:24576\n\t"
    "v_mfma_f32_16x16x32_bf16 v[120:123], a[60:63], a[152:155], v[120:123]\n\t"
    "v_mfma_f32_16x16x32_bf16 v[124:127], a[56:59], a[140:143], 0\n\t"
    "ds_read_b64_tr_b16 a[46:47], v133 offset:28672\n\t"
    "v_mfma_f32_16x16x32_bf16 v[124:127], a[60:63], a[156:159], v[124:127]\n\t"
    "s_waitcnt vmcnt(26) lgkmcnt(12)\n\t"
    "v_mfma_f32_16x16x32_bf16 v[0:3], a[0:3], a[160:163], v[0:3]\n\t"
    "ds_read_b64_tr_b16 a[48:49], v134 offset:16384\n\t"
    "v_mfma_f32_16x16x32_bf16 v[0:3], a[4:7], a[176:179], v[0:3]\n\t"
    "buffer_load_dwordx4 a[104:107], v142, s[36:39], s44 offen offset:2048 sc1\n\t"
    "v_mfma_f32_16x16x32_bf16 v[4:7], a[0:3], a[164:167], v[4:7]\n\t"
    "ds_read_b64_tr_b16 a[50:51], v134 offset:20480\n\t"
    "v_mfma_f32_16x16x32_bf16 v[4:7], a[4:7], a[180:183], v[4:7]\n\t"
    "v_mfma_f32_16x16x32_bf16 v[8:11], a[0:3], a[168:171], v[8:11]\n\t"
    "ds_read_b64_tr_b16 a[52:53], v134 offset:24576\n\t"
    "v_mfma_f32_16x16x32_bf16 v[8:11], a[4:7], a[184:187], v[8:11]\n\t"
    "buffer_load_dwordx4 v139, s[40:43], s45 offen offset:3072 lds\n\t"
    "s_add_u32 s45, s45, 131072\n\t"
    "v_mfma_f32_16x16x32_bf16 v[12:15], a[0:3], a[172:175], v[12:15]\n\t"
    "ds_read_b64_tr_b16 a[54:55], v134 offset:28672\n\t"
    "s_add_u32 s52, s52, 16384\n\t"
    "v_mfma_f32_16x16x32_bf16 v[12:15], a[4:7], a[188:191], v[12:15]\n\t"
    "s_waitcnt lgkmcnt(12)\n\t"
    "s_and_b32 s52, s52, 0x1ffff\n\t"
    "v_mfma_f32_16x16x32_bf16 v[16:19], a[8:11], a[160:163], v[16:19]\n\t"
    "ds_read_b64_tr_b16 a[56:57], v135 offset:16384\n\t"
    "v_mfma_f32_16x16x32_bf16 v[16:19], a[12:15], a[176:179], v[16:19]\n\t"
    "buffer_load_dwordx4 a[108:111], v143, s[36:39], s44 offen offset:2048 sc1\n\t"
    "v_mfma_f32_16x16x32_bf16 v[20:23], a[8:11], a[164:167], v[20:23]\n\t"
    "ds_read_b64_tr_b16 a[58:59], v135 offset:20480\n\t"
    "v_mfma_f32_16x16x32_bf16 v[20:23], a[12:15], a[180:183], v[20:23]\n\t"
    "v_mfma_f32_16x16x32_bf16 v[24:27], a[8:11], a[168:171], v[24:27]\n\t"
    "ds_read_b64_tr_b16 a[60:61], v135 offset:24576\n\t"
    "v_mfma_f32_16x16x32_bf16 v[24:27], a[12:15], a[184:187], v[24:27]\n\t"
    "v_mfma_f32_16x16x32_bf16 v[28:31], a[8:11], a[172:175], v[28:31]\n\t"
    "ds_read_b64_tr_b16 a[62:63], v135 offset:28672\n\t"
    "v_mfma_f32_16x16x32_bf16 v[28:31], a[12:15], a[188:191], v[28:31]\n\t"
    "s_waitcnt lgkmcnt(12) vmcnt(21)\n\t"
    "s_barrier\n\t"
    "s_mov_b32 s53, 1\n\t"
    "s_mov_b32 s47, -2\n\t"
    "s_branch L_p3_loop%=\n\t"
    "L_p3_tail%=:\n\t"
    "v_mfma_f32_16x16x32_bf16 v[32:35], a[16:19], a[160:163], v[32:35]\n\t"
    "ds_read_b64_tr_b16 a[0:1], v128 offset:32768\n\t"
    "v_mfma_f32_16x16x32_bf16 v[32:35], a[20:23], a[176:179], v[32:35]\n\t"
    "buffer_load_dwordx4 a[112:115], v140, s[36:39], s44 offen offset:3072 sc1\n\t"
    "v_mfma_f32_16x16x32_bf16 v[36:39], a[16:19], a[164:167], v[36:39]\n\t"
    "ds_read_b64_tr_b16 a[2:3], v128 offset:36864\n\t"
    "v_mfma_f32_16x16x32_bf16 v[36:39], a[20:23], a[180:183], v[36:39]\n\t"
    "buffer_load_dwordx4 v[144:147], v240, s[56:59], 0 offen offset:0 sc1\n\t"
    "v_mfma_f32_16x16x32_bf16 v[40:43], a[16:19], a[168:171], v[40:43]\n\t"
    "ds_read_b64_tr_b16 a[4:5], v128 offset:40960\n\t"
    "v_mfma_f32_16x16x32_bf16 v[40:43], a[20:23], a[184:187], v[40:43]\n\t"
    "buffer_load_dwordx4 a[116:119], v141, s[36:39], s44 offen offset:3072 sc1\n\t"
    "v_mfma_f32_16x16x32_bf16 v[44:47], a[16:19], a[172:175], v[44:47]\n\t"
    "ds_read_b64_tr_b16 a[6:7], v128 offset:45056\n\t"
    "v_mfma_f32_16x16x32_bf16 v[44:47], a[20:23], a[188:191], v[44:47]\n\t"
    "s_waitcnt lgkmcnt(12)\n\t"
    "v_mfma_f32_16x16x32_bf16 v[48:51], a[24:27], a[160:163], v[48:51]\n\t"
    "ds_read_b64_tr_b16 a[8:9], v129 offset:32768\n\t"
    "v_mfma_f32_16x16x32_bf16 v[48:51], a[28:31], a[176:179], v[48:51]\n\t"
    "buffer_load_dwordx4 a[120:123], v142, s[36:39], s44 offen offset:3072 sc1\n\t"
    "v_mfma_f32_16x16x32_bf16 v[52:55], a[24:27], a[164:167], v[52:55]\n\t"
    "ds_read_b64_tr_b16 a[10:11], v129 offset:36864\n\t"
    "v_mfma_f32_16x16x32_bf16 v[52:55], a[28:31], a[180:183], v[52:55]\n\t"
    "buffer_load_dwordx4 v[148:151], v240, s[56:59], 0 offen offset:1024 sc1\n\t"
    "v_mfma_f32_16x16x32_bf16 v[56:59], a[24:27], a[168:171], v[56:59]\n\t"
    "ds_read_b64_tr_b16 a[12:13], v129 offset:40960\n\t"
    "v_mfma_f32_16x16x32_bf16 v[56:59], a[28:31], a[184:187], v[56:59]\n\t"
    "buffer_load_dwordx4 a[124:127], v143, s[36:39], s44 offen offset:3072 sc1\n\t"
    "s_add_u32 s44, s44, 4096\n\t"
    "v_mfma_f32_16x16x32_bf16 v[60:63], a[24:27], a[172:175], v[60:63]\n\t"
    "ds_read_b64_tr_b16 a[14:15], v129 offset:45056\n\t"
    "v_mfma_f32_16x16x32_bf16 v[60:63], a[28:31], a[188:191], v[60:63]\n\t"
    "s_waitcnt lgkmcnt(12)\n\t"
    "v_mfma_f32_16x16x32_bf16 v[64:67], a[32:35], a[160:163], v[64:67]\n\t"
    "ds_read_b64_tr_b16 a[16:17], v130 offset:32768\n\t"
    "s_add_u32 m0, s52, s46\n\t"
    "v_mfma_f32_16x16x32_bf16 v[64:67], a[36:39], a[176:179], v[64:67]\n\t"
    "buffer_load_dwordx4 v136, s[40:43], s45 offen offset:0 lds\n\t"
    "v_mfma_f32_16x16x32_bf16 v[68:71], a[32:35], a[164:167], v[68:71]\n\t"
    "ds_read_b64_tr_b16 a[18:19], v130 offset:36864\n\t"
    "v_mfma_f32_16x16x32_bf16 v[68:71], a[36:39], a[180:183], v[68:71]\n\t"
    "buffer_load_dwordx4 v[152:155], v240, s[56:59], 0 offen offset:2048 sc1\n\t"
    "v_mfma_f32_16x16x32_bf16 v[72:75], a[32:35], a[168:171], v[72:75]\n\t"
    "ds_read_b64_tr_b16 a[20:21], v130 offset:40960\n\t"
    "v_mfma_f32_16x16x32_bf16 v[72:75], a[36:39], a[184:187], v[72:75]\n\t"
    "buffer_load_dwordx4 v137, s[40:43], s45 offen offset:1024 lds\n\t"
    "v_mfma_f32_16x16x32_bf16 v[76:79], a[32:35], a[172:175], v[76:79]\n\t"
    "ds_read_b64_tr_b16 a[22:23], v130 offset:45056\n\t"
    "v_mfma_f32_16x16x32_bf16 v[76:79], a[36:39], a[188:191], v[76:79]\n\t"
    "s_waitcnt lgkmcnt(12)\n\t"
    "v_mfma_f32_16x16x32_bf16 v[80:83], a[40:43], a[160:163], v[80:83]\n\t"
    "ds_read_b64_tr_b16 a[24:25], v131 offset:32768\n\t"
    "v_mfma_f32_16x16x32_bf16 v[80:83], a[44:47], a[176:179], v[80:83]\n\t"
    "buffer_load_dwordx4 v138, s[40:43], s45 offen offset:2048 lds\n\t"
    "v_mfma_f32_16x16x32_bf16 v[84:87], a[40:43], a[164:167], v[84:87]\n\t"
    "ds_read_b64_tr_b16 a[26:27], v131 offset:36864\n\t"
    "v_mfma_f32_16x16x32_bf16 v[84:87], a[44:47], a[180:183], v[84:87]\n\t"
    "buffer_load_dwordx4 v[156:159], v240, s[56:59], 0 offen offset:3072 sc1\n\t"
    "v_mfma_f32_16x16x32_bf16 v[88:91], a[40:43], a[168:171], v[88:91]\n\t"
    "ds_read_b64_tr_b16 a[28:29], v131 offset:40960\n\t"
    "v_mfma_f32_16x16x32_bf16 v[88:91], a[44:47], a[184:187], v[88:91]\n\t"
    "buffer_load_dwordx4 v139, s[40:43], s45 offen offset:3072 lds\n\t"
    "s_add_u32 s45, s45, 131072\n\t"
    "v_mfma_f32_16x16x32_bf16 v[92:95], a[40:43], a[172:175], v[92:95]\n\t"
    "ds_read_b64_tr_b16 a[30:31], v131 offset:45056\n\t"
    "s_add_u32 s52, s52, 16384\n\t"
    "v_mfma_f32_16x16x32_bf16 v[92:95], a[44:47], a[188:191], v[92:95]\n\t"
    "s_waitcnt lgkmcnt(12)\n\t"
    "s_and_b32 s52, s52, 0x1ffff\n\t"
    "v_mfma_f32_16x16x32_bf16 v[96:99], a[48:51], a[160:163], v[96:99]\n\t"
    "ds_read_b64_tr_b16 a[32:33], v132 offset:32768\n\t"
    "v_mfma_f32_16x16x32_bf16 v[96:99], a[52:55], a[176:179], v[96:99]\n\t"
    "buffer_load_dwordx4 a[128:131], v140, s[36:39], s44 offen offset:0 sc1\n\t"
    "v_mfma_f32_16x16x32_bf16 v[100:103], a[48:51], a[164:167], v[100:103]\n\t"
    "ds_read_b64_tr_b16 a[34:35], v132 offset:36864\n\t"
    "v_mfma_f32_16x16x32_bf16 v[100:103], a[52:55], a[180:183], v[100:103]\n\t"
    "buffer_load_dwordx4 v[160:163], v240, s[56:59], s64 offen offset:0 sc1\n\t"
    "v_mfma_f32_16x16x32_bf16 v[104:107], a[48:51], a[168:171], v[104:107]\n\t"
    "ds_read_b64_tr_b16 a[36:37], v132 offset:40960\n\t"
    "v_mfma_f32_16x16x32_bf16 v[104:107], a[52:55], a[184:187], v[104:107]\n\t"
    "buffer_load_dwordx4 a[132:135], v141, s[36:39], s44 offen offset:0 sc1\n\t"
    "v_mfma_f32_16x16x32_bf16 v[108:111], a[48:51], a[172:175], v[108:111]\n\t"
    "ds_read_b64_tr_b16 a[38:39], v132 offset:45056\n\t"
    "v_mfma_f32_16x16x32_bf16 v[108:111], a[52:55], a[188:191], v[108:111]\n\t"
    "s_waitcnt lgkmcnt(12)\n\t"
    "v_mfma_f32_16x16x32_bf16 v[112:115], a[56:59], a[160:163], v[112:115]\n\t"
    "ds_read_b64_tr_b16 a[40:41], v133 offset:32768\n\t"
    "v_mfma_f32_16x16x32_bf16 v[112:115], a[60:63], a[176:179], v[112:115]\n\t"
    "buffer_load_dwordx4 a[136:139], v142, s[36:39], s44 offen offset:0 sc1\n\t"
    "v_mfma_f32_16x16x32_bf16 v[116:119], a[56:59], a[164:167], v[116:119]\n\t"
    "ds_read_b64_tr_b16 a[42:43], v133 offset:36864\n\t"
    "v_mfma_f32_16x16x32_bf16 v[116:119], a[60:63], a[180:183], v[116:119]\n\t"
    "buffer_load_dwordx4 v[164:167], v240, s[56:59], s64 offen offset:1024 sc1\n\t"
    "v_mfma_f32_16x16x32_bf16 v[120:123], a[56:59], a[168:171], v[120:123]\n\t"
    "ds_read_b64_tr_b16 a[44:45], v133 offset:40960\n\t"
    "v_mfma_f32_16x16x32_bf16 v[120:123], a[60:63], a[184:187], v[120:123]\n\t"
    "buffer_load_dwordx4 a[140:143], v143, s[36:39], s44 offen offset:0 sc1\n\t"
    "v_mfma_f32_16x16x32_bf16 v[124:127], a[56:59], a[172:175], v[124:127]\n\t"
    "ds_read_b64_tr_b16 a[46:47], v133 offset:45056\n\t"
    "v_mfma_f32_16x16x32_bf16 v[124:127], a[60:63], a[188:191], v[124:127]\n\t"
    "s_waitcnt vmcnt(24) lgkmcnt(12)\n\t"
    "v_mfma_f32_16x16x32_bf16 v[0:3], a[0:3], a[64:67], v[0:3]\n\t"
    "ds_read_b64_tr_b16 a[48:49], v134 offset:32768\n\t"
    "s_add_u32 m0, s52, s46\n\t"
    "v_mfma_f32_16x16x32_bf16 v[0:3], a[4:7], a[80:83], v[0:3]\n\t"
    "buffer_load_dwordx4 v136, s[40:43], s45 offen offset:0 lds\n\t"
    "v_mfma_f32_16x16x32_bf16 v[4:7], a[0:3], a[68:71], v[4:7]\n\t"
    "ds_read_b64_tr_b16 a[50:51], v134 offset:36864\n\t"
    "v_mfma_f32_16x16x32_bf16 v[4:7], a[4:7], a[84:87], v[4:7]\n\t"
    "buffer_load_dwordx4 v[168:171], v240, s[56:59], s64 offen offset:2048 sc1\n\t"
    "v_mfma_f32_16x16x32_bf16 v[8:11], a[0:3], a[72:75], v[8:11]\n\t"
    "ds_read_b64_tr_b16 a[52:53], v134 offset:40960\n\t"
    "v_mfma_f32_16x16x32_bf16 v[8:11], a[4:7], a[88:91], v[8:11]\n\t"
    "buffer_load_dwordx4 v137, s[40:43], s45 offen offset:1024 lds\n\t"
    "v_mfma_f32_16x16x32_bf16 v[12:15], a[0:3], a[76:79], v[12:15]\n\t"
    "ds_read_b64_tr_b16 a[54:55], v134 offset:45056\n\t"
    "v_mfma_f32_16x16x32_bf16 v[12:15], a[4:7], a[92:95], v[12:15]\n\t"
    "s_waitcnt lgkmcnt(12)\n\t"
    "v_mfma_f32_16x16x32_bf16 v[16:19], a[8:11], a[64:67], v[16:19]\n\t"
    "ds_read_b64_tr_b16 a[56:57], v135 offset:32768\n\t"
    "v_mfma_f32_16x16x32_bf16 v[16:19], a[12:15], a[80:83], v[16:19]\n\t"
    "buffer_load_dwordx4 v138, s[40:43], s45 offen offset:2048 lds\n\t"
    "v_mfma_f32_16x16x32_bf16 v[20:23], a[8:11], a[68:71], v[20:23]\n\t"
    "ds_read_b64_tr_b16 a[58:59], v135 offset:36864\n\t"
    "v_mfma_f32_16x16x32_bf16 v[20:23], a[12:15], a[84:87], v[20:23]\n\t"
    "buffer_load_dwordx4 v[172:175], v240, s[56:59], s64 offen offset:3072 sc1\n\t"
    "v_mfma_f32_16x16x32_bf16 v[24:27], a[8:11], a[72:75], v[24:27]\n\t"
    "ds_read_b64_tr_b16 a[60:61], v135 offset:40960\n\t"
    "v_mfma_f32_16x16x32_bf16 v[24:27], a[12:15], a[88:91], v[24:27]\n\t"
    "buffer_load_dwordx4 v139, s[40:43], s45 offen offset:3072 lds\n\t"
    "s_add_u32 s45, s45, 131072\n\t"
    "v_mfma_f32_16x16x32_bf16 v[28:31], a[8:11], a[76:79], v[28:31]\n\t"
    "ds_read_b64_tr_b16 a[62:63], v135 offset:45056\n\t"
    "s_add_u32 s52, s52, 16384\n\t"
    "v_mfma_f32_16x16x32_bf16 v[28:31], a[12:15], a[92:95], v[28:31]\n\t"
    "s_waitcnt lgkmcnt(12) vmcnt(25)\n\t"
    "s_barrier\n\t"
    "s_and_b32 s52, s52, 0x1ffff\n\t"
    "v_mfma_f32_16x16x32_bf16 v[32:35], a[16:19], a[64:67], v[32:35]\n\t"
    "ds_read_b64_tr_b16 a[0:1], v128 offset:49152\n\t"
    "v_mfma_f32_16x16x32_bf16 v[32:35], a[20:23], a[80:83], v[32:35]\n\t"
    "buffer_load_dwordx4 a[144:147], v140, s[36:39], s44 offen offset:1024 sc1\n\t"
    "v_mfma_f32_16x16x32_bf16 v[36:39], a[16:19], a[68:71], v[36:39]\n\t"
    "ds_read_b64_tr_b16 a[2:3], v128 offset:53248\n\t"
    "v_mfma_f32_16x16x32_bf16 v[36:39], a[20:23], a[84:87], v[36:39]\n\t"
    "buffer_load_dwordx4 v[224:227], v240, s[56:59], s65 offen offset:0 sc1\n\t"
    "v_mfma_f32_16x16x32_bf16 v[40:43], a[16:19], a[72:75], v[40:43]\n\t"
    "ds_read_b64_tr_b16 a[4:5], v128 offset:57344\n\t"
    "v_mfma_f32_16x16x32_bf16 v[40:43], a[20:23], a[88:91], v[40:43]\n\t"
    "buffer_load_dwordx4 a[148:151], v141, s[36:39], s44 offen offset:1024 sc1\n\t"
    "v_mfma_f32_16x16x32_bf16 v[44:47], a[16:19], a[76:79], v[44:47]\n\t"
    "ds_read_b64_tr_b16 a[6:7], v128 offset:61440\n\t"
    "v_xor_b32 v128, 0x10000, v128\n\t"
    "v_mfma_f32_16x16x32_bf16 v[44:47], a[20:23], a[92:95], v[44:47]\n\t"
    "s_waitcnt lgkmcnt(12)\n\t"
    "v_mfma_f32_16x16x32_bf16 v[48:51], a[24:27], a[64:67], v[48:51]\n\t"
    "ds_read_b64_tr_b16 a[8:9], v129 offset:49152\n\t"
    "v_mfma_f32_16x16x32_bf16 v[48:51], a[28:31], a[80:83], v[48:51]\n\t"
    "buffer_load_dwordx4 a[152:155], v142, s[36:39], s44 offen offset:1024 sc1\n\t"
    "v_mfma_f32_16x16x32_bf16 v[52:55], a[24:27], a[68:71], v[52:55]\n\t"
    "ds_read_b64_tr_b16 a[10:11], v129 offset:53248\n\t"
    "v_mfma_f32_16x16x32_bf16 v[52:55], a[28:31], a[84:87], v[52:55]\n\t"
    "buffer_load_dwordx4 v[228:231], v240, s[56:59], s65 offen offset:1024 sc1\n\t"
    "v_mfma_f32_16x16x32_bf16 v[56:59], a[24:27], a[72:75], v[56:59]\n\t"
    "ds_read_b64_tr_b16 a[12:13], v129 offset:57344\n\t"
    "v_mfma_f32_16x16x32_bf16 v[56:59], a[28:31], a[88:91], v[56:59]\n\t"
    "buffer_load_dwordx4 a[156:159], v143, s[36:39], s44 offen offset:1024 sc1\n\t"
    "v_mfma_f32_16x16x32_bf16 v[60:63], a[24:27], a[76:79], v[60:63]\n\t"
    "ds_read_b64_tr_b16 a[14:15], v129 offset:61440\n\t"
    "v_xor_b32 v129, 0x10000, v129\n\t"
    "v_mfma_f32_16x16x32_bf16 v[60:63], a[28:31], a[92:95], v[60:63]\n\t"
    "s_waitcnt lgkmcnt(12)\n\t"
    "v_mfma_f32_16x16x32_bf16 v[64:67], a[32:35], a[64:67], v[64:67]\n\t"
    "ds_read_b64_tr_b16 a[16:17], v130 offset:49152\n\t"
    "s_add_u32 m0, s52, s46\n\t"
    "v_mfma_f32_16x16x32_bf16 v[64:67], a[36:39], a[80:83], v[64:67]\n\t"
    "buffer_load_dwordx4 v136, s[40:43], s45 offen offset:0 lds\n\t"
    "v_mfma_f32_16x16x32_bf16 v[68:71], a[32:35], a[68:71], v[68:71]\n\t"
    "ds_read_b64_tr_b16 a[18:19], v130 offset:53248\n\t"
    "v_mfma_f32_16x16x32_bf16 v[68:71], a[36:39], a[84:87], v[68:71]\n\t"
    "buffer_load_dwordx4 v[232:235], v240, s[56:59], s65 offen offset:2048 sc1\n\t"
    "v_mfma_f32_16x16x32_bf16 v[72:75], a[32:35], a[72:75], v[72:75]\n\t"
    "ds_read_b64_tr_b16 a[20:21], v130 offset:57344\n\t"
    "v_mfma_f32_16x16x32_bf16 v[72:75], a[36:39], a[88:91], v[72:75]\n\t"
    "buffer_load_dwordx4 v137, s[40:43], s45 offen offset:1024 lds\n\t"
    "v_mfma_f32_16x16x32_bf16 v[76:79], a[32:35], a[76:79], v[76:79]\n\t"
    "ds_read_b64_tr_b16 a[22:23], v130 offset:61440\n\t"
    "v_xor_b32 v130, 0x10000, v130\n\t"
    "v_mfma_f32_16x16x32_bf16 v[76:79], a[36:39], a[92:95], v[76:79]\n\t"
    "s_waitcnt lgkmcnt(12)\n\t"
    "v_mfma_f32_16x16x32_bf16 v[80:83], a[40:43], a[64:67], v[80:83]\n\t"
    "ds_read_b64_tr_b16 a[24:25], v131 offset:49152\n\t"
    "v_mfma_f32_16x16x32_bf16 v[80:83], a[44:47], a[80:83], v[80:83]\n\t"
    "buffer_load_dwordx4 v138, s[40:43], s45 offen offset:2048 lds\n\t"
    "v_mfma_f32_16x16x32_bf16 v[84:87], a[40:43], a[68:71], v[84:87]\n\t"
    "ds_read_b64_tr_b16 a[26:27], v131 offset:53248\n\t"
    "v_mfma_f32_16x16x32_bf16 v[84:87], a[44:47], a[84:87], v[84:87]\n\t"
    "buffer_load_dwordx4 v[236:239], v240, s[56:59], s65 offen offset:3072 sc1\n\t"
    "v_mfma_f32_16x16x32_bf16 v[88:91], a[40:43], a[72:75], v[88:91]\n\t"
    "ds_read_b64_tr_b16 a[28:29], v131 offset:57344\n\t"
    "v_mfma_f32_16x16x32_bf16 v[88:91], a[44:47], a[88:91], v[88:91]\n\t"
    "buffer_load_dwordx4 v139, s[40:43], s45 offen offset:3072 lds\n\t"
    "s_add_u32 s45, s45, 131072\n\t"
    "v_mfma_f32_16x16x32_bf16 v[92:95], a[40:43], a[76:79], v[92:95]\n\t"
    "ds_read_b64_tr_b16 a[30:31], v131 offset:61440\n\t"
    "v_xor_b32 v131, 0x10000, v131\n\t"
    "s_add_u32 s52, s52, 16384\n\t"
    "v_mfma_f32_16x16x32_bf16 v[92:95], a[44:47], a[92:95], v[92:95]\n\t"
    "s_waitcnt lgkmcnt(12)\n\t"
    "s_and_b32 s52, s52, 0x1ffff\n\t"
    "v_mfma_f32_16x16x32_bf16 v[96:99], a[48:51], a[64:67], v[96:99]\n\t"
    "ds_read_b64_tr_b16 a[32:33], v132 offset:49152\n\t"
    "v_mfma_f32_16x16x32_bf16 v[96:99], a[52:55], a[80:83], v[96:99]\n\t"
    "buffer_load_dwordx4 a[160:163], v140, s[36:39], s44 offen offset:2048 sc1\n\t"
    "v_mfma_f32_16x16x32_bf16 v[100:103], a[48:51], a[68:71], v[100:103]\n\t"
    "ds_read_b64_tr_b16 a[34:35], v132 offset:53248\n\t"
    "v_mfma_f32_16x16x32_bf16 v[100:103], a[52:55], a[84:87], v[100:103]\n\t"
    "v_mfma_f32_16x16x32_bf16 v[104:107], a[48:51], a[72:75], v[104:107]\n\t"
    "ds_read_b64_tr_b16 a[36:37], v132 offset:57344\n\t"
    "v_mfma_f32_16x16x32_bf16 v[104:107], a[52:55], a[88:91], v[104:107]\n\t"
    "buffer_load_dwordx4 a[164:167], v141, s[36:39], s44 offen offset:2048 sc1\n\t"
    "v_mfma_f32_16x16x32_bf16 v[108:111], a[48:51], a[76:79], v[108:111]\n\t"
    "ds_read_b64_tr_b16 a[38:39], v132 offset:61440\n\t"
    "v_xor_b32 v132, 0x10000, v132\n\t"
    "v_mfma_f32_16x16x32_bf16 v[108:111], a[52:55], a[92:95], v[108:111]\n\t"
    "s_waitcnt lgkmcnt(12)\n\t"
    "v_mfma_f32_16x16x32_bf16 v[112:115], a[56:59], a[64:67], v[112:115]\n\t"
    "ds_read_b64_tr_b16 a[40:41], v133 offset:49152\n\t"
    "v_mfma_f32_16x16x32_bf16 v[112:115], a[60:63], a[80:83], v[112:115]\n\t"
    "buffer_load_dwordx4 a[168:171], v142, s[36:39], s44 offen offset:2048 sc1\n\t"
    "v_mfma_f32_16x16x32_bf16 v[116:119], a[56:59], a[68:71], v[116:119]\n\t"
    "ds_read_b64_tr_b16 a[42:43], v133 offset:53248\n\t"
    "v_mfma_f32_16x16x32_bf16 v[116:119], a[60:63], a[84:87], v[116:119]\n\t"
    "v_mfma_f32_16x16x32_bf16 v[120:123], a[56:59], a[72:75], v[120:123]\n\t"
    "ds_read_b64_tr_b16 a[44:45], v133 offset:57344\n\t"
    "v_mfma_f32_16x16x32_bf16 v[120:123], a[60:63], a[88:91], v[120:123]\n\t"
    "buffer_load_dwordx4 a[172:175], v143, s[36:39], s44 offen offset:2048 sc1\n\t"
    "v_mfma_f32_16x16x32_bf16 v[124:127], a[56:59], a[76:79], v[124:127]\n\t"
    "ds_read_b64_tr_b16 a[46:47], v133 offset:61440\n\t"
    "v_xor_b32 v133, 0x10000, v133\n\t"
    "v_mfma_f32_16x16x32_bf16 v[124:127], a[60:63], a[92:95], v[124:127]\n\t"
    "s_waitcnt vmcnt(34) lgkmcnt(12)\n\t"
    "v_mfma_f32_16x16x32_bf16 v[0:3], a[0:3], a[96:99], v[0:3]\n\t"
    "ds_read_b64_tr_b16 a[48:49], v134 offset:49152\n\t"
    "s_add_u32 m0, s52, s46\n\t"
    "v_mfma_f32_16x16x32_bf16 v[0:3], a[4:7], a[112:115], v[0:3]\n\t"
    "buffer_load_dwordx4 v136, s[40:43], s45 offen offset:0 lds\n\t"
    "v_mfma_f32_16x16x32_bf16 v[4:7], a[0:3], a[100:103], v[4:7]\n\t"
    "ds_read_b64_tr_b16 a[50:51], v134 offset:53248\n\t"
    "v_mfma_f32_16x16x32_bf16 v[4:7], a[4:7], a[116:119], v[4:7]\n\t"
    "v_mfma_f32_16x16x32_bf16 v[8:11], a[0:3], a[104:107], v[8:11]\n\t"
    "ds_read_b64_tr_b16 a[52:53], v134 offset:57344\n\t"
    "v_mfma_f32_16x16x32_bf16 v[8:11], a[4:7], a[120:123], v[8:11]\n\t"
    "buffer_load_dwordx4 v137, s[40:43], s45 offen offset:1024 lds\n\t"
    "v_mfma_f32_16x16x32_bf16 v[12:15], a[0:3], a[108:111], v[12:15]\n\t"
    "ds_read_b64_tr_b16 a[54:55], v134 offset:61440\n\t"
    "v_xor_b32 v134, 0x10000, v134\n\t"
    "v_mfma_f32_16x16x32_bf16 v[12:15], a[4:7], a[124:127], v[12:15]\n\t"
    "s_waitcnt lgkmcnt(12)\n\t"
    "v_mfma_f32_16x16x32_bf16 v[16:19], a[8:11], a[96:99], v[16:19]\n\t"
    "ds_read_b64_tr_b16 a[56:57], v135 offset:49152\n\t"
    "v_mfma_f32_16x16x32_bf16 v[16:19], a[12:15], a[112:115], v[16:19]\n\t"
    "buffer_load_dwordx4 v138, s[40:43], s45 offen offset:2048 lds\n\t"
    "v_mfma_f32_16x16x32_bf16 v[20:23], a[8:11], a[100:103], v[20:23]\n\t"
    "ds_read_b64_tr_b16 a[58:59], v135 offset:53248\n\t"
    "v_mfma_f32_16x16x32_bf16 v[20:23], a[12:15], a[116:119], v[20:23]\n\t"
    "v_mfma_f32_16x16x32_bf16 v[24:27], a[8:11], a[104:107], v[24:27]\n\t"
    "ds_read_b64_tr_b16 a[60:61], v135 offset:57344\n\t"
    "v_mfma_f32_16x16x32_bf16 v[24:27], a[12:15], a[120:123], v[24:27]\n\t"
    "buffer_load_dwordx4 v139, s[40:43], s45 offen offset:3072 lds\n\t"
    "s_add_u32 s45, s45, 131072\n\t"
    "v_mfma_f32_16x16x32_bf16 v[28:31], a[8:11], a[108:111], v[28:31]\n\t"
    "ds_read_b64_tr_b16 a[62:63], v135 offset:61440\n\t"
    "v_xor_b32 v135, 0x10000, v135\n\t"
    "s_add_u32 s52, s52, 16384\n\t"
    "v_mfma_f32_16x16x32_bf16 v[28:31], a[12:15], a[124:127], v[28:31]\n\t"
    "s_waitcnt lgkmcnt(12) vmcnt(32)\n\t"
    "s_barrier\n\t"
    "s_and_b32 s52, s52, 0x1ffff\n\t"
    "v_mfma_f32_16x16x32_bf16 v[32:35], a[16:19], a[96:99], v[32:35]\n\t"
    "ds_read_b64_tr_b16 a[0:1], v132 offset:0\n\t"
    "v_mfma_f32_16x16x32_bf16 v[32:35], a[20:23], a[112:115], v[32:35]\n\t"
    "buffer_load_dwordx4 a[176:179], v140, s[36:39], s44 offen offset:3072 sc1\n\t"
    "v_mfma_f32_16x16x32_bf16 v[36:39], a[16:19], a[100:103], v[36:39]\n\t"
    "ds_read_b64_tr_b16 a[2:3], v132 offset:4096\n\t"
    "v_mfma_f32_16x16x32_bf16 v[36:39], a[20:23], a[116:119], v[36:39]\n\t"
    "v_mfma_f32_16x16x32_bf16 v[40:43], a[16:19], a[104:107], v[40:43]\n\t"
    "ds_read_b64_tr_b16 a[4:5], v132 offset:8192\n\t"
    "v_mfma_f32_16x16x32_bf16 v[40:43], a[20:23], a[120:123], v[40:43]\n\t"
    "buffer_load_dwordx4 a[180:183], v141, s[36:39], s44 offen offset:3072 sc1\n\t"
    "v_mfma_f32_16x16x32_bf16 v[44:47], a[16:19], a[108:111], v[44:47]\n\t"
    "ds_read_b64_tr_b16 a[6:7], v132 offset:12288\n\t"
    "v_mfma_f32_16x16x32_bf16 v[44:47], a[20:23], a[124:127], v[44:47]\n\t"
    "s_waitcnt lgkmcnt(12)\n\t"
    "v_mfma_f32_16x16x32_bf16 v[48:51], a[24:27], a[96:99], v[48:51]\n\t"
    "ds_read_b64_tr_b16 a[8:9], v133 offset:0\n\t"
    "v_mfma_f32_16x16x32_bf16 v[48:51], a[28:31], a[112:115], v[48:51]\n\t"
    "buffer_load_dwordx4 a[184:187], v142, s[36:39], s44 offen offset:3072 sc1\n\t"
    "v_mfma_f32_16x16x32_bf16 v[52:55], a[24:27], a[100:103], v[52:55]\n\t"
    "ds_read_b64_tr_b16 a[10:11], v133 offset:4096\n\t"
    "v_mfma_f32_16x16x32_bf16 v[52:55], a[28:31], a[116:119], v[52:55]\n\t"
    "v_mfma_f32_16x16x32_bf16 v[56:59], a[24:27], a[104:107], v[56:59]\n\t"
    "ds_read_b64_tr_b16 a[12:13], v133 offset:8192\n\t"
    "v_mfma_f32_16x16x32_bf16 v[56:59], a[28:31], a[120:123], v[56:59]\n\t"
    "buffer_load_dwordx4 a[188:191], v143, s[36:39], s44 offen offset:3072 sc1\n\t"
    "s_add_u32 s44, s44, 4096\n\t"
    "v_mfma_f32_16x16x32_bf16 v[60:63], a[24:27], a[108:111], v[60:63]\n\t"
    "ds_read_b64_tr_b16 a[14:15], v133 offset:12288\n\t"
    "v_mfma_f32_16x16x32_bf16 v[60:63], a[28:31], a[124:127], v[60:63]\n\t"
    "s_waitcnt lgkmcnt(12)\n\t"
    "v_mfma_f32_16x16x32_bf16 v[64:67], a[32:35], a[96:99], v[64:67]\n\t"
    "ds_read_b64_tr_b16 a[16:17], v134 offset:0\n\t"
    "v_mfma_f32_16x16x32_bf16 v[64:67], a[36:39], a[112:115], v[64:67]\n\t"
    "buffer_load_dwordx4 a[64:67], v140, s[36:39], s44 offen offset:0 sc1\n\t"
    "v_mfma_f32_16x16x32_bf16 v[68:71], a[32:35], a[100:103], v[68:71]\n\t"
    "ds_read_b64_tr_b16 a[18:19], v134 offset:4096\n\t"
    "v_mfma_f32_16x16x32_bf16 v[68:71], a[36:39], a[116:119], v[68:71]\n\t"
    "v_mfma_f32_16x16x32_bf16 v[72:75], a[32:35], a[104:107], v[72:75]\n\t"
    "ds_read_b64_tr_b16 a[20:21], v134 offset:8192\n\t"
    "v_mfma_f32_16x16x32_bf16 v[72:75], a[36:39], a[120:123], v[72:75]\n\t"
    "buffer_load_dwordx4 a[68:71], v141, s[36:39], s44 offen offset:0 sc1\n\t"
    "v_mfma_f32_16x16x32_bf16 v[76:79], a[32:35], a[108:111], v[76:79]\n\t"
    "ds_read_b64_tr_b16 a[22:23], v134 offset:12288\n\t"
    "s_waitcnt vmcnt(15)\n\t"
    "s_mov_b32 s54, 0\n\t"
    "L_p3_dc%=:\n\t"
    "v_max3_u32 v246, v144, v145, v146\n\t"
    "v_max_u32 v247, v147, v147\n\t"
    "v_max3_u32 v246, v246, v148, v149\n\t"
    "v_max3_u32 v247, v247, v150, v151\n\t"
    "v_max3_u32 v246, v246, v152, v153\n\t"
    "v_max3_u32 v247, v247, v154, v155\n\t"
    "v_max3_u32 v246, v246, v156, v157\n\t"
    "v_max3_u32 v247, v247, v158, v159\n\t"
    "v_max3_u32 v246, v246, v160, v161\n\t"
    "v_max3_u32 v247, v247, v162, v163\n\t"
    "v_max3_u32 v246, v246, v164, v165\n\t"
    "v_max3_u32 v247, v247, v166, v167\n\t"
    "v_max3_u32 v246, v246, v168, v169\n\t"
    "v_max3_u32 v247, v247, v170, v171\n\t"
    "v_max3_u32 v246, v246, v172, v173\n\t"
    "v_max3_u32 v247, v247, v174, v175\n\t"
    "v_max3_u32 v246, v246, v224, v225\n\t"
    "v_max3_u32 v247, v247, v226, v227\n\t"
    "v_max3_u32 v246, v246, v228, v229\n\t"
    "v_max3_u32 v247, v247, v230, v231\n\t"
    "v_max3_u32 v246, v246, v232, v233\n\t"
    "v_max3_u32 v247, v247, v234, v235\n\t"
    "v_max3_u32 v246, v246, v236, v237\n\t"
    "v_max3_u32 v247, v247, v238, v239\n\t"
    "v_max_u32 v246, v246, v247\n\t"
    "v_cmp_eq_u32 vcc, -1, v246\n\t"
    "s_cbranch_vccnz L_p3_dp%=\n\t"
    "L_p3_dk%=:\n\t"
    "v_mfma_f32_16x16x32_bf16 v[76:79], a[36:39], a[124:127], v[76:79]\n\t"
    "s_waitcnt lgkmcnt(12)\n\t"
    "v_lshlrev_b32 v248, 16, v144\n\t"
    "v_lshlrev_b32 v249, 16, v160\n\t"
    "v_mfma_f32_16x16x32_bf16 v[80:83], a[40:43], a[96:99], v[80:83]\n\t"
    "ds_read_b64_tr_b16 a[24:25], v135 offset:0\n\t"
    "v_lshlrev_b32 v250, 16, v224\n\t"
    "v_mfma_f32_16x16x32_bf16 v[80:83], a[44:47], a[112:115], v[80:83]\n\t"
    "buffer_load_dwordx4 a[72:75], v142, s[36:39], s44 offen offset:0 sc1\n\t"
    "v_add_f32 v192, v192, v248\n\t"
    "v_mfma_f32_16x16x32_bf16 v[84:87], a[40:43], a[100:103], v[84:87]\n\t"
    "ds_read_b64_tr_b16 a[26:27], v135 offset:4096\n\t"
    "v_add_f32 v249, v249, v250\n\t"
    "v_mfma_f32_16x16x32_bf16 v[84:87], a[44:47], a[116:119], v[84:87]\n\t"
    "v_add_f32 v192, v192, v249\n\t"
    "v_and_b32 v251, 0xffff0000, v144\n\t"
    "v_mfma_f32_16x16x32_bf16 v[88:91], a[40:43], a[104:107], v[88:91]\n\t"
    "ds_read_b64_tr_b16 a[28:29], v135 offset:8192\n\t"
    "v_and_b32 v252, 0xffff0000, v160\n\t"
    "v_mfma_f32_16x16x32_bf16 v[88:91], a[44:47], a[120:123], v[88:91]\n\t"
    "buffer_load_dwordx4 a[76:79], v143, s[36:39], s44 offen offset:0 sc1\n\t"
    "v_and_b32 v253, 0xffff0000, v224\n\t"
    "v_mfma_f32_16x16x32_bf16 v[92:95], a[40:43], a[108:111], v[92:95]\n\t"
    "ds_read_b64_tr_b16 a[30:31], v135 offset:12288\n\t"
    "v_add_f32 v193, v193, v251\n\t"
    "v_mfma_f32_16x16x32_bf16 v[92:95], a[44:47], a[124:127], v[92:95]\n\t"
    "s_waitcnt lgkmcnt(12) vmcnt(28)\n\t"
    "s_barrier\n\t"
    "v_mfma_f32_16x16x32_bf16 v[96:99], a[48:51], a[96:99], v[96:99]\n\t"
    "ds_read_b64_tr_b16 a[32:33], v132 offset:16384\n\t"
    "v_add_f32 v252, v252, v253\n\t"
    "v_mfma_f32_16x16x32_bf16 v[96:99], a[52:55], a[112:115], v[96:99]\n\t"
    "buffer_load_dwordx4 a[80:83], v140, s[36:39], s44 offen offset:1024 sc1\n\t"
    "v_add_f32 v193, v193, v252\n\t"
    "v_mfma_f32_16x16x32_bf16 v[100:103], a[48:51], a[100:103], v[100:103]\n\t"
    "ds_read_b64_tr_b16 a[34:35], v132 offset:20480\n\t"
    "v_lshlrev_b32 v248, 16, v145\n\t"
    "v_mfma_f32_16x16x32_bf16 v[100:103], a[52:55], a[116:119], v[100:103]\n\t"
    "v_lshlrev_b32 v249, 16, v161\n\t"
    "v_lshlrev_b32 v250, 16, v225\n\t"
    "v_mfma_f32_16x16x32_bf16 v[104:107], a[48:51], a[104:107], v[104:107]\n\t"
    "ds_read_b64_tr_b16 a[36:37], v132 offset:24576\n\t"
    "v_add_f32 v194, v194, v248\n\t"
    "v_mfma_f32_16x16x32_bf16 v[104:107], a[52:55], a[120:123], v[104:107]\n\t"
    "buffer_load_dwordx4 a[84:87], v141, s[36:39], s44 offen offset:1024 sc1\n\t"
    "v_add_f32 v249, v249, v250\n\t"
    "v_mfma_f32_16x16x32_bf16 v[108:111], a[48:51], a[108:111], v[108:111]\n\t"
    "ds_read_b64_tr_b16 a[38:39], v132 offset:28672\n\t"
    "v_add_f32 v194, v194, v249\n\t"
    "v_mfma_f32_16x16x32_bf16 v[108:111], a[52:55], a[124:127], v[108:111]\n\t"
    "s_waitcnt lgkmcnt(12)\n\t"
    "v_and_b32 v251, 0xffff0000, v145\n\t"
    "v_and_b32 v252, 0xffff0000, v161\n\t"
    "v_mfma_f32_16x16x32_bf16 v[112:115], a[56:59], a[96:99], v[112:115]\n\t"
    "ds_read_b64_tr_b16 a[40:41], v133 offset:16384\n\t"
    "v_and_b32 v253, 0xffff0000, v225\n\t"
    "v_mfma_f32_16x16x32_bf16 v[112:115], a[60:63], a[112:115], v[112:115]\n\t"
    "buffer_load_dwordx4 a[88:91], v142, s[36:39], s44 offen offset:1024 sc1\n\t"
    "v_add_f32 v195, v195, v251\n\t"
    "v_mfma_f32_16x16x32_bf16 v[116:119], a[56:59], a[100:103], v[116:119]\n\t"
    "ds_read_b64_tr_b16 a[42:43], v133 offset:20480\n\t"
    "v_add_f32 v252, v252, v253\n\t"
    "v_mfma_f32_16x16x32_bf16 v[116:119], a[60:63], a[116:119], v[116:119]\n\t"
    "v_add_f32 v195, v195, v252\n\t"
    "v_lshlrev_b32 v248, 16, v146\n\t"
    "v_mfma_f32_16x16x32_bf16 v[120:123], a[56:59], a[104:107], v[120:123]\n\t"
    "ds_read_b64_tr_b16 a[44:45], v133 offset:24576\n\t"
    "v_lshlrev_b32 v249, 16, v162\n\t"
    "v_mfma_f32_16x16x32_bf16 v[120:123], a[60:63], a[120:123], v[120:123]\n\t"
    "buffer_load_dwordx4 a[92:95], v143, s[36:39], s44 offen offset:1024 sc1\n\t"
    "v_lshlrev_b32 v250, 16, v226\n\t"
    "v_mfma_f32_16x16x32_bf16 v[124:127], a[56:59], a[108:111], v[124:127]\n\t"
    "ds_read_b64_tr_b16 a[46:47], v133 offset:28672\n\t"
    "v_add_f32 v208, v208, v248\n\t"
    "v_mfma_f32_16x16x32_bf16 v[124:127], a[60:63], a[124:127], v[124:127]\n\t"
    "s_waitcnt vmcnt(26) lgkmcnt(12)\n\t"
    "v_add_f32 v249, v249, v250\n\t"
    "v_add_f32 v208, v208, v249\n\t"
    "v_mfma_f32_16x16x32_bf16 v[64:67], a[0:3], a[128:131], v[64:67]\n\t"
    "ds_read_b64_tr_b16 a[48:49], v134 offset:16384\n\t"
    "v_and_b32 v251, 0xffff0000, v146\n\t"
    "v_mfma_f32_16x16x32_bf16 v[64:67], a[4:7], a[144:147], v[64:67]\n\t"
    "buffer_load_dwordx4 a[96:99], v140, s[36:39], s44 offen offset:2048 sc1\n\t"
    "v_and_b32 v252, 0xffff0000, v162\n\t"
    "v_mfma_f32_16x16x32_bf16 v[68:71], a[0:3], a[132:135], v[68:71]\n\t"
    "ds_read_b64_tr_b16 a[50:51], v134 offset:20480\n\t"
    "v_and_b32 v253, 0xffff0000, v226\n\t"
    "v_mfma_f32_16x16x32_bf16 v[68:71], a[4:7], a[148:151], v[68:71]\n\t"
    "v_add_f32 v209, v209, v251\n\t"
    "v_add_f32 v252, v252, v253\n\t"
    "v_mfma_f32_16x16x32_bf16 v[72:75], a[0:3], a[136:139], v[72:75]\n\t"
    "ds_read_b64_tr_b16 a[52:53], v134 offset:24576\n\t"
    "v_add_f32 v209, v209, v252\n\t"
    "v_mfma_f32_16x16x32_bf16 v[72:75], a[4:7], a[152:155], v[72:75]\n\t"
    "buffer_load_dwordx4 a[100:103], v141, s[36:39], s44 offen offset:2048 sc1\n\t"
    "v_lshlrev_b32 v248, 16, v147\n\t"
    "v_mfma_f32_16x16x32_bf16 v[76:79], a[0:3], a[140:143], v[76:79]\n\t"
    "ds_read_b64_tr_b16 a[54:55], v134 offset:28672\n\t"
    "v_lshlrev_b32 v249, 16, v163\n\t"
    "v_mfma_f32_16x16x32_bf16 v[76:79], a[4:7], a[156:159], v[76:79]\n\t"
    "s_waitcnt lgkmcnt(12)\n\t"
    "v_lshlrev_b32 v250, 16, v227\n\t"
    "v_add_f32 v210, v210, v248\n\t"
    "v_mfma_f32_16x16x32_bf16 v[80:83], a[8:11], a[128:131], v[80:83]\n\t"
    "ds_read_b64_tr_b16 a[56:57], v135 offset:16384\n\t"
    "v_add_f32 v249, v249, v250\n\t"
    "v_mfma_f32_16x16x32_bf16 v[80:83], a[12:15], a[144:147], v[80:83]\n\t"
    "buffer_load_dwordx4 a[104:107], v142, s[36:39], s44 offen offset:2048 sc1\n\t"
    "v_add_f32 v210, v210, v249\n\t"
    "v_mfma_f32_16x16x32_bf16 v[84:87], a[8:11], a[132:135], v[84:87]\n\t"
    "ds_read_b64_tr_b16 a[58:59], v135 offset:20480\n\t"
    "v_and_b32 v251, 0xffff0000, v147\n\t"
    "v_mfma_f32_16x16x32_bf16 v[84:87], a[12:15], a[148:151], v[84:87]\n\t"
    "v_and_b32 v252, 0xffff0000, v163\n\t"
    "v_and_b32 v253, 0xffff0000, v227\n\t"
    "v_mfma_f32_16x16x32_bf16 v[88:91], a[8:11], a[136:139], v[88:91]\n\t"
    "ds_read_b64_tr_b16 a[60:61], v135 offset:24576\n\t"
    "v_add_f32 v211, v211, v251\n\t"
    "v_mfma_f32_16x16x32_bf16 v[88:91], a[12:15], a[152:155], v[88:91]\n\t"
    "buffer_load_dwordx4 a[108:111], v143, s[36:39], s44 offen offset:2048 sc1\n\t"
    "v_add_f32 v252, v252, v253\n\t"
    "v_mfma_f32_16x16x32_bf16 v[92:95], a[8:11], a[140:143], v[92:95]\n\t"
    "ds_read_b64_tr_b16 a[62:63], v135 offset:28672\n\t"
    "v_add_f32 v211, v211, v252\n\t"
    "v_mfma_f32_16x16x32_bf16 v[92:95], a[12:15], a[156:159], v[92:95]\n\t"
    "s_waitcnt lgkmcnt(12) vmcnt(24)\n\t"
    "s_barrier\n\t"
    "v_mfma_f32_16x16x32_bf16 v[96:99], a[16:19], a[128:131], v[96:99]\n\t"
    "ds_read_b64_tr_b16 a[0:1], v132 offset:32768\n\t"
    "v_lshlrev_b32 v248, 16, v148\n\t"
    "v_mfma_f32_16x16x32_bf16 v[96:99], a[20:23], a[144:147], v[96:99]\n\t"
    "buffer_load_dwordx4 a[112:115], v140, s[36:39], s44 offen offset:3072 sc1\n\t"
    "v_lshlrev_b32 v249, 16, v164\n\t"
    "v_mfma_f32_16x16x32_bf16 v[100:103], a[16:19], a[132:135], v[100:103]\n\t"
    "ds_read_b64_tr_b16 a[2:3], v132 offset:36864\n\t"
    "v_lshlrev_b32 v250, 16, v228\n\t"
    "v_mfma_f32_16x16x32_bf16 v[100:103], a[20:23], a[148:151], v[100:103]\n\t"
    "v_add_f32 v196, v196, v248\n\t"
    "v_add_f32 v249, v249, v250\n\t"
    "v_mfma_f32_16x16x32_bf16 v[104:107], a[16:19], a[136:139], v[104:107]\n\t"
    "ds_read_b64_tr_b16 a[4:5], v132 offset:40960\n\t"
    "v_add_f32 v196, v196, v249\n\t"
    "v_mfma_f32_16x16x32_bf16 v[104:107], a[20:23], a[152:155], v[104:107]\n\t"
    "buffer_load_dwordx4 a[116:119], v141, s[36:39], s44 offen offset:3072 sc1\n\t"
    "v_and_b32 v251, 0xffff0000, v148\n\t"
    "v_mfma_f32_16x16x32_bf16 v[108:111], a[16:19], a[140:143], v[108:111]\n\t"
    "ds_read_b64_tr_b16 a[6:7], v132 offset:45056\n\t"
    "v_and_b32 v252, 0xffff0000, v164\n\t"
    "v_mfma_f32_16x16x32_bf16 v[108:111], a[20:23], a[156:159], v[108:111]\n\t"
    "s_waitcnt lgkmcnt(12)\n\t"
    "v_and_b32 v253, 0xffff0000, v228\n\t"
    "v_add_f32 v197, v197, v251\n\t"
    "v_mfma_f32_16x16x32_bf16 v[112:115], a[24:27], a[128:131], v[112:115]\n\t"
    "ds_read_b64_tr_b16 a[8:9], v133 offset:32768\n\t"
    "v_add_f32 v252, v252, v253\n\t"
    "v_mfma_f32_16x16x32_bf16 v[112:115], a[28:31], a[144:147], v[112:115]\n\t"
    "buffer_load_dwordx4 a[120:123], v142, s[36:39], s44 offen offset:3072 sc1\n\t"
    "v_add_f32 v197, v197, v252\n\t"
    "v_mfma_f32_16x16x32_bf16 v[116:119], a[24:27], a[132:135], v[116:119]\n\t"
    "ds_read_b64_tr_b16 a[10:11], v133 offset:36864\n\t"
    "v_lshlrev_b32 v248, 16, v149\n\t"
    "v_mfma_f32_16x16x32_bf16 v[116:119], a[28:31], a[148:151], v[116:119]\n\t"
    "v_lshlrev_b32 v249, 16, v165\n\t"
    "v_lshlrev_b32 v250, 16, v229\n\t"
    "v_mfma_f32_16x16x32_bf16 v[120:123], a[24:27], a[136:139], v[120:123]\n\t"
    "ds_read_b64_tr_b16 a[12:13], v133 offset:40960\n\t"
    "v_add_f32 v198, v198, v248\n\t"
    "v_mfma_f32_16x16x32_bf16 v[120:123], a[28:31], a[152:155], v[120:123]\n\t"
    "buffer_load_dwordx4 a[124:127], v143, s[36:39], s44 offen offset:3072 sc1\n\t"
    "s_add_u32 s44, s44, 4096\n\t"
    "v_add_f32 v249, v249, v250\n\t"
    "v_mfma_f32_16x16x32_bf16 v[124:127], a[24:27], a[140:143], v[124:127]\n\t"
    "ds_read_b64_tr_b16 a[14:15], v133 offset:45056\n\t"
    "v_add_f32 v198, v198, v249\n\t"
    "v_mfma_f32_16x16x32_bf16 v[124:127], a[28:31], a[156:159], v[124:127]\n\t"
    "s_waitcnt vmcnt(16) lgkmcnt(12)\n\t"
    "v_and_b32 v251, 0xffff0000, v149\n\t"
    "v_and_b32 v252, 0xffff0000, v165\n\t"
    "v_mfma_f32_16x16x32_bf16 v[64:67], a[32:35], a[160:163], v[64:67]\n\t"
    "ds_read_b64_tr_b16 a[16:17], v134 offset:32768\n\t"
    "v_and_b32 v253, 0xffff0000, v229\n\t"
    "v_mfma_f32_16x16x32_bf16 v[64:67], a[36:39], a[176:179], v[64:67]\n\t"
    "buffer_load_dwordx4 v[176:179], v241, s[60:63], 0 offen\n\t"
    "v_add_f32 v199, v199, v251\n\t"
    "v_mfma_f32_16x16x32_bf16 v[68:71], a[32:35], a[164:167], v[68:71]\n\t"
    "ds_read_b64_tr_b16 a[18:19], v134 offset:36864\n\t"
    "v_add_f32 v252, v252, v253\n\t"
    "v_mfma_f32_16x16x32_bf16 v[68:71], a[36:39], a[180:183], v[68:71]\n\t"
    "v_add_f32 v199, v199, v252\n\t"
    "v_lshlrev_b32 v248, 16, v150\n\t"
    "v_mfma_f32_16x16x32_bf16 v[72:75], a[32:35], a[168:171], v[72:75]\n\t"
    "ds_read_b64_tr_b16 a[20:21], v134 offset:40960\n\t"
    "v_lshlrev_b32 v249, 16, v166\n\t"
    "v_mfma_f32_16x16x32_bf16 v[72:75], a[36:39], a[184:187], v[72:75]\n\t"
    "buffer_load_dwordx4 v[180:183], v242, s[60:63], 0 offen\n\t"
    "v_lshlrev_b32 v250, 16, v230\n\t"
    "v_mfma_f32_16x16x32_bf16 v[76:79], a[32:35], a[172:175], v[76:79]\n\t"
    "ds_read_b64_tr_b16 a[22:23], v134 offset:45056\n\t"
    "v_add_f32 v212, v212, v248\n\t"
    "v_mfma_f32_16x16x32_bf16 v[76:79], a[36:39], a[188:191], v[76:79]\n\t"
    "s_waitcnt lgkmcnt(12)\n\t"
    "v_add_f32 v249, v249, v250\n\t"
    "v_add_f32 v212, v212, v249\n\t"
    "v_mfma_f32_16x16x32_bf16 v[80:83], a[40:43], a[160:163], v[80:83]\n\t"
    "ds_read_b64_tr_b16 a[24:25], v135 offset:32768\n\t"
    "v_and_b32 v251, 0xffff0000, v150\n\t"
    "v_mfma_f32_16x16x32_bf16 v[80:83], a[44:47], a[176:179], v[80:83]\n\t"
    "buffer_load_dwordx4 v[184:187], v243, s[60:63], 0 offen\n\t"
    "v_and_b32 v252, 0xffff0000, v166\n\t"
    "v_mfma_f32_16x16x32_bf16 v[84:87], a[40:43], a[164:167], v[84:87]\n\t"
    "ds_read_b64_tr_b16 a[26:27], v135 offset:36864\n\t"
    "v_and_b32 v253, 0xffff0000, v230\n\t"
    "v_mfma_f32_16x16x32_bf16 v[84:87], a[44:47], a[180:183], v[84:87]\n\t"
    "v_add_f32 v213, v213, v251\n\t"
    "v_add_f32 v252, v252, v253\n\t"
    "v_mfma_f32_16x16x32_bf16 v[88:91], a[40:43], a[168:171], v[88:91]\n\t"
    "ds_read_b64_tr_b16 a[28:29], v135 offset:40960\n\t"
    "v_add_f32 v213, v213, v252\n\t"
    "v_mfma_f32_16x16x32_bf16 v[88:91], a[44:47], a[184:187], v[88:91]\n\t"
    "buffer_load_dwordx4 v[188:191], v244, s[60:63], 0 offen\n\t"
    "v_lshlrev_b32 v248, 16, v151\n\t"
    "v_mfma_f32_16x16x32_bf16 v[92:95], a[40:43], a[172:175], v[92:95]\n\t"
    "ds_read_b64_tr_b16 a[30:31], v135 offset:45056\n\t"
    "v_lshlrev_b32 v249, 16, v167\n\t"
    "v_mfma_f32_16x16x32_bf16 v[92:95], a[44:47], a[188:191], v[92:95]\n\t"
    "s_waitcnt lgkmcnt(12) vmcnt(24)\n\t"
    "s_barrier\n\t"
    "v_mfma_f32_16x16x32_bf16 v[96:99], a[48:51], a[160:163], v[96:99]\n\t"
    "ds_read_b64_tr_b16 a[32:33], v132 offset:49152\n\t"
    "v_lshlrev_b32 v250, 16, v231\n\t"
    "v_mfma_f32_16x16x32_bf16 v[96:99], a[52:55], a[176:179], v[96:99]\n\t"
    "v_add_f32 v214, v214, v248\n\t"
    "v_add_f32 v249, v249, v250\n\t"
    "v_mfma_f32_16x16x32_bf16 v[100:103], a[48:51], a[164:167], v[100:103]\n\t"
    "ds_read_b64_tr_b16 a[34:35], v132 offset:53248\n\t"
    "v_add_f32 v214, v214, v249\n\t"
    "v_mfma_f32_16x16x32_bf16 v[100:103], a[52:55], a[180:183], v[100:103]\n\t"
    "v_and_b32 v251, 0xffff0000, v151\n\t"
    "v_and_b32 v252, 0xffff0000, v167\n\t"
    "v_mfma_f32_16x16x32_bf16 v[104:107], a[48:51], a[168:171], v[104:107]\n\t"
    "ds_read_b64_tr_b16 a[36:37], v132 offset:57344\n\t"
    "v_and_b32 v253, 0xffff0000, v231\n\t"
    "v_mfma_f32_16x16x32_bf16 v[104:107], a[52:55], a[184:187], v[104:107]\n\t"
    "v_add_f32 v215, v215, v251\n\t"
    "v_add_f32 v252, v252, v253\n\t"
    "v_mfma_f32_16x16x32_bf16 v[108:111], a[48:51], a[172:175], v[108:111]\n\t"
    "ds_read_b64_tr_b16 a[38:39], v132 offset:61440\n\t"
    "v_add_f32 v215, v215, v252\n\t"
    "v_mfma_f32_16x16x32_bf16 v[108:111], a[52:55], a[188:191], v[108:111]\n\t"
    "s_waitcnt lgkmcnt(12)\n\t"
    "v_lshlrev_b32 v248, 16, v152\n\t"
    "v_lshlrev_b32 v249, 16, v168\n\t"
    "v_mfma_f32_16x16x32_bf16 v[112:115], a[56:59], a[160:163], v[112:115]\n\t"
    "ds_read_b64_tr_b16 a[40:41], v133 offset:49152\n\t"
    "v_lshlrev_b32 v250, 16, v232\n\t"
    "v_mfma_f32_16x16x32_bf16 v[112:115], a[60:63], a[176:179], v[112:115]\n\t"
    "v_add_f32 v200, v200, v248\n\t"
    "v_add_f32 v249, v249, v250\n\t"
    "v_mfma_f32_16x16x32_bf16 v[116:119], a[56:59], a[164:167], v[116:119]\n\t"
    "ds_read_b64_tr_b16 a[42:43], v133 offset:53248\n\t"
    "v_add_f32 v200, v200, v249\n\t"
    "v_mfma_f32_16x16x32_bf16 v[116:119], a[60:63], a[180:183], v[116:119]\n\t"
    "v_and_b32 v251, 0xffff0000, v152\n\t"
    "v_and_b32 v252, 0xffff0000, v168\n\t"
    "v_mfma_f32_16x16x32_bf16 v[120:123], a[56:59], a[168:171], v[120:123]\n\t"
    "ds_read_b64_tr_b16 a[44:45], v133 offset:57344\n\t"
    "v_and_b32 v253, 0xffff0000, v232\n\t"
    "v_mfma_f32_16x16x32_bf16 v[120:123], a[60:63], a[184:187], v[120:123]\n\t"
    "v_add_f32 v201, v201, v251\n\t"
    "v_add_f32 v252, v252, v253\n\t"
    "v_mfma_f32_16x16x32_bf16 v[124:127], a[56:59], a[172:175], v[124:127]\n\t"
    "ds_read_b64_tr_b16 a[46:47], v133 offset:61440\n\t"
    "v_add_f32 v201, v201, v252\n\t"
    "v_mfma_f32_16x16x32_bf16 v[124:127], a[60:63], a[188:191], v[124:127]\n\t"
    "s_waitcnt vmcnt(12) lgkmcnt(12)\n\t"
    "v_lshlrev_b32 v248, 16, v153\n\t"
    "v_lshlrev_b32 v249, 16, v169\n\t"
    "v_mfma_f32_16x16x32_bf16 v[64:67], a[0:3], a[64:67], v[64:67]\n\t"
    "ds_read_b64_tr_b16 a[48:49], v134 offset:49152\n\t"
    "v_lshlrev_b32 v250, 16, v233\n\t"
    "v_mfma_f32_16x16x32_bf16 v[64:67], a[4:7], a[80:83], v[64:67]\n\t"
    "v_add_f32 v202, v202, v248\n\t"
    "v_add_f32 v249, v249, v250\n\t"
    "v_mfma_f32_16x16x32_bf16 v[68:71], a[0:3], a[68:71], v[68:71]\n\t"
    "ds_read_b64_tr_b16 a[50:51], v134 offset:53248\n\t"
    "v_add_f32 v202, v202, v249\n\t"
    "v_mfma_f32_16x16x32_bf16 v[68:71], a[4:7], a[84:87], v[68:71]\n\t"
    "v_and_b32 v251, 0xffff0000, v153\n\t"
    "v_and_b32 v252, 0xffff0000, v169\n\t"
    "v_mfma_f32_16x16x32_bf16 v[72:75], a[0:3], a[72:75], v[72:75]\n\t"
    "ds_read_b64_tr_b16 a[52:53], v134 offset:57344\n\t"
    "v_and_b32 v253, 0xffff0000, v233\n\t"
    "v_mfma_f32_16x16x32_bf16 v[72:75], a[4:7], a[88:91], v[72:75]\n\t"
    "v_add_f32 v203, v203, v251\n\t"
    "v_add_f32 v252, v252, v253\n\t"
    "v_mfma_f32_16x16x32_bf16 v[76:79], a[0:3], a[76:79], v[76:79]\n\t"
    "ds_read_b64_tr_b16 a[54:55], v134 offset:61440\n\t"
    "v_add_f32 v203, v203, v252\n\t"
    "v_mfma_f32_16x16x32_bf16 v[76:79], a[4:7], a[92:95], v[76:79]\n\t"
    "s_waitcnt lgkmcnt(12)\n\t"
    "v_lshlrev_b32 v248, 16, v154\n\t"
    "v_lshlrev_b32 v249, 16, v170\n\t"
    "v_mfma_f32_16x16x32_bf16 v[80:83], a[8:11], a[64:67], v[80:83]\n\t"
    "ds_read_b64_tr_b16 a[56:57], v135 offset:49152\n\t"
    "v_lshlrev_b32 v250, 16, v234\n\t"
    "v_mfma_f32_16x16x32_bf16 v[80:83], a[12:15], a[80:83], v[80:83]\n\t"
    "v_add_f32 v216, v216, v248\n\t"
    "v_add_f32 v249, v249, v250\n\t"
    "v_mfma_f32_16x16x32_bf16 v[84:87], a[8:11], a[68:71], v[84:87]\n\t"
    "ds_read_b64_tr_b16 a[58:59], v135 offset:53248\n\t"
    "v_add_f32 v216, v216, v249\n\t"
    "v_mfma_f32_16x16x32_bf16 v[84:87], a[12:15], a[84:87], v[84:87]\n\t"
    "v_and_b32 v251, 0xffff0000, v154\n\t"
    "v_and_b32 v252, 0xffff0000, v170\n\t"
    "v_mfma_f32_16x16x32_bf16 v[88:91], a[8:11], a[72:75], v[88:91]\n\t"
    "ds_read_b64_tr_b16 a[60:61], v135 offset:57344\n\t"
    "v_and_b32 v253, 0xffff0000, v234\n\t"
    "v_mfma_f32_16x16x32_bf16 v[88:91], a[12:15], a[88:91], v[88:91]\n\t"
    "v_add_f32 v217, v217, v251\n\t"
    "v_add_f32 v252, v252, v253\n\t"
    "v_mfma_f32_16x16x32_bf16 v[92:95], a[8:11], a[76:79], v[92:95]\n\t"
    "ds_read_b64_tr_b16 a[62:63], v135 offset:61440\n\t"
    "v_add_f32 v217, v217, v252\n\t"
    "v_mfma_f32_16x16x32_bf16 v[92:95], a[12:15], a[92:95], v[92:95]\n\t"
    "s_waitcnt lgkmcnt(12)\n\t"
    "v_lshlrev_b32 v248, 16, v155\n\t"
    "v_lshlrev_b32 v249, 16, v171\n\t"
    "v_mfma_f32_16x16x32_bf16 v[96:99], a[16:19], a[64:67], v[96:99]\n\t"
    "ds_read_b64_tr_b16 a[0:1], v128 offset:0\n\t"
    "v_lshlrev_b32 v250, 16, v235\n\t"
    "v_mfma_f32_16x16x32_bf16 v[96:99], a[20:23], a[80:83], v[96:99]\n\t"
    "v_add_f32 v218, v218, v248\n\t"
    "v_add_f32 v249, v249, v250\n\t"
    "v_mfma_f32_16x16x32_bf16 v[100:103], a[16:19], a[68:71], v[100:103]\n\t"
    "ds_read_b64_tr_b16 a[2:3], v128 offset:4096\n\t"
    "v_add_f32 v218, v218, v249\n\t"
    "v_mfma_f32_16x16x32_bf16 v[100:103], a[20:23], a[84:87], v[100:103]\n\t"
    "v_and_b32 v251, 0xffff0000, v155\n\t"
    "v_and_b32 v252, 0xffff0000, v171\n\t"
    "v_mfma_f32_16x16x32_bf16 v[104:107], a[16:19], a[72:75], v[104:107]\n\t"
    "ds_read_b64_tr_b16 a[4:5], v128 offset:8192\n\t"
    "v_and_b32 v253, 0xffff0000, v235\n\t"
    "v_mfma_f32_16x16x32_bf16 v[104:107], a[20:23], a[88:91], v[104:107]\n\t"
    "v_add_f32 v219, v219, v251\n\t"
    "v_add_f32 v252, v252, v253\n\t"
    "v_mfma_f32_16x16x32_bf16 v[108:111], a[16:19], a[76:79], v[108:111]\n\t"
    "ds_read_b64_tr_b16 a[6:7], v128 offset:12288\n\t"
    "v_add_f32 v219, v219, v252\n\t"
    "v_mfma_f32_16x16x32_bf16 v[108:111], a[20:23], a[92:95], v[108:111]\n\t"
    "s_waitcnt lgkmcnt(12)\n\t"
    "v_lshlrev_b32 v248, 16, v156\n\t"
    "v_lshlrev_b32 v249, 16, v172\n\t"
    "v_mfma_f32_16x16x32_bf16 v[112:115], a[24:27], a[64:67], v[112:115]\n\t"
    "ds_read_b64_tr_b16 a[8:9], v128 offset:16384\n\t"
    "v_lshlrev_b32 v250, 16, v236\n\t"
    "v_mfma_f32_16x16x32_bf16 v[112:115], a[28:31], a[80:83], v[112:115]\n\t"
    "v_add_f32 v204, v204, v248\n\t"
    "v_add_f32 v249, v249, v250\n\t"
    "v_mfma_f32_16x16x32_bf16 v[116:119], a[24:27], a[68:71], v[116:119]\n\t"
    "ds_read_b64_tr_b16 a[10:11], v128 offset:20480\n\t"
    "v_add_f32 v204, v204, v249\n\t"
    "v_mfma_f32_16x16x32_bf16 v[116:119], a[28:31], a[84:87], v[116:119]\n\t"
    "ds_read_b64_tr_b16 a[12:13], v128 offset:24576\n\t"
    "v_and_b32 v251, 0xffff0000, v156\n\t"
    "v_mfma_f32_16x16x32_bf16 v[120:123], a[24:27], a[72:75], v[120:123]\n\t"
    "ds_read_b64_tr_b16 a[14:15], v128 offset:28672\n\t"
    "v_and_b32 v252, 0xffff0000, v172\n\t"
    "v_mfma_f32_16x16x32_bf16 v[120:123], a[28:31], a[88:91], v[120:123]\n\t"
    "ds_read_b64_tr_b16 a[16:17], v128 offset:32768\n\t"
    "v_and_b32 v253, 0xffff0000, v236\n\t"
    "v_mfma_f32_16x16x32_bf16 v[124:127], a[24:27], a[76:79], v[124:127]\n\t"
    "ds_read_b64_tr_b16 a[18:19], v128 offset:36864\n\t"
    "v_add_f32 v205, v205, v251\n\t"
    "v_mfma_f32_16x16x32_bf16 v[124:127], a[28:31], a[92:95], v[124:127]\n\t"
    "s_waitcnt vmcnt(4) lgkmcnt(12)\n\t"
    "ds_read_b64_tr_b16 a[20:21], v128 offset:40960\n\t"
    "v_add_f32 v252, v252, v253\n\t"
    "v_mfma_f32_16x16x32_bf16 v[64:67], a[32:35], a[96:99], v[64:67]\n\t"
    "ds_read_b64_tr_b16 a[22:23], v128 offset:45056\n\t"
    "v_add_f32 v205, v205, v252\n\t"
    "v_mfma_f32_16x16x32_bf16 v[64:67], a[36:39], a[112:115], v[64:67]\n\t"
    "ds_read_b64_tr_b16 a[24:25], v128 offset:49152\n\t"
    "v_lshlrev_b32 v248, 16, v157\n\t"
    "v_mfma_f32_16x16x32_bf16 v[68:71], a[32:35], a[100:103], v[68:71]\n\t"
    "ds_read_b64_tr_b16 a[26:27], v128 offset:53248\n\t"
    "v_lshlrev_b32 v249, 16, v173\n\t"
    "v_mfma_f32_16x16x32_bf16 v[68:71], a[36:39], a[116:119], v[68:71]\n\t"
    "ds_read_b64_tr_b16 a[28:29], v128 offset:57344\n\t"
    "v_lshlrev_b32 v250, 16, v237\n\t"
    "v_mfma_f32_16x16x32_bf16 v[72:75], a[32:35], a[104:107], v[72:75]\n\t"
    "ds_read_b64_tr_b16 a[30:31], v128 offset:61440\n\t"
    "v_add_f32 v206, v206, v248\n\t"
    "v_mfma_f32_16x16x32_bf16 v[72:75], a[36:39], a[120:123], v[72:75]\n\t"
    "v_add_f32 v249, v249, v250\n\t"
    "v_add_f32 v206, v206, v249\n\t"
    "v_mfma_f32_16x16x32_bf16 v[76:79], a[32:35], a[108:111], v[76:79]\n\t"
    "v_and_b32 v251, 0xffff0000, v157\n\t"
    "v_and_b32 v252, 0xffff0000, v173\n\t"
    "v_mfma_f32_16x16x32_bf16 v[76:79], a[36:39], a[124:127], v[76:79]\n\t"
    "s_waitcnt lgkmcnt(12)\n\t"
    "v_and_b32 v253, 0xffff0000, v237\n\t"
    "v_add_f32 v207, v207, v251\n\t"
    "v_mfma_f32_16x16x32_bf16 v[80:83], a[40:43], a[96:99], v[80:83]\n\t"
    "ds_read_b64_tr_b16 a[32:33], v129 offset:0\n\t"
    "v_add_f32 v252, v252, v253\n\t"
    "v_mfma_f32_16x16x32_bf16 v[80:83], a[44:47], a[112:115], v[80:83]\n\t"
    "v_add_f32 v207, v207, v252\n\t"
    "v_lshlrev_b32 v248, 16, v158\n\t"
    "v_mfma_f32_16x16x32_bf16 v[84:87], a[40:43], a[100:103], v[84:87]\n\t"
    "ds_read_b64_tr_b16 a[34:35], v129 offset:4096\n\t"
    "v_lshlrev_b32 v249, 16, v174\n\t"
    "v_mfma_f32_16x16x32_bf16 v[84:87], a[44:47], a[116:119], v[84:87]\n\t"
    "v_lshlrev_b32 v250, 16, v238\n\t"
    "v_add_f32 v220, v220, v248\n\t"
    "v_mfma_f32_16x16x32_bf16 v[88:91], a[40:43], a[104:107], v[88:91]\n\t"
    "ds_read_b64_tr_b16 a[36:37], v129 offset:8192\n\t"
    "v_add_f32 v249, v249, v250\n\t"
    "v_mfma_f32_16x16x32_bf16 v[88:91], a[44:47], a[120:123], v[88:91]\n\t"
    "v_add_f32 v220, v220, v249\n\t"
    "v_and_b32 v251, 0xffff0000, v158\n\t"
    "v_mfma_f32_16x16x32_bf16 v[92:95], a[40:43], a[108:111], v[92:95]\n\t"
    "ds_read_b64_tr_b16 a[38:39], v129 offset:12288\n\t"
    "v_and_b32 v252, 0xffff0000, v174\n\t"
    "v_mfma_f32_16x16x32_bf16 v[92:95], a[44:47], a[124:127], v[92:95]\n\t"
    "s_waitcnt lgkmcnt(12)\n\t"
    "v_and_b32 v253, 0xffff0000, v238\n\t"
    "v_add_f32 v221, v221, v251\n\t"
    "v_mfma_f32_16x16x32_bf16 v[96:99], a[48:51], a[96:99], v[96:99]\n\t"
    "ds_read_b64_tr_b16 a[40:41], v129 offset:16384\n\t"
    "v_add_f32 v252, v252, v253\n\t"
    "v_mfma_f32_16x16x32_bf16 v[96:99], a[52:55], a[112:115], v[96:99]\n\t"
    "v_add_f32 v221, v221, v252\n\t"
    "v_lshlrev_b32 v248, 16, v159\n\t"
    "v_mfma_f32_16x16x32_bf16 v[100:103], a[48:51], a[100:103], v[100:103]\n\t"
    "ds_read_b64_tr_b16 a[42:43], v129 offset:20480\n\t"
    "v_lshlrev_b32 v249, 16, v175\n\t"
    "v_mfma_f32_16x16x32_bf16 v[100:103], a[52:55], a[116:119], v[100:103]\n\t"
    "v_lshlrev_b32 v250, 16, v239\n\t"
    "v_add_f32 v222, v222, v248\n\t"
    "v_mfma_f32_16x16x32_bf16 v[104:107], a[48:51], a[104:107], v[104:107]\n\t"
    "ds_read_b64_tr_b16 a[44:45], v129 offset:24576\n\t"
    "v_add_f32 v249, v249, v250\n\t"
    "v_mfma_f32_16x16x32_bf16 v[104:107], a[52:55], a[120:123], v[104:107]\n\t"
    "v_add_f32 v222, v222, v249\n\t"
    "v_and_b32 v251, 0xffff0000, v159\n\t"
    "v_mfma_f32_16x16x32_bf16 v[108:111], a[48:51], a[108:111], v[108:111]\n\t"
    "ds_read_b64_tr_b16 a[46:47], v129 offset:28672\n\t"
    "v_and_b32 v252, 0xffff0000, v175\n\t"
    "v_mfma_f32_16x16x32_bf16 v[108:111], a[52:55], a[124:127], v[108:111]\n\t"
    "s_waitcnt lgkmcnt(12)\n\t"
    "v_and_b32 v253, 0xffff0000, v239\n\t"
    "v_add_f32 v223, v223, v251\n\t"
    "v_mfma_f32_16x16x32_bf16 v[112:115], a[56:59], a[96:99], v[112:115]\n\t"
    "ds_read_b64_tr_b16 a[48:49], v129 offset:32768\n\t"
    "v_add_f32 v252, v252, v253\n\t"
    "v_mfma_f32_16x16x32_bf16 v[112:115], a[60:63], a[112:115], v[112:115]\n\t"
    "v_add_f32 v223, v223, v252\n\t"
    "v_mfma_f32_16x16x32_bf16 v[116:119], a[56:59], a[100:103], v[116:119]\n\t"
    "ds_read_b64_tr_b16 a[50:51], v129 offset:36864\n\t"
    "v_mul_f32 v246, s69, v192\n\t"
    "v_mfma_f32_16x16x32_bf16 v[116:119], a[60:63], a[116:119], v[116:119]\n\t"
    "ds_read_b64_tr_b16 a[52:53], v129 offset:40960\n\t"
    "v_mul_f32 v247, s69, v193\n\t"
    "v_mfma_f32_16x16x32_bf16 v[120:123], a[56:59], a[104:107], v[120:123]\n\t"
    "ds_read_b64_tr_b16 a[54:55], v129 offset:45056\n\t"
    "v_mul_f32 v248, s69, v194\n\t"
    "v_mfma_f32_16x16x32_bf16 v[120:123], a[60:63], a[120:123], v[120:123]\n\t"
    "v_mul_f32 v249, s69, v195\n\t"
    "v_mfma_f32_16x16x32_bf16 v[124:127], a[56:59], a[108:111], v[124:127]\n\t"
    "v_exp_f32 v246, v246\n\t"
    "v_mfma_f32_16x16x32_bf16 v[124:127], a[60:63], a[124:127], v[124:127]\n\t"
    "s_waitcnt lgkmcnt(12)\n\t"
    "v_exp_f32 v247, v247\n\t"
    "v_mfma_f32_16x16x32_bf16 v[0:3], a[0:3], a[128:131], v[0:3]\n\t"
    "ds_read_b64_tr_b16 a[56:57], v129 offset:49152\n\t"
    "v_mfma_f32_16x16x32_bf16 v[0:3], a[4:7], a[144:147], v[0:3]\n\t"
    "ds_read_b64_tr_b16 a[58:59], v129 offset:53248\n\t"
    "v_mfma_f32_16x16x32_bf16 v[0:3], a[8:11], a[160:163], v[0:3]\n\t"
    "ds_read_b64_tr_b16 a[60:61], v129 offset:57344\n\t"
    "v_mfma_f32_16x16x32_bf16 v[0:3], a[12:15], a[176:179], v[0:3]\n\t"
    "ds_read_b64_tr_b16 a[62:63], v129 offset:61440\n\t"
    "v_mfma_f32_16x16x32_bf16 v[0:3], a[16:19], a[64:67], v[0:3]\n\t"
    "v_exp_f32 v248, v248\n\t"
    "v_mfma_f32_16x16x32_bf16 v[0:3], a[20:23], a[80:83], v[0:3]\n\t"
    "v_exp_f32 v249, v249\n\t"
    "v_mfma_f32_16x16x32_bf16 v[0:3], a[24:27], a[96:99], v[0:3]\n\t"
    "v_add_f32 v246, 1.0, v246\n\t"
    "v_add_f32 v247, 1.0, v247\n\t"
    "v_mfma_f32_16x16x32_bf16 v[0:3], a[28:31], a[112:115], v[0:3]\n\t"
    "v_add_f32 v248, 1.0, v248\n\t"
    "v_add_f32 v249, 1.0, v249\n\t"
    "v_mfma_f32_16x16x32_bf16 v[4:7], a[0:3], a[132:135], v[4:7]\n\t"
    "v_cvt_pk_bf16_f32 v144, v64, v65\n\t"
    "v_mfma_f32_16x16x32_bf16 v[4:7], a[4:7], a[148:151], v[4:7]\n\t"
    "v_min_u32 v144, 0xfffffffe, v144\n\t"
    "v_cvt_pk_bf16_f32 v145, v66, v67\n\t"
    "v_mfma_f32_16x16x32_bf16 v[4:7], a[8:11], a[164:167], v[4:7]\n\t"
    "v_min_u32 v145, 0xfffffffe, v145\n\t"
    "v_cvt_pk_bf16_f32 v146, v80, v81\n\t"
    "v_mfma_f32_16x16x32_bf16 v[4:7], a[12:15], a[180:183], v[4:7]\n\t"
    "v_min_u32 v146, 0xfffffffe, v146\n\t"
    "v_cvt_pk_bf16_f32 v147, v82, v83\n\t"
    "v_mfma_f32_16x16x32_bf16 v[4:7], a[16:19], a[68:71], v[4:7]\n\t"
    "v_min_u32 v147, 0xfffffffe, v147\n\t"
    "v_cvt_pk_bf16_f32 v148, v68, v69\n\t"
    "v_mfma_f32_16x16x32_bf16 v[4:7], a[20:23], a[84:87], v[4:7]\n\t"
    "buffer_store_dwordx4 v[144:147], v240, s[80:83], s66 offen offset:0 sc1\n\t"
    "v_min_u32 v148, 0xfffffffe, v148\n\t"
    "v_cvt_pk_bf16_f32 v149, v70, v71\n\t"
    "v_mfma_f32_16x16x32_bf16 v[4:7], a[24:27], a[100:103], v[4:7]\n\t"
    "v_min_u32 v149, 0xfffffffe, v149\n\t"
    "v_cvt_pk_bf16_f32 v150, v84, v85\n\t"
    "v_mfma_f32_16x16x32_bf16 v[4:7], a[28:31], a[116:119], v[4:7]\n\t"
    "v_min_u32 v150, 0xfffffffe, v150\n\t"
    "v_cvt_pk_bf16_f32 v151, v86, v87\n\t"
    "v_mfma_f32_16x16x32_bf16 v[8:11], a[0:3], a[136:139], v[8:11]\n\t"
    "v_min_u32 v151, 0xfffffffe, v151\n\t"
    "v_cvt_pk_bf16_f32 v152, v72, v73\n\t"
    "v_mfma_f32_16x16x32_bf16 v[8:11], a[4:7], a[152:155], v[8:11]\n\t"
    "buffer_store_dwordx4 v[148:151], v240, s[80:83], s66 offen offset:1024 sc1\n\t"
    "v_min_u32 v152, 0xfffffffe, v152\n\t"
    "v_cvt_pk_bf16_f32 v153, v74, v75\n\t"
    "v_mfma_f32_16x16x32_bf16 v[8:11], a[8:11], a[168:171], v[8:11]\n\t"
    "v_min_u32 v153, 0xfffffffe, v153\n\t"
    "v_cvt_pk_bf16_f32 v154, v88, v89\n\t"
    "v_mfma_f32_16x16x32_bf16 v[8:11], a[12:15], a[184:187], v[8:11]\n\t"
    "v_min_u32 v154, 0xfffffffe, v154\n\t"
    "v_cvt_pk_bf16_f32 v155, v90, v91\n\t"
    "v_mfma_f32_16x16x32_bf16 v[8:11], a[16:19], a[72:75], v[8:11]\n\t"
    "v_min_u32 v155, 0xfffffffe, v155\n\t"
    "v_cvt_pk_bf16_f32 v156, v76, v77\n\t"
    "v_mfma_f32_16x16x32_bf16 v[8:11], a[20:23], a[88:91], v[8:11]\n\t"
    "buffer_store_dwordx4 v[152:155], v240, s[80:83], s66 offen offset:2048 sc1\n\t"
    "v_min_u32 v156, 0xfffffffe, v156\n\t"
    "v_cvt_pk_bf16_f32 v157, v78, v79\n\t"
    "v_mfma_f32_16x16x32_bf16 v[8:11], a[24:27], a[104:107], v[8:11]\n\t"
    "v_min_u32 v157, 0xfffffffe, v157\n\t"
    "v_cvt_pk_bf16_f32 v158, v92, v93\n\t"
    "v_mfma_f32_16x16x32_bf16 v[8:11], a[28:31], a[120:123], v[8:11]\n\t"
    "v_min_u32 v158, 0xfffffffe, v158\n\t"
    "v_cvt_pk_bf16_f32 v159, v94, v95\n\t"
    "v_mfma_f32_16x16x32_bf16 v[12:15], a[0:3], a[140:143], v[12:15]\n\t"
    "v_min_u32 v159, 0xfffffffe, v159\n\t"
    "v_cvt_pk_bf16_f32 v160, v96, v97\n\t"
    "v_mfma_f32_16x16x32_bf16 v[12:15], a[4:7], a[156:159], v[12:15]\n\t"
    "buffer_store_dwordx4 v[156:159], v240, s[80:83], s66 offen offset:3072 sc1\n\t"
    "v_min_u32 v160, 0xfffffffe, v160\n\t"
    "v_cvt_pk_bf16_f32 v161, v98, v99\n\t"
    "v_mfma_f32_16x16x32_bf16 v[12:15], a[8:11], a[172:175], v[12:15]\n\t"
    "v_min_u32 v161, 0xfffffffe, v161\n\t"
    "v_cvt_pk_bf16_f32 v162, v112, v113\n\t"
    "v_mfma_f32_16x16x32_bf16 v[12:15], a[12:15], a[188:191], v[12:15]\n\t"
    "v_min_u32 v162, 0xfffffffe, v162\n\t"
    "v_cvt_pk_bf16_f32 v163, v114, v115\n\t"
    "v_mfma_f32_16x16x32_bf16 v[12:15], a[16:19], a[76:79], v[12:15]\n\t"
    "v_min_u32 v163, 0xfffffffe, v163\n\t"
    "v_cvt_pk_bf16_f32 v164, v100, v101\n\t"
    "v_mfma_f32_16x16x32_bf16 v[12:15], a[20:23], a[92:95], v[12:15]\n\t"
    "buffer_store_dwordx4 v[160:163], v240, s[84:87], s66 offen offset:0 sc1\n\t"
    "v_min_u32 v164, 0xfffffffe, v164\n\t"
    "v_cvt_pk_bf16_f32 v165, v102, v103\n\t"
    "v_mfma_f32_16x16x32_bf16 v[12:15], a[24:27], a[108:111], v[12:15]\n\t"
    "v_min_u32 v165, 0xfffffffe, v165\n\t"
    "v_cvt_pk_bf16_f32 v166, v116, v117\n\t"
    "v_mfma_f32_16x16x32_bf16 v[12:15], a[28:31], a[124:127], v[12:15]\n\t"
    "s_waitcnt lgkmcnt(0)\n\t"
    "v_min_u32 v166, 0xfffffffe, v166\n\t"
    "v_cvt_pk_bf16_f32 v167, v118, v119\n\t"
    "v_mfma_f32_16x16x32_bf16 v[16:19], a[32:35], a[128:131], v[16:19]\n\t"
    "ds_read_b64_tr_b16 a[0:1], v130 offset:0\n\t"
    "v_min_u32 v167, 0xfffffffe, v167\n\t"
    "v_cvt_pk_bf16_f32 v168, v104, v105\n\t"
    "v_mfma_f32_16x16x32_bf16 v[16:19], a[36:39], a[144:147], v[16:19]\n\t"
    "ds_read_b64_tr_b16 a[2:3], v130 offset:4096\n\t"
    "v_min_u32 v168, 0xfffffffe, v168\n\t"
    "v_cvt_pk_bf16_f32 v169, v106, v107\n\t"
    "v_mfma_f32_16x16x32_bf16 v[16:19], a[40:43], a[160:163], v[16:19]\n\t"
    "ds_read_b64_tr_b16 a[4:5], v130 offset:8192\n\t"
    "v_min_u32 v169, 0xfffffffe, v169\n\t"
    "v_cvt_pk_bf16_f32 v170, v120, v121\n\t"
    "v_mfma_f32_16x16x32_bf16 v[16:19], a[44:47], a[176:179], v[16:19]\n\t"
    "ds_read_b64_tr_b16 a[6:7], v130 offset:12288\n\t"
    "v_min_u32 v170, 0xfffffffe, v170\n\t"
    "v_cvt_pk_bf16_f32 v171, v122, v123\n\t"
    "v_mfma_f32_16x16x32_bf16 v[16:19], a[48:51], a[64:67], v[16:19]\n\t"
    "ds_read_b64_tr_b16 a[8:9], v130 offset:16384\n\t"
    "v_min_u32 v171, 0xfffffffe, v171\n\t"
    "v_cvt_pk_bf16_f32 v172, v108, v109\n\t"
    "v_mfma_f32_16x16x32_bf16 v[16:19], a[52:55], a[80:83], v[16:19]\n\t"
    "ds_read_b64_tr_b16 a[10:11], v130 offset:20480\n\t"
    "v_min_u32 v172, 0xfffffffe, v172\n\t"
    "v_cvt_pk_bf16_f32 v173, v110, v111\n\t"
    "v_mfma_f32_16x16x32_bf16 v[16:19], a[56:59], a[96:99], v[16:19]\n\t"
    "ds_read_b64_tr_b16 a[12:13], v130 offset:24576\n\t"
    "v_min_u32 v173, 0xfffffffe, v173\n\t"
    "v_cvt_pk_bf16_f32 v174, v124, v125\n\t"
    "v_mfma_f32_16x16x32_bf16 v[16:19], a[60:63], a[112:115], v[16:19]\n\t"
    "ds_read_b64_tr_b16 a[14:15], v130 offset:28672\n\t"
    "v_min_u32 v174, 0xfffffffe, v174\n\t"
    "v_cvt_pk_bf16_f32 v175, v126, v127\n\t"
    "v_mfma_f32_16x16x32_bf16 v[20:23], a[32:35], a[132:135], v[20:23]\n\t"
    "ds_read_b64_tr_b16 a[16:17], v130 offset:32768\n\t"
    "v_min_u32 v175, 0xfffffffe, v175\n\t"
    "v_mfma_f32_16x16x32_bf16 v[20:23], a[36:39], a[148:151], v[20:23]\n\t"
    "ds_read_b64_tr_b16 a[18:19], v130 offset:36864\n\t"
    "v_mfma_f32_16x16x32_bf16 v[20:23], a[40:43], a[164:167], v[20:23]\n\t"
    "ds_read_b64_tr_b16 a[20:21], v130 offset:40960\n\t"
    "v_mfma_f32_16x16x32_bf16 v[20:23], a[44:47], a[180:183], v[20:23]\n\t"
    "ds_read_b64_tr_b16 a[22:23], v130 offset:45056\n\t"
    "v_mfma_f32_16x16x32_bf16 v[20:23], a[48:51], a[68:71], v[20:23]\n\t"
    "ds_read_b64_tr_b16 a[24:25], v130 offset:49152\n\t"
    "v_mfma_f32_16x16x32_bf16 v[20:23], a[52:55], a[84:87], v[20:23]\n\t"
    "ds_read_b64_tr_b16 a[26:27], v130 offset:53248\n\t"
    "v_mfma_f32_16x16x32_bf16 v[20:23], a[56:59], a[100:103], v[20:23]\n\t"
    "ds_read_b64_tr_b16 a[28:29], v130 offset:57344\n\t"
    "v_mfma_f32_16x16x32_bf16 v[20:23], a[60:63], a[116:119], v[20:23]\n\t"
    "ds_read_b64_tr_b16 a[30:31], v130 offset:61440\n\t"
    "v_mfma_f32_16x16x32_bf16 v[24:27], a[32:35], a[136:139], v[24:27]\n\t"
    "buffer_store_dwordx4 v[164:167], v240, s[84:87], s66 offen offset:1024 sc1\n\t"
    "v_mfma_f32_16x16x32_bf16 v[24:27], a[36:39], a[152:155], v[24:27]\n\t"
    "buffer_store_dwordx4 v[168:171], v240, s[84:87], s66 offen offset:2048 sc1\n\t"
    "v_mfma_f32_16x16x32_bf16 v[24:27], a[40:43], a[168:171], v[24:27]\n\t"
    "buffer_store_dwordx4 v[172:175], v240, s[84:87], s66 offen offset:3072 sc1\n\t"
    "v_mfma_f32_16x16x32_bf16 v[24:27], a[44:47], a[184:187], v[24:27]\n\t"
    "v_rcp_f32 v246, v246\n\t"
    "v_mfma_f32_16x16x32_bf16 v[24:27], a[48:51], a[72:75], v[24:27]\n\t"
    "v_rcp_f32 v247, v247\n\t"
    "v_mfma_f32_16x16x32_bf16 v[24:27], a[52:55], a[88:91], v[24:27]\n\t"
    "v_rcp_f32 v248, v248\n\t"
    "v_mfma_f32_16x16x32_bf16 v[24:27], a[56:59], a[104:107], v[24:27]\n\t"
    "v_rcp_f32 v249, v249\n\t"
    "v_mfma_f32_16x16x32_bf16 v[24:27], a[60:63], a[120:123], v[24:27]\n\t"
    "v_mul_f32 v192, v192, v246\n\t"
    "v_mul_f32 v193, v193, v247\n\t"
    "v_mfma_f32_16x16x32_bf16 v[28:31], a[32:35], a[140:143], v[28:31]\n\t"
    "v_mul_f32 v194, v194, v248\n\t"
    "v_mul_f32 v195, v195, v249\n\t"
    "v_mfma_f32_16x16x32_bf16 v[28:31], a[36:39], a[156:159], v[28:31]\n\t"
    "v_mul_f32 v246, s69, v196\n\t"
    "v_mul_f32 v247, s69, v197\n\t"
    "v_mfma_f32_16x16x32_bf16 v[28:31], a[40:43], a[172:175], v[28:31]\n\t"
    "v_mul_f32 v248, s69, v198\n\t"
    "v_mul_f32 v249, s69, v199\n\t"
    "v_mfma_f32_16x16x32_bf16 v[28:31], a[44:47], a[188:191], v[28:31]\n\t"
    "v_exp_f32 v246, v246\n\t"
    "v_mfma_f32_16x16x32_bf16 v[28:31], a[48:51], a[76:79], v[28:31]\n\t"
    "v_exp_f32 v247, v247\n\t"
    "v_mfma_f32_16x16x32_bf16 v[28:31], a[52:55], a[92:95], v[28:31]\n\t"
    "v_exp_f32 v248, v248\n\t"
    "v_mfma_f32_16x16x32_bf16 v[28:31], a[56:59], a[108:111], v[28:31]\n\t"
    "v_exp_f32 v249, v249\n\t"
    "v_mfma_f32_16x16x32_bf16 v[28:31], a[60:63], a[124:127], v[28:31]\n\t"
    "s_waitcnt lgkmcnt(0)\n\t"
    "v_add_f32 v246, 1.0, v246\n\t"
    "v_add_f32 v247, 1.0, v247\n\t"
    "v_mfma_f32_16x16x32_bf16 v[32:35], a[0:3], a[128:131], v[32:35]\n\t"
    "ds_read_b64_tr_b16 a[32:33], v131 offset:0\n\t"
    "v_add_f32 v248, 1.0, v248\n\t"
    "v_mfma_f32_16x16x32_bf16 v[32:35], a[4:7], a[144:147], v[32:35]\n\t"
    "ds_read_b64_tr_b16 a[34:35], v131 offset:4096\n\t"
    "v_add_f32 v249, 1.0, v249\n\t"
    "v_mfma_f32_16x16x32_bf16 v[32:35], a[8:11], a[160:163], v[32:35]\n\t"
    "ds_read_b64_tr_b16 a[36:37], v131 offset:8192\n\t"
    "v_mfma_f32_16x16x32_bf16 v[32:35], a[12:15], a[176:179], v[32:35]\n\t"
    "ds_read_b64_tr_b16 a[38:39], v131 offset:12288\n\t"
    "v_mfma_f32_16x16x32_bf16 v[32:35], a[16:19], a[64:67], v[32:35]\n\t"
    "ds_read_b64_tr_b16 a[40:41], v131 offset:16384\n\t"
    "v_mfma_f32_16x16x32_bf16 v[32:35], a[20:23], a[80:83], v[32:35]\n\t"
    "ds_read_b64_tr_b16 a[42:43], v131 offset:20480\n\t"
    "v_mfma_f32_16x16x32_bf16 v[32:35], a[24:27], a[96:99], v[32:35]\n\t"
    "ds_read_b64_tr_b16 a[44:45], v131 offset:24576\n\t"
    "v_mfma_f32_16x16x32_bf16 v[32:35], a[28:31], a[112:115], v[32:35]\n\t"
    "ds_read_b64_tr_b16 a[46:47], v131 offset:28672\n\t"
    "v_mfma_f32_16x16x32_bf16 v[36:39], a[0:3], a[132:135], v[36:39]\n\t"
    "ds_read_b64_tr_b16 a[48:49], v131 offset:32768\n\t"
    "v_cvt_pk_bf16_f32 v144, v0, v1\n\t"
    "v_mfma_f32_16x16x32_bf16 v[36:39], a[4:7], a[148:151], v[36:39]\n\t"
    "ds_read_b64_tr_b16 a[50:51], v131 offset:36864\n\t"
    "v_min_u32 v144, 0xfffffffe, v144\n\t"
    "v_cvt_pk_bf16_f32 v145, v2, v3\n\t"
    "v_mfma_f32_16x16x32_bf16 v[36:39], a[8:11], a[164:167], v[36:39]\n\t"
    "ds_read_b64_tr_b16 a[52:53], v131 offset:40960\n\t"
    "v_min_u32 v145, 0xfffffffe, v145\n\t"
    "v_cvt_pk_bf16_f32 v146, v16, v17\n\t"
    "v_mfma_f32_16x16x32_bf16 v[36:39], a[12:15], a[180:183], v[36:39]\n\t"
    "ds_read_b64_tr_b16 a[54:55], v131 offset:45056\n\t"
    "v_min_u32 v146, 0xfffffffe, v146\n\t"
    "v_cvt_pk_bf16_f32 v147, v18, v19\n\t"
    "v_mfma_f32_16x16x32_bf16 v[36:39], a[16:19], a[68:71], v[36:39]\n\t"
    "ds_read_b64_tr_b16 a[56:57], v131 offset:49152\n\t"
    "v_min_u32 v147, 0xfffffffe, v147\n\t"
    "v_cvt_pk_bf16_f32 v148, v4, v5\n\t"
    "v_mfma_f32_16x16x32_bf16 v[36:39], a[20:23], a[84:87], v[36:39]\n\t"
    "ds_read_b64_tr_b16 a[58:59], v131 offset:53248\n\t"
    "v_min_u32 v148, 0xfffffffe, v148\n\t"
    "v_cvt_pk_bf16_f32 v149, v6, v7\n\t"
    "v_mfma_f32_16x16x32_bf16 v[36:39], a[24:27], a[100:103], v[36:39]\n\t"
    "ds_read_b64_tr_b16 a[60:61], v131 offset:57344\n\t"
    "v_min_u32 v149, 0xfffffffe, v149\n\t"
    "v_cvt_pk_bf16_f32 v150, v20, v21\n\t"
    "v_mfma_f32_16x16x32_bf16 v[36:39], a[28:31], a[116:119], v[36:39]\n\t"
    "ds_read_b64_tr_b16 a[62:63], v131 offset:61440\n\t"
    "v_min_u32 v150, 0xfffffffe, v150\n\t"
    "v_cvt_pk_bf16_f32 v151, v22, v23\n\t"
    "v_mfma_f32_16x16x32_bf16 v[40:43], a[0:3], a[136:139], v[40:43]\n\t"
    "buffer_store_dwordx4 v[144:147], v240, s[48:51], s66 offen offset:0 sc1\n\t"
    "v_min_u32 v151, 0xfffffffe, v151\n\t"
    "v_cvt_pk_bf16_f32 v152, v8, v9\n\t"
    "v_mfma_f32_16x16x32_bf16 v[40:43], a[4:7], a[152:155], v[40:43]\n\t"
    "buffer_store_dwordx4 v[148:151], v240, s[48:51], s66 offen offset:1024 sc1\n\t"
    "v_min_u32 v152, 0xfffffffe, v152\n\t"
    "v_cvt_pk_bf16_f32 v153, v10, v11\n\t"
    "v_mfma_f32_16x16x32_bf16 v[40:43], a[8:11], a[168:171], v[40:43]\n\t"
    "v_min_u32 v153, 0xfffffffe, v153\n\t"
    "v_cvt_pk_bf16_f32 v154, v24, v25\n\t"
    "v_mfma_f32_16x16x32_bf16 v[40:43], a[12:15], a[184:187], v[40:43]\n\t"
    "v_min_u32 v154, 0xfffffffe, v154\n\t"
    "v_cvt_pk_bf16_f32 v155, v26, v27\n\t"
    "v_mfma_f32_16x16x32_bf16 v[40:43], a[16:19], a[72:75], v[40:43]\n\t"
    "v_min_u32 v155, 0xfffffffe, v155\n\t"
    "v_cvt_pk_bf16_f32 v156, v12, v13\n\t"
    "v_mfma_f32_16x16x32_bf16 v[40:43], a[20:23], a[88:91], v[40:43]\n\t"
    "buffer_store_dwordx4 v[152:155], v240, s[48:51], s66 offen offset:2048 sc1\n\t"
    "v_min_u32 v156, 0xfffffffe, v156\n\t"
    "v_cvt_pk_bf16_f32 v157, v14, v15\n\t"
    "v_mfma_f32_16x16x32_bf16 v[40:43], a[24:27], a[104:107], v[40:43]\n\t"
    "v_min_u32 v157, 0xfffffffe, v157\n\t"
    "v_cvt_pk_bf16_f32 v158, v28, v29\n\t"
    "v_mfma_f32_16x16x32_bf16 v[40:43], a[28:31], a[120:123], v[40:43]\n\t"
    "v_min_u32 v158, 0xfffffffe, v158\n\t"
    "v_cvt_pk_bf16_f32 v159, v30, v31\n\t"
    "v_mfma_f32_16x16x32_bf16 v[44:47], a[0:3], a[140:143], v[44:47]\n\t"
    "v_min_u32 v159, 0xfffffffe, v159\n\t"
    "v_mfma_f32_16x16x32_bf16 v[44:47], a[4:7], a[156:159], v[44:47]\n\t"
    "buffer_store_dwordx4 v[156:159], v240, s[48:51], s66 offen offset:3072 sc1\n\t"
    "v_mfma_f32_16x16x32_bf16 v[44:47], a[8:11], a[172:175], v[44:47]\n\t"
    "v_rcp_f32 v246, v246\n\t"
    "v_mfma_f32_16x16x32_bf16 v[44:47], a[12:15], a[188:191], v[44:47]\n\t"
    "v_rcp_f32 v247, v247\n\t"
    "v_mfma_f32_16x16x32_bf16 v[44:47], a[16:19], a[76:79], v[44:47]\n\t"
    "v_rcp_f32 v248, v248\n\t"
    "v_mfma_f32_16x16x32_bf16 v[44:47], a[20:23], a[92:95], v[44:47]\n\t"
    "v_rcp_f32 v249, v249\n\t"
    "v_mfma_f32_16x16x32_bf16 v[44:47], a[24:27], a[108:111], v[44:47]\n\t"
    "v_mul_f32 v196, v196, v246\n\t"
    "v_mul_f32 v197, v197, v247\n\t"
    "v_mfma_f32_16x16x32_bf16 v[44:47], a[28:31], a[124:127], v[44:47]\n\t"
    "s_waitcnt lgkmcnt(0)\n\t"
    "v_mul_f32 v198, v198, v248\n\t"
    "v_mul_f32 v199, v199, v249\n\t"
    "v_mfma_f32_16x16x32_bf16 v[48:51], a[32:35], a[128:131], v[48:51]\n\t"
    "v_mul_f32 v246, s69, v200\n\t"
    "v_mul_f32 v247, s69, v201\n\t"
    "v_mfma_f32_16x16x32_bf16 v[48:51], a[36:39], a[144:147], v[48:51]\n\t"
    "buffer_load_dwordx4 v[64:67], v240, s[56:59], s67 offen offset:0 sc1\n\t"
    "v_mul_f32 v248, s69, v202\n\t"
    "v_mul_f32 v249, s69, v203\n\t"
    "v_mfma_f32_16x16x32_bf16 v[48:51], a[40:43], a[160:163], v[48:51]\n\t"
    "v_exp_f32 v246, v246\n\t"
    "v_mfma_f32_16x16x32_bf16 v[48:51], a[44:47], a[176:179], v[48:51]\n\t"
    "v_exp_f32 v247, v247\n\t"
    "v_mfma_f32_16x16x32_bf16 v[48:51], a[48:51], a[64:67], v[48:51]\n\t"
    "v_exp_f32 v248, v248\n\t"
    "v_mfma_f32_16x16x32_bf16 v[48:51], a[52:55], a[80:83], v[48:51]\n\t"
    "buffer_load_dwordx4 v[68:71], v240, s[56:59], s67 offen offset:1024 sc1\n\t"
    "v_exp_f32 v249, v249\n\t"
    "v_mfma_f32_16x16x32_bf16 v[48:51], a[56:59], a[96:99], v[48:51]\n\t"
    "v_add_f32 v246, 1.0, v246\n\t"
    "v_add_f32 v247, 1.0, v247\n\t"
    "v_mfma_f32_16x16x32_bf16 v[48:51], a[60:63], a[112:115], v[48:51]\n\t"
    "v_add_f32 v248, 1.0, v248\n\t"
    "v_add_f32 v249, 1.0, v249\n\t"
    "v_mfma_f32_16x16x32_bf16 v[52:55], a[32:35], a[132:135], v[52:55]\n\t"
    "v_rcp_f32 v246, v246\n\t"
    "v_mfma_f32_16x16x32_bf16 v[52:55], a[36:39], a[148:151], v[52:55]\n\t"
    "buffer_load_dwordx4 v[72:75], v240, s[56:59], s67 offen offset:2048 sc1\n\t"
    "v_rcp_f32 v247, v247\n\t"
    "v_mfma_f32_16x16x32_bf16 v[52:55], a[40:43], a[164:167], v[52:55]\n\t"
    "v_rcp_f32 v248, v248\n\t"
    "v_mfma_f32_16x16x32_bf16 v[52:55], a[44:47], a[180:183], v[52:55]\n\t"
    "v_rcp_f32 v249, v249\n\t"
    "v_mfma_f32_16x16x32_bf16 v[52:55], a[48:51], a[68:71], v[52:55]\n\t"
    "v_mul_f32 v200, v200, v246\n\t"
    "v_mul_f32 v201, v201, v247\n\t"
    "v_mfma_f32_16x16x32_bf16 v[52:55], a[52:55], a[84:87], v[52:55]\n\t"
    "buffer_load_dwordx4 v[76:79], v240, s[56:59], s67 offen offset:3072 sc1\n\t"
    "v_mul_f32 v202, v202, v248\n\t"
    "v_mul_f32 v203, v203, v249\n\t"
    "v_mfma_f32_16x16x32_bf16 v[52:55], a[56:59], a[100:103], v[52:55]\n\t"
    "v_mul_f32 v246, s69, v204\n\t"
    "v_mul_f32 v247, s69, v205\n\t"
    "v_mfma_f32_16x16x32_bf16 v[52:55], a[60:63], a[116:119], v[52:55]\n\t"
    "v_mul_f32 v248, s69, v206\n\t"
    "v_mul_f32 v249, s69, v207\n\t"
    "v_mfma_f32_16x16x32_bf16 v[56:59], a[32:35], a[136:139], v[56:59]\n\t"
    "v_exp_f32 v246, v246\n\t"
    "v_mfma_f32_16x16x32_bf16 v[56:59], a[36:39], a[152:155], v[56:59]\n\t"
    "buffer_load_dwordx4 v[80:83], v240, s[56:59], s68 offen offset:0 sc1\n\t"
    "v_exp_f32 v247, v247\n\t"
    "v_mfma_f32_16x16x32_bf16 v[56:59], a[40:43], a[168:171], v[56:59]\n\t"
    "v_exp_f32 v248, v248\n\t"
    "v_mfma_f32_16x16x32_bf16 v[56:59], a[44:47], a[184:187], v[56:59]\n\t"
    "v_exp_f32 v249, v249\n\t"
    "v_mfma_f32_16x16x32_bf16 v[56:59], a[48:51], a[72:75], v[56:59]\n\t"
    "v_add_f32 v246, 1.0, v246\n\t"
    "v_add_f32 v247, 1.0, v247\n\t"
    "v_mfma_f32_16x16x32_bf16 v[56:59], a[52:55], a[88:91], v[56:59]\n\t"
    "buffer_load_dwordx4 v[84:87], v240, s[56:59], s68 offen offset:1024 sc1\n\t"
    "v_add_f32 v248, 1.0, v248\n\t"
    "v_add_f32 v249, 1.0, v249\n\t"
    "v_mfma_f32_16x16x32_bf16 v[56:59], a[56:59], a[104:107], v[56:59]\n\t"
    "v_rcp_f32 v246, v246\n\t"
    "v_mfma_f32_16x16x32_bf16 v[56:59], a[60:63], a[120:123], v[56:59]\n\t"
    "v_rcp_f32 v247, v247\n\t"
    "v_mfma_f32_16x16x32_bf16 v[60:63], a[32:35], a[140:143], v[60:63]\n\t"
    "v_rcp_f32 v248, v248\n\t"
    "v_mfma_f32_16x16x32_bf16 v[60:63], a[36:39], a[156:159], v[60:63]\n\t"
    "buffer_load_dwordx4 v[88:91], v240, s[56:59], s68 offen offset:2048 sc1\n\t"
    "v_rcp_f32 v249, v249\n\t"
    "v_mfma_f32_16x16x32_bf16 v[60:63], a[40:43], a[172:175], v[60:63]\n\t"
    "v_mul_f32 v204, v204, v246\n\t"
    "v_mul_f32 v205, v205, v247\n\t"
    "v_mfma_f32_16x16x32_bf16 v[60:63], a[44:47], a[188:191], v[60:63]\n\t"
    "v_mul_f32 v206, v206, v248\n\t"
    "v_mul_f32 v207, v207, v249\n\t"
    "v_mfma_f32_16x16x32_bf16 v[60:63], a[48:51], a[76:79], v[60:63]\n\t"
    "v_mul_f32 v246, s69, v208\n\t"
    "v_mul_f32 v247, s69, v209\n\t"
    "v_mfma_f32_16x16x32_bf16 v[60:63], a[52:55], a[92:95], v[60:63]\n\t"
    "buffer_load_dwordx4 v[92:95], v240, s[56:59], s68 offen offset:3072 sc1\n\t"
    "v_mul_f32 v248, s69, v210\n\t"
    "v_mul_f32 v249, s69, v211\n\t"
    "v_mfma_f32_16x16x32_bf16 v[60:63], a[56:59], a[108:111], v[60:63]\n\t"
    "v_exp_f32 v246, v246\n\t"
    "v_mfma_f32_16x16x32_bf16 v[60:63], a[60:63], a[124:127], v[60:63]\n\t"
    "v_exp_f32 v247, v247\n\t"
    "v_exp_f32 v248, v248\n\t"
    "v_exp_f32 v249, v249\n\t"
    "v_add_f32 v246, 1.0, v246\n\t"
    "v_add_f32 v247, 1.0, v247\n\t"
    "v_add_f32 v248, 1.0, v248\n\t"
    "v_add_f32 v249, 1.0, v249\n\t"
    "v_rcp_f32 v246, v246\n\t"
    "v_rcp_f32 v247, v247\n\t"
    "v_rcp_f32 v248, v248\n\t"
    "v_rcp_f32 v249, v249\n\t"
    "v_mul_f32 v208, v208, v246\n\t"
    "v_mul_f32 v209, v209, v247\n\t"
    "v_mul_f32 v210, v210, v248\n\t"
    "v_mul_f32 v211, v211, v249\n\t"
    "v_mul_f32 v246, s69, v212\n\t"
    "v_mul_f32 v247, s69, v213\n\t"
    "v_mul_f32 v248, s69, v214\n\t"
    "v_mul_f32 v249, s69, v215\n\t"
    "v_exp_f32 v246, v246\n\t"
    "v_exp_f32 v247, v247\n\t"
    "v_exp_f32 v248, v248\n\t"
    "v_exp_f32 v249, v249\n\t"
    "v_add_f32 v246, 1.0, v246\n\t"
    "v_add_f32 v247, 1.0, v247\n\t"
    "v_add_f32 v248, 1.0, v248\n\t"
    "v_add_f32 v249, 1.0, v249\n\t"
    "v_rcp_f32 v246, v246\n\t"
    "v_rcp_f32 v247, v247\n\t"
    "v_rcp_f32 v248, v248\n\t"
    "v_rcp_f32 v249, v249\n\t"
    "v_mul_f32 v212, v212, v246\n\t"
    "v_mul_f32 v213, v213, v247\n\t"
    "v_mul_f32 v214, v214, v248\n\t"
    "v_mul_f32 v215, v215, v249\n\t"
    "v_mul_f32 v246, s69, v216\n\t"
    "v_mul_f32 v247, s69, v217\n\t"
    "v_mul_f32 v248, s69, v218\n\t"
    "v_mul_f32 v249, s69, v219\n\t"
    "v_exp_f32 v246, v246\n\t"
    "v_exp_f32 v247, v247\n\t"
    "v_exp_f32 v248, v248\n\t"
    "v_exp_f32 v249, v249\n\t"
    "v_add_f32 v246, 1.0, v246\n\t"
    "v_add_f32 v247, 1.0, v247\n\t"
    "v_add_f32 v248, 1.0, v248\n\t"
    "v_add_f32 v249, 1.0, v249\n\t"
    "v_rcp_f32 v246, v246\n\t"
    "v_rcp_f32 v247, v247\n\t"
    "v_rcp_f32 v248, v248\n\t"
    "v_rcp_f32 v249, v249\n\t"
    "v_mul_f32 v216, v216, v246\n\t"
    "v_mul_f32 v217, v217, v247\n\t"
    "v_mul_f32 v218, v218, v248\n\t"
    "v_mul_f32 v219, v219, v249\n\t"
    "v_mul_f32 v246, s69, v220\n\t"
    "v_mul_f32 v247, s69, v221\n\t"
    "v_mul_f32 v248, s69, v222\n\t"
    "v_mul_f32 v249, s69, v223\n\t"
    "v_exp_f32 v246, v246\n\t"
    "v_exp_f32 v247, v247\n\t"
    "v_exp_f32 v248, v248\n\t"
    "v_exp_f32 v249, v249\n\t"
    "v_add_f32 v246, 1.0, v246\n\t"
    "v_add_f32 v247, 1.0, v247\n\t"
    "v_add_f32 v248, 1.0, v248\n\t"
    "v_add_f32 v249, 1.0, v249\n\t"
    "v_rcp_f32 v246, v246\n\t"
    "v_rcp_f32 v247, v247\n\t"
    "v_rcp_f32 v248, v248\n\t"
    "v_rcp_f32 v249, v249\n\t"
    "v_mul_f32 v220, v220, v246\n\t"
    "v_mul_f32 v221, v221, v247\n\t"
    "v_mul_f32 v222, v222, v248\n\t"
    "v_mul_f32 v223, v223, v249\n\t"
    "buffer_load_dwordx4 v[96:99], v240, s[56:59], s66 offen offset:0 sc1\n\t"
    "buffer_load_dwordx4 v[100:103], v240, s[56:59], s66 offen offset:1024 sc1\n\t"
    "buffer_load_dwordx4 v[104:107], v240, s[56:59], s66 offen offset:2048 sc1\n\t"
    "buffer_load_dwordx4 v[108:111], v240, s[56:59], s66 offen offset:3072 sc1\n\t"
    "s_mov_b32 s54, 0\n\t"
    "s_waitcnt vmcnt(4)\n\t"
    "L_p3_ec%=:\n\t"
    "v_max3_u32 v246, v64, v65, v66\n\t"
    "v_max_u32 v247, v67, v67\n\t"
    "v_max3_u32 v246, v246, v68, v69\n\t"
    "v_max3_u32 v247, v247, v70, v71\n\t"
    "v_max3_u32 v246, v246, v72, v73\n\t"
    "v_max3_u32 v247, v247, v74, v75\n\t"
    "v_max3_u32 v246, v246, v76, v77\n\t"
    "v_max3_u32 v247, v247, v78, v79\n\t"
    "v_max3_u32 v246, v246, v80, v81\n\t"
    "v_max3_u32 v247, v247, v82, v83\n\t"
    "v_max3_u32 v246, v246, v84, v85\n\t"
    "v_max3_u32 v247, v247, v86, v87\n\t"
    "v_max3_u32 v246, v246, v88, v89\n\t"
    "v_max3_u32 v247, v247, v90, v91\n\t"
    "v_max3_u32 v246, v246, v92, v93\n\t"
    "v_max3_u32 v247, v247, v94, v95\n\t"
    "v_max_u32 v246, v246, v247\n\t"
    "v_cmp_eq_u32 vcc, -1, v246\n\t"
    "s_cbranch_vccnz L_p3_ep%=\n\t"
    "L_p3_ek%=:\n\t"
    "v_lshlrev_b32 v248, 16, v64\n\t"
    "v_lshlrev_b32 v249, 16, v80\n\t"
    "v_add_f32 v32, v32, v248\n\t"
    "v_add_f32 v32, v32, v249\n\t"
    "v_and_b32 v250, 0xffff0000, v64\n\t"
    "v_and_b32 v251, 0xffff0000, v80\n\t"
    "v_add_f32 v33, v33, v250\n\t"
    "v_add_f32 v33, v33, v251\n\t"
    "v_lshlrev_b32 v248, 16, v65\n\t"
    "v_lshlrev_b32 v249, 16, v81\n\t"
    "v_add_f32 v34, v34, v248\n\t"
    "v_add_f32 v34, v34, v249\n\t"
    "v_and_b32 v250, 0xffff0000, v65\n\t"
    "v_and_b32 v251, 0xffff0000, v81\n\t"
    "v_add_f32 v35, v35, v250\n\t"
    "v_add_f32 v35, v35, v251\n\t"
    "v_lshlrev_b32 v248, 16, v66\n\t"
    "v_lshlrev_b32 v249, 16, v82\n\t"
    "v_add_f32 v48, v48, v248\n\t"
    "v_add_f32 v48, v48, v249\n\t"
    "v_and_b32 v250, 0xffff0000, v66\n\t"
    "v_and_b32 v251, 0xffff0000, v82\n\t"
    "v_add_f32 v49, v49, v250\n\t"
    "v_add_f32 v49, v49, v251\n\t"
    "v_lshlrev_b32 v248, 16, v67\n\t"
    "v_lshlrev_b32 v249, 16, v83\n\t"
    "v_add_f32 v50, v50, v248\n\t"
    "v_add_f32 v50, v50, v249\n\t"
    "v_and_b32 v250, 0xffff0000, v67\n\t"
    "v_and_b32 v251, 0xffff0000, v83\n\t"
    "v_add_f32 v51, v51, v250\n\t"
    "v_add_f32 v51, v51, v251\n\t"
    "v_lshlrev_b32 v248, 16, v68\n\t"
    "v_lshlrev_b32 v249, 16, v84\n\t"
    "v_add_f32 v36, v36, v248\n\t"
    "v_add_f32 v36, v36, v249\n\t"
    "v_and_b32 v250, 0xffff0000, v68\n\t"
    "v_and_b32 v251, 0xffff0000, v84\n\t"
    "v_add_f32 v37, v37, v250\n\t"
    "v_add_f32 v37, v37, v251\n\t"
    "v_lshlrev_b32 v248, 16, v69\n\t"
    "v_lshlrev_b32 v249, 16, v85\n\t"
    "v_add_f32 v38, v38, v248\n\t"
    "v_add_f32 v38, v38, v249\n\t"
    "v_and_b32 v250, 0xffff0000, v69\n\t"
    "v_and_b32 v251, 0xffff0000, v85\n\t"
    "v_add_f32 v39, v39, v250\n\t"
    "v_add_f32 v39, v39, v251\n\t"
    "v_lshlrev_b32 v248, 16, v70\n\t"
    "v_lshlrev_b32 v249, 16, v86\n\t"
    "v_add_f32 v52, v52, v248\n\t"
    "v_add_f32 v52, v52, v249\n\t"
    "v_and_b32 v250, 0xffff0000, v70\n\t"
    "v_and_b32 v251, 0xffff0000, v86\n\t"
    "v_add_f32 v53, v53, v250\n\t"
    "v_add_f32 v53, v53, v251\n\t"
    "v_lshlrev_b32 v248, 16, v71\n\t"
    "v_lshlrev_b32 v249, 16, v87\n\t"
    "v_add_f32 v54, v54, v248\n\t"
    "v_add_f32 v54, v54, v249\n\t"
    "v_and_b32 v250, 0xffff0000, v71\n\t"
    "v_and_b32 v251, 0xffff0000, v87\n\t"
    "v_add_f32 v55, v55, v250\n\t"
    "v_add_f32 v55, v55, v251\n\t"
    "v_lshlrev_b32 v248, 16, v72\n\t"
    "v_lshlrev_b32 v249, 16, v88\n\t"
    "v_add_f32 v40, v40, v248\n\t"
    "v_add_f32 v40, v40, v249\n\t"
    "v_and_b32 v250, 0xffff0000, v72\n\t"
    "v_and_b32 v251, 0xffff0000, v88\n\t"
    "v_add_f32 v41, v41, v250\n\t"
    "v_add_f32 v41, v41, v251\n\t"
    "v_lshlrev_b32 v248, 16, v73\n\t"
    "v_lshlrev_b32 v249, 16, v89\n\t"
    "v_add_f32 v42, v42, v248\n\t"
    "v_add_f32 v42, v42, v249\n\t"
    "v_and_b32 v250, 0xffff0000, v73\n\t"
    "v_and_b32 v251, 0xffff0000, v89\n\t"
    "v_add_f32 v43, v43, v250\n\t"
    "v_add_f32 v43, v43, v251\n\t"
    "v_lshlrev_b32 v248, 16, v74\n\t"
    "v_lshlrev_b32 v249, 16, v90\n\t"
    "v_add_f32 v56, v56, v248\n\t"
    "v_add_f32 v56, v56, v249\n\t"
    "v_and_b32 v250, 0xffff0000, v74\n\t"
    "v_and_b32 v251, 0xffff0000, v90\n\t"
    "v_add_f32 v57, v57, v250\n\t"
    "v_add_f32 v57, v57, v251\n\t"
    "v_lshlrev_b32 v248, 16, v75\n\t"
    "v_lshlrev_b32 v249, 16, v91\n\t"
    "v_add_f32 v58, v58, v248\n\t"
    "v_add_f32 v58, v58, v249\n\t"
    "v_and_b32 v250, 0xffff0000, v75\n\t"
    "v_and_b32 v251, 0xffff0000, v91\n\t"
    "v_add_f32 v59, v59, v250\n\t"
    "v_add_f32 v59, v59, v251\n\t"
    "v_lshlrev_b32 v248, 16, v76\n\t"
    "v_lshlrev_b32 v249, 16, v92\n\t"
    "v_add_f32 v44, v44, v248\n\t"
    "v_add_f32 v44, v44, v249\n\t"
    "v_and_b32 v250, 0xffff0000, v76\n\t"
    "v_and_b32 v251, 0xffff0000, v92\n\t"
    "v_add_f32 v45, v45, v250\n\t"
    "v_add_f32 v45, v45, v251\n\t"
    "v_lshlrev_b32 v248, 16, v77\n\t"
    "v_lshlrev_b32 v249, 16, v93\n\t"
    "v_add_f32 v46, v46, v248\n\t"
    "v_add_f32 v46, v46, v249\n\t"
    "v_and_b32 v250, 0xffff0000, v77\n\t"
    "v_and_b32 v251, 0xffff0000, v93\n\t"
    "v_add_f32 v47, v47, v250\n\t"
    "v_add_f32 v47, v47, v251\n\t"
    "v_lshlrev_b32 v248, 16, v78\n\t"
    "v_lshlrev_b32 v249, 16, v94\n\t"
    "v_add_f32 v60, v60, v248\n\t"
    "v_add_f32 v60, v60, v249\n\t"
    "v_and_b32 v250, 0xffff0000, v78\n\t"
    "v_and_b32 v251, 0xffff0000, v94\n\t"
    "v_add_f32 v61, v61, v250\n\t"
    "v_add_f32 v61, v61, v251\n\t"
    "v_lshlrev_b32 v248, 16, v79\n\t"
    "v_lshlrev_b32 v249, 16, v95\n\t"
    "v_add_f32 v62, v62, v248\n\t"
    "v_add_f32 v62, v62, v249\n\t"
    "v_and_b32 v250, 0xffff0000, v79\n\t"
    "v_and_b32 v251, 0xffff0000, v95\n\t"
    "v_add_f32 v63, v63, v250\n\t"
    "v_add_f32 v63, v63, v251\n\t"
    "s_waitcnt vmcnt(0)\n\t"
    "L_p3_fc%=:\n\t"
    "v_max3_u32 v246, v96, v97, v98\n\t"
    "v_max_u32 v247, v99, v99\n\t"
    "v_max3_u32 v246, v246, v100, v101\n\t"
    "v_max3_u32 v247, v247, v102, v103\n\t"
    "v_max3_u32 v246, v246, v104, v105\n\t"
    "v_max3_u32 v247, v247, v106, v107\n\t"
    "v_max3_u32 v246, v246, v108, v109\n\t"
    "v_max3_u32 v247, v247, v110, v111\n\t"
    "v_max_u32 v246, v246, v247\n\t"
    "v_cmp_eq_u32 vcc, -1, v246\n\t"
    "s_cbranch_vccnz L_p3_fp%=\n\t"
    "L_p3_fk%=:\n\t"
    "v_lshlrev_b32 v248, 16, v96\n\t"
    "v_add_f32 v32, v32, v248\n\t"
    "v_mul_f32 v32, v32, v192\n\t"
    "v_and_b32 v249, 0xffff0000, v96\n\t"
    "v_add_f32 v33, v33, v249\n\t"
    "v_mul_f32 v33, v33, v193\n\t"
    "v_lshlrev_b32 v250, 16, v97\n\t"
    "v_add_f32 v34, v34, v250\n\t"
    "v_mul_f32 v34, v34, v194\n\t"
    "v_and_b32 v251, 0xffff0000, v97\n\t"
    "v_add_f32 v35, v35, v251\n\t"
    "v_mul_f32 v35, v35, v195\n\t"
    "v_lshlrev_b32 v248, 16, v98\n\t"
    "v_add_f32 v48, v48, v248\n\t"
    "v_mul_f32 v48, v48, v208\n\t"
    "v_and_b32 v249, 0xffff0000, v98\n\t"
    "v_add_f32 v49, v49, v249\n\t"
    "v_mul_f32 v49, v49, v209\n\t"
    "v_lshlrev_b32 v250, 16, v99\n\t"
    "v_add_f32 v50, v50, v250\n\t"
    "v_mul_f32 v50, v50, v210\n\t"
    "v_and_b32 v251, 0xffff0000, v99\n\t"
    "v_add_f32 v51, v51, v251\n\t"
    "v_mul_f32 v51, v51, v211\n\t"
    "v_lshlrev_b32 v64, 16, v176\n\t"
    "v_and_b32 v65, 0xffff0000, v176\n\t"
    "v_lshlrev_b32 v66, 16, v177\n\t"
    "v_and_b32 v67, 0xffff0000, v177\n\t"
    "v_lshlrev_b32 v68, 16, v178\n\t"
    "v_and_b32 v69, 0xffff0000, v178\n\t"
    "v_lshlrev_b32 v70, 16, v179\n\t"
    "v_and_b32 v71, 0xffff0000, v179\n\t"
    "v_permlane16_swap_b32 v32, v48\n\t"
    "v_permlane16_swap_b32 v33, v49\n\t"
    "v_permlane16_swap_b32 v34, v50\n\t"
    "v_permlane16_swap_b32 v35, v51\n\t"
    "v_fma_f32 v64, v32, s70, v64\n\t"
    "v_fma_f32 v65, v33, s70, v65\n\t"
    "v_fma_f32 v66, v34, s70, v66\n\t"
    "v_fma_f32 v67, v35, s70, v67\n\t"
    "v_fma_f32 v68, v48, s70, v68\n\t"
    "v_fma_f32 v69, v49, s70, v69\n\t"
    "v_fma_f32 v70, v50, s70, v70\n\t"
    "v_fma_f32 v71, v51, s70, v71\n\t"
    "v_cvt_pk_bf16_f32 v72, v64, v65\n\t"
    "v_cvt_pk_bf16_f32 v73, v66, v67\n\t"
    "v_cvt_pk_bf16_f32 v74, v68, v69\n\t"
    "v_cvt_pk_bf16_f32 v75, v70, v71\n\t"
    "buffer_store_dwordx4 v[72:75], v241, s[88:91], 0 offen sc1\n\t"
    "v_lshlrev_b32 v248, 16, v100\n\t"
    "v_add_f32 v36, v36, v248\n\t"
    "v_mul_f32 v36, v36, v196\n\t"
    "v_and_b32 v249, 0xffff0000, v100\n\t"
    "v_add_f32 v37, v37, v249\n\t"
    "v_mul_f32 v37, v37, v197\n\t"
    "v_lshlrev_b32 v250, 16, v101\n\t"
    "v_add_f32 v38, v38, v250\n\t"
    "v_mul_f32 v38, v38, v198\n\t"
    "v_and_b32 v251, 0xffff0000, v101\n\t"
    "v_add_f32 v39, v39, v251\n\t"
    "v_mul_f32 v39, v39, v199\n\t"
    "v_lshlrev_b32 v248, 16, v102\n\t"
    "v_add_f32 v52, v52, v248\n\t"
    "v_mul_f32 v52, v52, v212\n\t"
    "v_and_b32 v249, 0xffff0000, v102\n\t"
    "v_add_f32 v53, v53, v249\n\t"
    "v_mul_f32 v53, v53, v213\n\t"
    "v_lshlrev_b32 v250, 16, v103\n\t"
    "v_add_f32 v54, v54, v250\n\t"
    "v_mul_f32 v54, v54, v214\n\t"
    "v_and_b32 v251, 0xffff0000, v103\n\t"
    "v_add_f32 v55, v55, v251\n\t"
    "v_mul_f32 v55, v55, v215\n\t"
    "v_lshlrev_b32 v64, 16, v180\n\t"
    "v_and_b32 v65, 0xffff0000, v180\n\t"
    "v_lshlrev_b32 v66, 16, v181\n\t"
    "v_and_b32 v67, 0xffff0000, v181\n\t"
    "v_lshlrev_b32 v68, 16, v182\n\t"
    "v_and_b32 v69, 0xffff0000, v182\n\t"
    "v_lshlrev_b32 v70, 16, v183\n\t"
    "v_and_b32 v71, 0xffff0000, v183\n\t"
    "v_permlane16_swap_b32 v36, v52\n\t"
    "v_permlane16_swap_b32 v37, v53\n\t"
    "v_permlane16_swap_b32 v38, v54\n\t"
    "v_permlane16_swap_b32 v39, v55\n\t"
    "v_fma_f32 v64, v36, s70, v64\n\t"
    "v_fma_f32 v65, v37, s70, v65\n\t"
    "v_fma_f32 v66, v38, s70, v66\n\t"
    "v_fma_f32 v67, v39, s70, v67\n\t"
    "v_fma_f32 v68, v52, s70, v68\n\t"
    "v_fma_f32 v69, v53, s70, v69\n\t"
    "v_fma_f32 v70, v54, s70, v70\n\t"
    "v_fma_f32 v71, v55, s70, v71\n\t"
    "v_cvt_pk_bf16_f32 v76, v64, v65\n\t"
    "v_cvt_pk_bf16_f32 v77, v66, v67\n\t"
    "v_cvt_pk_bf16_f32 v78, v68, v69\n\t"
    "v_cvt_pk_bf16_f32 v79, v70, v71\n\t"
    "buffer_store_dwordx4 v[76:79], v242, s[88:91], 0 offen sc1\n\t"
    "v_lshlrev_b32 v248, 16, v104\n\t"
    "v_add_f32 v40, v40, v248\n\t"
    "v_mul_f32 v40, v40, v200\n\t"
    "v_and_b32 v249, 0xffff0000, v104\n\t"
    "v_add_f32 v41, v41, v249\n\t"
    "v_mul_f32 v41, v41, v201\n\t"
    "v_lshlrev_b32 v250, 16, v105\n\t"
    "v_add_f32 v42, v42, v250\n\t"
    "v_mul_f32 v42, v42, v202\n\t"
    "v_and_b32 v251, 0xffff0000, v105\n\t"
    "v_add_f32 v43, v43, v251\n\t"
    "v_mul_f32 v43, v43, v203\n\t"
    "v_lshlrev_b32 v248, 16, v106\n\t"
    "v_add_f32 v56, v56, v248\n\t"
    "v_mul_f32 v56, v56, v216\n\t"
    "v_and_b32 v249, 0xffff0000, v106\n\t"
    "v_add_f32 v57, v57, v249\n\t"
    "v_mul_f32 v57, v57, v217\n\t"
    "v_lshlrev_b32 v250, 16, v107\n\t"
    "v_add_f32 v58, v58, v250\n\t"
    "v_mul_f32 v58, v58, v218\n\t"
    "v_and_b32 v251, 0xffff0000, v107\n\t"
    "v_add_f32 v59, v59, v251\n\t"
    "v_mul_f32 v59, v59, v219\n\t"
    "v_lshlrev_b32 v64, 16, v184\n\t"
    "v_and_b32 v65, 0xffff0000, v184\n\t"
    "v_lshlrev_b32 v66, 16, v185\n\t"
    "v_and_b32 v67, 0xffff0000, v185\n\t"
    "v_lshlrev_b32 v68, 16, v186\n\t"
    "v_and_b32 v69, 0xffff0000, v186\n\t"
    "v_lshlrev_b32 v70, 16, v187\n\t"
    "v_and_b32 v71, 0xffff0000, v187\n\t"
    "v_permlane16_swap_b32 v40, v56\n\t"
    "v_permlane16_swap_b32 v41, v57\n\t"
    "v_permlane16_swap_b32 v42, v58\n\t"
    "v_permlane16_swap_b32 v43, v59\n\t"
    "v_fma_f32 v64, v40, s70, v64\n\t"
    "v_fma_f32 v65, v41, s70, v65\n\t"
    "v_fma_f32 v66, v42, s70, v66\n\t"
    "v_fma_f32 v67, v43, s70, v67\n\t"
    "v_fma_f32 v68, v56, s70, v68\n\t"
    "v_fma_f32 v69, v57, s70, v69\n\t"
    "v_fma_f32 v70, v58, s70, v70\n\t"
    "v_fma_f32 v71, v59, s70, v71\n\t"
    "v_cvt_pk_bf16_f32 v80, v64, v65\n\t"
    "v_cvt_pk_bf16_f32 v81, v66, v67\n\t"
    "v_cvt_pk_bf16_f32 v82, v68, v69\n\t"
    "v_cvt_pk_bf16_f32 v83, v70, v71\n\t"
    "buffer_store_dwordx4 v[80:83], v243, s[88:91], 0 offen sc1\n\t"
    "v_lshlrev_b32 v248, 16, v108\n\t"
    "v_add_f32 v44, v44, v248\n\t"
    "v_mul_f32 v44, v44, v204\n\t"
    "v_and_b32 v249, 0xffff0000, v108\n\t"
    "v_add_f32 v45, v45, v249\n\t"
    "v_mul_f32 v45, v45, v205\n\t"
    "v_lshlrev_b32 v250, 16, v109\n\t"
    "v_add_f32 v46, v46, v250\n\t"
    "v_mul_f32 v46, v46, v206\n\t"
    "v_and_b32 v251, 0xffff0000, v109\n\t"
    "v_add_f32 v47, v47, v251\n\t"
    "v_mul_f32 v47, v47, v207\n\t"
    "v_lshlrev_b32 v248, 16, v110\n\t"
    "v_add_f32 v60, v60, v248\n\t"
    "v_mul_f32 v60, v60, v220\n\t"
    "v_and_b32 v249, 0xffff0000, v110\n\t"
    "v_add_f32 v61, v61, v249\n\t"
    "v_mul_f32 v61, v61, v221\n\t"
    "v_lshlrev_b32 v250, 16, v111\n\t"
    "v_add_f32 v62, v62, v250\n\t"
    "v_mul_f32 v62, v62, v222\n\t"
    "v_and_b32 v251, 0xffff0000, v111\n\t"
    "v_add_f32 v63, v63, v251\n\t"
    "v_mul_f32 v63, v63, v223\n\t"
    "v_lshlrev_b32 v64, 16, v188\n\t"
    "v_and_b32 v65, 0xffff0000, v188\n\t"
    "v_lshlrev_b32 v66, 16, v189\n\t"
    "v_and_b32 v67, 0xffff0000, v189\n\t"
    "v_lshlrev_b32 v68, 16, v190\n\t"
    "v_and_b32 v69, 0xffff0000, v190\n\t"
    "v_lshlrev_b32 v70, 16, v191\n\t"
    "v_and_b32 v71, 0xffff0000, v191\n\t"
    "v_permlane16_swap_b32 v44, v60\n\t"
    "v_permlane16_swap_b32 v45, v61\n\t"
    "v_permlane16_swap_b32 v46, v62\n\t"
    "v_permlane16_swap_b32 v47, v63\n\t"
    "v_fma_f32 v64, v44, s70, v64\n\t"
    "v_fma_f32 v65, v45, s70, v65\n\t"
    "v_fma_f32 v66, v46, s70, v66\n\t"
    "v_fma_f32 v67, v47, s70, v67\n\t"
    "v_fma_f32 v68, v60, s70, v68\n\t"
    "v_fma_f32 v69, v61, s70, v69\n\t"
    "v_fma_f32 v70, v62, s70, v70\n\t"
    "v_fma_f32 v71, v63, s70, v71\n\t"
    "v_cvt_pk_bf16_f32 v84, v64, v65\n\t"
    "v_cvt_pk_bf16_f32 v85, v66, v67\n\t"
    "v_cvt_pk_bf16_f32 v86, v68, v69\n\t"
    "v_cvt_pk_bf16_f32 v87, v70, v71\n\t"
    "buffer_store_dwordx4 v[84:87], v244, s[88:91], 0 offen sc1\n\t"
    "s_branch L_p3_end%=\n\t"
    "L_p3_dp%=:\n\t"
    "s_add_u32 s54, s54, 1\n\t"
    "s_cmp_gt_u32 s54, 0x400000\n\t"
    "s_cbranch_scc1 L_p3_dk%=\n\t"
    "s_sleep 2\n\t"
    "buffer_load_dwordx4 v[144:147], v240, s[56:59], 0 offen offset:0 sc1\n\t"
    "buffer_load_dwordx4 v[148:151], v240, s[56:59], 0 offen offset:1024 sc1\n\t"
    "buffer_load_dwordx4 v[152:155], v240, s[56:59], 0 offen offset:2048 sc1\n\t"
    "buffer_load_dwordx4 v[156:159], v240, s[56:59], 0 offen offset:3072 sc1\n\t"
    "buffer_load_dwordx4 v[160:163], v240, s[56:59], s64 offen offset:0 sc1\n\t"
    "buffer_load_dwordx4 v[164:167], v240, s[56:59], s64 offen offset:1024 sc1\n\t"
    "buffer_load_dwordx4 v[168:171], v240, s[56:59], s64 offen offset:2048 sc1\n\t"
    "buffer_load_dwordx4 v[172:175], v240, s[56:59], s64 offen offset:3072 sc1\n\t"
    "buffer_load_dwordx4 v[224:227], v240, s[56:59], s65 offen offset:0 sc1\n\t"
    "buffer_load_dwordx4 v[228:231], v240, s[56:59], s65 offen offset:1024 sc1\n\t"
    "buffer_load_dwordx4 v[232:235], v240, s[56:59], s65 offen offset:2048 sc1\n\t"
    "buffer_load_dwordx4 v[236:239], v240, s[56:59], s65 offen offset:3072 sc1\n\t"
    "s_waitcnt vmcnt(0)\n\t"
    "s_branch L_p3_dc%=\n\t"
    "L_p3_ep%=:\n\t"
    "s_add_u32 s54, s54, 1\n\t"
    "s_cmp_gt_u32 s54, 0x400000\n\t"
    "s_cbranch_scc1 L_p3_ek%=\n\t"
    "s_sleep 2\n\t"
    "buffer_load_dwordx4 v[64:67], v240, s[56:59], s67 offen offset:0 sc1\n\t"
    "buffer_load_dwordx4 v[68:71], v240, s[56:59], s67 offen offset:1024 sc1\n\t"
    "buffer_load_dwordx4 v[72:75], v240, s[56:59], s67 offen offset:2048 sc1\n\t"
    "buffer_load_dwordx4 v[76:79], v240, s[56:59], s67 offen offset:3072 sc1\n\t"
    "buffer_load_dwordx4 v[80:83], v240, s[56:59], s68 offen offset:0 sc1\n\t"
    "buffer_load_dwordx4 v[84:87], v240, s[56:59], s68 offen offset:1024 sc1\n\t"
    "buffer_load_dwordx4 v[88:91], v240, s[56:59], s68 offen offset:2048 sc1\n\t"
    "buffer_load_dwordx4 v[92:95], v240, s[56:59], s68 offen offset:3072 sc1\n\t"
    "s_waitcnt vmcnt(0)\n\t"
    "s_branch L_p3_ec%=\n\t"
    "L_p3_fp%=:\n\t"
    "s_add_u32 s54, s54, 1\n\t"
    "s_cmp_gt_u32 s54, 0x400000\n\t"
    "s_cbranch_scc1 L_p3_fk%=\n\t"
    "s_sleep 2\n\t"
    "buffer_load_dwordx4 v[96:99], v240, s[56:59], s66 offen offset:0 sc1\n\t"
    "buffer_load_dwordx4 v[100:103], v240, s[56:59], s66 offen offset:1024 sc1\n\t"
    "buffer_load_dwordx4 v[104:107], v240, s[56:59], s66 offen offset:2048 sc1\n\t"
    "buffer_load_dwordx4 v[108:111], v240, s[56:59], s66 offen offset:3072 sc1\n\t"
    "s_waitcnt vmcnt(0)\n\t"
    "s_branch L_p3_fc%=\n\t"
    "L_p3_end%=:\n\t"
    "s_nop 1\n\t"
      : "+{v246}"(vfo), "+{v248}"(vx2[0]), "+{v249}"(vx2[1]), "+{v250}"(vx2[2]), "+{v251}"(vx2[3]),
        "+{s44}"(soffX), "+{s45}"(soffW), "+{s[36:39]}"(srdA), "+{s[40:43]}"(srdW),
        "+{v128}"(vw_rd[0]), "+{v129}"(vw_rd[1]), "+{v130}"(vw_rd[2]), "+{v131}"(vw_rd[3]), "+{v132}"(vw_rd[4]), "+{v133}"(vw_rd[5]), "+{v134}"(vw_rd[6]), "+{v135}"(vw_rd[7]),
        "+{v140}"(vx[0]), "+{v141}"(vx[1]), "+{v142}"(vx[2]), "+{v143}"(vx[3])
      : "{v136}"(vw_dma[0]), "{v137}"(vw_dma[1]), "{v138}"(vw_dma[2]), "{v139}"(vw_dma[3]),
        "{v240}"(woff), "{v241}"(vxo[0]), "{v242}"(vxo[1]), "{v243}"(vxo[2]), "{v244}"(vxo[3]),
        "{s[48:51]}"(make_srd(inb + (q ^ 1) * 98304, 49152u + 16384u)), "{s[80:83]}"(make_srd(inb + (q ^ 2) * 98304 + 16384, 49152u + 16384u)), "{s[84:87]}"(make_srd(inb + (q ^ 3) * 98304 + 32768, 49152u + 16384u)),
        "{s[56:59]}"(make_srd(inb + q * 98304, 98304u)), "{s[60:63]}"(make_srd(x + tbase, (unsigned)((SEQ * DM - tbase) * 2))),
        "{s[72:75]}"(srdA2), "{s[76:79]}"(srdW2), "{s[92:95]}"(make_srd(ws + WS_CNT + 73728 + (rb3 * 32 + q * 8) * FST, 8u * FST)), "{s[88:91]}"(make_srd(out + tbase, (unsigned)((SEQ * DM - tbase) * 2))),
        "{s[96:99]}"(make_srd(ws + WS_CNT + 73728 + (rb * 32 + c) * FST, w == 0 ? 4u : 0u)),
        "{s71}"(tag), "{s69}"(-1.4426950408889634f / (XNS * XNS)), "{s70}"(1.0f / (XNS * XNS)), "{s46}"((unsigned)(ldsb + w * 4096))
      : "v0", "v1", "v2", "v3", "v4", "v5", "v6", "v7", "v8", "v9", "v10", "v11", "v12", "v13", "v14", "v15", "v16", "v17", "v18", "v19", "v20", "v21", "v22", "v23", "v24", "v25", "v26", "v27", "v28", "v29", "v30", "v31", "v32", "v33", "v34", "v35", "v36", "v37", "v38", "v39", "v40", "v41", "v42", "v43", "v44", "v45", "v46", "v47", "v48", "v49", "v50", "v51", "v52", "v53", "v54", "v55", "v56", "v57", "v58", "v59", "v60", "v61", "v62", "v63", "v64", "v65", "v66", "v67", "v68", "v69", "v70", "v71", "v72", "v73", "v74", "v75", "v76", "v77", "v78", "v79", "v80", "v81", "v82", "v83", "v84", "v85", "v86", "v87", "v88", "v89", "v90", "v91", "v92", "v93", "v94", "v95", "v96", "v97", "v98", "v99", "v100", "v101", "v102", "v103", "v104", "v105", "v106", "v107", "v108", "v109", "v110", "v111", "v112", "v113", "v114", "v115", "v116", "v117", "v118", "v119", "v120", "v121", "v122", "v123", "v124", "v125", "v126", "v127", "v144", "v145", "v146", "v147", "v148", "v149", "v150", "v151", "v152", "v153", "v154", "v155", "v156", "v157", "v158", "v159", "v160", "v161", "v162", "v163", "v164", "v165", "v166", "v167", "v168", "v169", "v170", "v171", "v172", "v173", "v174", "v175", "v176", "v177", "v178", "v179", "v180", "v181", "v182", "v183", "v184", "v185", "v186", "v187", "v188", "v189", "v190", "v191", "v192", "v193", "v194", "v195", "v196", "v197", "v198", "v199", "v200", "v201", "v202", "v203", "v204", "v205", "v206", "v207", "v208", "v209", "v210", "v211", "v212", "v213", "v214", "v215", "v216", "v217", "v218", "v219", "v220", "v221", "v222", "v223", "v224", "v225", "v226", "v227", "v228", "v229", "v230", "v231", "v232", "v233", "v234", "v235", "v236", "v237", "v238", "v239", "v245", "v247", "v252", "v253", "a0", "a1", "a2", "a3", "a4", "a5", "a6", "a7", "a8", "a9", "a10", "a11", "a12", "a13", "a14", "a15", "a16", "a17", "a18", "a19", "a20", "a21", "a22", "a23", "a24", "a25", "a26", "a27", "a28", "a29", "a30", "a31", "a32", "a33", "a34", "a35", "a36", "a37", "a38", "a39", "a40", "a41", "a42", "a43", "a44", "a45", "a46", "a47", "a48", "a49", "a50", "a51", "a52", "a53", "a54", "a55", "a56", "a57", "a58", "a59", "a60", "a61", "a62", "a63", "a64", "a65", "a66", "a67", "a68", "a69", "a70", "a71", "a72", "a73", "a74", "a75", "a76", "a77", "a78", "a79", "a80", "a81", "a82", "a83", "a84", "a85", "a86", "a87", "a88", "a89", "a90", "a91", "a92", "a93", "a94", "a95", "a96", "a97", "a98", "a99", "a100", "a101", "a102", "a103", "a104", "a105", "a106", "a107", "a108", "a109", "a110", "a111", "a112", "a113", "a114", "a115", "a116", "a117", "a118", "a119", "a120", "a121", "a122", "a123", "a124", "a125", "a126", "a127", "a128", "a129", "a130", "a131", "a132", "a133", "a134", "a135", "a136", "a137", "a138", "a139", "a140", "a141", "a142", "a143", "a144", "a145", "a146", "a147", "a148", "a149", "a150", "a151", "a152", "a153", "a154", "a155", "a156", "a157", "a158", "a159", "a160", "a161", "a162", "a163", "a164", "a165", "a166", "a167", "a168", "a169", "a170", "a171", "a172", "a173", "a174", "a175", "a176", "a177", "a178", "a179", "a180", "a181", "a182", "a183", "a184", "a185", "a186", "a187", "a188", "a189", "a190", "a191", "s47", "s52", "s53", "s54", "s64", "s65", "s66", "s67", "s68", "vcc", "scc", "memory");
  }

}

extern "C" void kernel_launch(void* const* d_in, const int* in_sizes, int n_in, void* d_out, int out_size, void* d_ws, size_t ws_size, hipStream_t stream) {
  const u16* x = (const u16*)d_in[0]; const u16* Kc = (const u16*)d_in[1]; const u16* Vc = (const u16*)d_in[2];
  const u16* wq = (const u16*)d_in[3]; const u16* wo = (const u16*)d_in[4];
  const u16* wg = (const u16*)d_in[6]; const u16* wu = (const u16*)d_in[7]; const u16* wd = (const u16*)d_in[8];
  char* ws = (char*)d_ws;
  k_main<<<256, 256, 0, stream>>>(wq, wg, wu, Kc, Vc, ws, wo, wd, x, (u16*)d_out);
}
```
